```python
import torch

def _softmax(x, axis=-1):
    return torch.softmax(x, dim=axis)
def _gelu(x):
    return torch.nn.functional.gelu(x, approximate="tanh")
def _silu(x):
    return torch.nn.functional.silu(x)
def _sigmoid(x):
    return torch.sigmoid(x)
def _layernorm(x, axis=-1, eps=1e-5):
    m = x.mean(dim=axis, keepdim=True)
    v = x.var(dim=axis, keepdim=True, unbiased=False)
    return (x - m) / torch.sqrt(v + eps)
def _scatter_add(tgt, idx, src, axis=0):
    return tgt.clone().index_add_(axis, idx, src)
def _segment_sum(src, seg, n):
    out = torch.zeros((n,) + src.shape[1:], dtype=src.dtype)
    return out.index_add_(0, seg, src)
def _pad(x, pw):
    flat = tuple(v for pair in reversed(pw) for v in pair)
    return torch.nn.functional.pad(x, flat)

kernel_name = 'standalone__sliding_block_cached__sampling'

def setup_inputs():

    _w = (lambda a: (lambda t: t.to(torch.bfloat16) if t.is_floating_point() else t)(torch.as_tensor(a)))
    B, Sq, D = 8, 8, 448
    Hq, Hkv, Sk, Dh = 16, 8, 8192, 256
    assert Hq % Hkv == 0, (Hq, Hkv)
    return {
        "x": _w((lambda *s: torch.randn(*s, dtype=torch.bfloat16) * 0.8)(B, Sq, D)),
        "k_cache": _w((lambda *s: torch.randn(*s, dtype=torch.bfloat16) * 0.8)(B, Hkv, Sk, Dh)),
        "v_cache": _w((lambda *s: torch.randn(*s, dtype=torch.bfloat16) * 0.05)(B, Hkv, Sk, Dh)),
        "wq": _w((lambda *s: torch.randn(*s, dtype=torch.bfloat16) * 0.8)(D, Hq * Dh) * (D ** -0.5)),
        "wo": _w((lambda *s: torch.randn(*s, dtype=torch.bfloat16) * 0.05)(Hq * Dh, D) * ((Hq * Dh) ** -0.5)),
    }

def reference(x, k_cache, v_cache, wq, wo):
    B, Sq, D = x.shape
    Hq, Dh = 16, 256
    Hkv, Sk = k_cache.shape[1], k_cache.shape[2]
    W = min(4096, max(256, Sk // 4))
    rep = Hq // Hkv
    q = (lambda x, *d: x.permute(*d))((x @ wq).reshape(B, Sq, Hq, Dh), 0, 2, 1, 3)
    k = (lambda x, n, axis: torch.repeat_interleave(x, n, dim=axis))(k_cache[:, :, Sk - W:, :], rep, axis=1)
    v = (lambda x, n, axis: torch.repeat_interleave(x, n, dim=axis))(v_cache[:, :, Sk - W:, :], rep, axis=1)
    scores = torch.einsum("bhqd,bhkd->bhqk", q, k) * (Dh ** -0.5)
    p = _softmax(scores, axis=-1)
    o = torch.einsum("bhqk,bhkd->bhqd", p, v)
    return (lambda x, *d: x.permute(*d))(o, 0, 2, 1, 3).reshape(B, Sq, Hq * Dh) @ wo
```

```cpp
#include <hip/hip_runtime.h>
#include <hip/hip_bf16.h>

namespace {

typedef unsigned v4u __attribute__((ext_vector_type(4)));
typedef unsigned v2u __attribute__((ext_vector_type(2)));
typedef float f32x4 __attribute__((ext_vector_type(4)));
typedef float f32x2 __attribute__((ext_vector_type(2)));
typedef __bf16 bf16x8 __attribute__((ext_vector_type(8)));
typedef __bf16 bf16x2 __attribute__((ext_vector_type(2)));
typedef short s16x4 __attribute__((ext_vector_type(4)));
#define LAS __attribute__((address_space(3)))

constexpr int NB = 8, SQ = 8, DM = 448, HKV = 8, SK = 8192, WIN = 2048;
constexpr int NQ = 4096;
constexpr float C2 = 0.0625f * 1.4426950408889634f;
constexpr unsigned SPIN_CAP = 1u << 18;

constexpr int L_RING = 0;
constexpr int L_P = 131072;
constexpr int L_QF = L_P + 16384;
constexpr int L_MX = L_QF + 8192;
constexpr int L_LS = L_MX + 512;
constexpr int L_OWN = L_LS + 512;
constexpr int L_OB = L_OWN + 4096;
constexpr int L_RED = L_P;
constexpr int LDS_BYTES = 163840;
static_assert(L_OB + 16 * 144 <= LDS_BYTES, "LDS map");

constexpr size_t W_EP = 0;
constexpr size_t W_QX = 4096;
constexpr size_t W_ST = W_QX + (size_t)8 * 4 * 8 * 4096;
constexpr size_t W_H1 = W_ST + (size_t)64 * 4 * 256;
constexpr size_t W_H2 = W_H1 + (size_t)64 * 16 * 8192;
constexpr size_t W_END = W_H2 + (size_t)8 * 32 * 8 * 224 * 16;

__device__ __forceinline__ f32x4 mfma16(v4u a, v4u b, f32x4 c) {
  return __builtin_amdgcn_mfma_f32_16x16x32_bf16(__builtin_bit_cast(bf16x8, a), __builtin_bit_cast(bf16x8, b), c, 0, 0, 0);
}
__device__ __forceinline__ unsigned pack_bf16(float lo, float hi) {
  bf16x2 r; r[0] = (__bf16)lo; r[1] = (__bf16)hi;
  return __builtin_bit_cast(unsigned, r);
}
__device__ __forceinline__ float xg_max(float x) {
  auto s = __builtin_amdgcn_permlane16_swap(__float_as_uint(x), __float_as_uint(x), false, false);
  x = fmaxf(__uint_as_float(s[0]), __uint_as_float(s[1]));
  auto t = __builtin_amdgcn_permlane32_swap(__float_as_uint(x), __float_as_uint(x), false, false);
  return fmaxf(__uint_as_float(t[0]), __uint_as_float(t[1]));
}
__device__ __forceinline__ float xg_sum(float x) {
  auto s = __builtin_amdgcn_permlane16_swap(__float_as_uint(x), __float_as_uint(x), false, false);
  x = __uint_as_float(s[0]) + __uint_as_float(s[1]);
  auto t = __builtin_amdgcn_permlane32_swap(__float_as_uint(x), __float_as_uint(x), false, false);
  return __uint_as_float(t[0]) + __uint_as_float(t[1]);
}
__device__ __forceinline__ unsigned perm_lo(unsigned a, unsigned b) { return __builtin_amdgcn_perm(b, a, 0x05040100u); }
__device__ __forceinline__ unsigned perm_hi(unsigned a, unsigned b) { return __builtin_amdgcn_perm(b, a, 0x07060302u); }

#define VMWAIT(N) asm volatile("s_waitcnt vmcnt(" #N ")" ::: "memory")
#define LGKM0() asm volatile("s_waitcnt lgkmcnt(0)" ::: "memory")
#define BARRIER() asm volatile("s_waitcnt lgkmcnt(0)\n\ts_barrier" ::: "memory")

__device__ __forceinline__ void dma_unit(const char* gbase, unsigned lds_dst, const unsigned (&voff)[8]) {
  unsigned keep;
  asm volatile(
      "s_mov_b32 %[keep], m0\n\ts_mov_b32 m0, %[l0]\n\ts_nop 2\n\t"
      "global_load_lds_dwordx4 %[v0], %[sb] nt\n\t"
      "global_load_lds_dwordx4 %[v1], %[sb] offset:1024 nt\n\t"
      "global_load_lds_dwordx4 %[v2], %[sb] offset:2048 nt\n\t"
      "global_load_lds_dwordx4 %[v3], %[sb] offset:3072 nt\n\t"
      "s_add_u32 m0, m0, 4096\n\ts_nop 0\n\t"
      "global_load_lds_dwordx4 %[v4], %[sb] nt\n\t"
      "global_load_lds_dwordx4 %[v5], %[sb] offset:1024 nt\n\t"
      "global_load_lds_dwordx4 %[v6], %[sb] offset:2048 nt\n\t"
      "global_load_lds_dwordx4 %[v7], %[sb] offset:3072 nt\n\t"
      "s_mov_b32 m0, %[keep]"
      : [keep] "=&s"(keep)
      : [v0] "v"(voff[0]), [v1] "v"(voff[1]), [v2] "v"(voff[2]), [v3] "v"(voff[3]), [v4] "v"(voff[4]), [v5] "v"(voff[5]),
        [v6] "v"(voff[6]), [v7] "v"(voff[7]), [sb] "s"(gbase), [l0] "s"(lds_dst)
      : "memory", "scc");
}
__device__ __forceinline__ void ld128(v4u& d, unsigned voff, const char* sb) {
  asm volatile("global_load_dwordx4 %0, %1, %2" : "=v"(d) : "v"(voff), "s"(sb) : "memory");
}
__device__ __forceinline__ void ld64(v2u& d, unsigned voff, const char* sb) {
  asm volatile("global_load_dwordx2 %0, %1, %2" : "=v"(d) : "v"(voff), "s"(sb) : "memory");
}
__device__ __forceinline__ void ld128_dev(v4u& d, unsigned voff, const char* sb) {
  asm volatile("global_load_dwordx4 %0, %1, %2 sc1" : "=v"(d) : "v"(voff), "s"(sb) : "memory");
}
__device__ __forceinline__ void ld32_dev(unsigned& d, unsigned voff, const char* sb) {
  asm volatile("global_load_dword %0, %1, %2 sc1" : "=v"(d) : "v"(voff), "s"(sb) : "memory");
}
__device__ __forceinline__ void st128_dev(unsigned voff, v4u v, char* sb) {
  asm volatile("global_store_dwordx4 %0, %1, %2 sc1\n\ts_nop 1" : : "v"(voff), "v"(v), "s"(sb) : "memory");
}
__device__ __forceinline__ void st32_dev(unsigned voff, unsigned v, char* sb) {
  asm volatile("global_store_dword %0, %1, %2 sc1" : : "v"(voff), "v"(v), "s"(sb) : "memory");
}
__device__ __forceinline__ bool tags_ok(v4u u, unsigned T) { return u.y == T && u.w == T; }
template <class P> __device__ __forceinline__ P* uni(P* p) {
  const unsigned long long a = (unsigned long long)p;
  const unsigned lo = __builtin_amdgcn_readfirstlane((unsigned)a), hi = __builtin_amdgcn_readfirstlane((unsigned)(a >> 32));
  return (P*)(((unsigned long long)hi << 32) | lo);
}
__device__ __forceinline__ bool wave_all(bool ok) { return __builtin_amdgcn_ballot_w64(!ok) == 0ull; }

__global__ void __launch_bounds__(512, 2)
attn_fused_kernel(const char* __restrict__ x, const char* __restrict__ kc, const char* __restrict__ vc,
                  const char* __restrict__ wq, const char* __restrict__ wo, unsigned* __restrict__ out, char* __restrict__ ws) {
  extern __shared__ __attribute__((aligned(16))) unsigned char lds_raw[];
  LAS unsigned char* L = (LAS unsigned char*)lds_raw;
  const unsigned lbase = (unsigned)(size_t)L;
  const int tid = threadIdx.x, lane = tid & 63, w = __builtin_amdgcn_readfirstlane(tid >> 6);
  const int g = lane >> 4, c = lane & 15;
  const int bid = blockIdx.x, kvh = bid & 7, s = (bid >> 3) & 3, b = bid >> 5;
  const int rot = 8 * s + b;
  const int rb = b >> 1, cb = (b & 1) * 4 + s;

  const size_t headrow = (size_t)(b * HKV + kvh) * SK + (SK - WIN) + s * 512;
  const char* kwin = kc + headrow * 512;
  const char* vwin = vc + headrow * 512;
  unsigned ko[8], vo8[8];
  {
    const unsigned hi = lane >> 5, l31 = lane & 31;
    const unsigned laneK = (hi << 9) | ((l31 ^ hi) << 4), laneV = (hi << 9) | ((l31 ^ (hi << 1)) << 4);
#pragma unroll
    for (int q = 0; q < 8; ++q) {
      ko[q] = (laneK ^ (unsigned)(q << 5)) + ((q >> 2) << 12);
      vo8[q] = (laneV ^ (unsigned)(((2 * q) & 7) << 5)) + ((q >> 2) << 12);
    }
  }
  auto tile_off = [&](int unit) -> size_t { return (size_t)((unit + rot) & 31) * 8192; };
  unsigned ep;
  if (w < 7) {
    v2u wr[2][8];
    v4u xf[2];
    ld32_dev(ep, 0u, ws + W_EP + rb * 256);
    const char* wqb = wq + (size_t)(64 * w) * (NQ * 2) + (kvh * 512 + cb * 64) * 2;
    const unsigned vo = (unsigned)(8 * g) * (NQ * 2) + c * 8;
#pragma unroll
    for (int kk = 0; kk < 2; ++kk)
#pragma unroll
      for (int i = 0; i < 8; ++i) ld64(wr[kk][i], vo + (unsigned)(32 * kk + i) * (NQ * 2), wqb);
    const char* xb = x + (size_t)(16 * rb) * (DM * 2) + 64 * w * 2;
#pragma unroll
    for (int kk = 0; kk < 2; ++kk) ld128(xf[kk], c * (DM * 2) + (32 * kk + 8 * g) * 2, xb);
    dma_unit(kwin + tile_off(w), lbase + L_RING + w * 8192, ko);
    asm volatile("s_waitcnt vmcnt(8)"
                 : "+v"(wr[0][0]), "+v"(wr[0][1]), "+v"(wr[0][2]), "+v"(wr[0][3]), "+v"(wr[0][4]), "+v"(wr[0][5]), "+v"(wr[0][6]), "+v"(wr[0][7]),
                   "+v"(wr[1][0]), "+v"(wr[1][1]), "+v"(wr[1][2]), "+v"(wr[1][3]), "+v"(wr[1][4]), "+v"(wr[1][5]), "+v"(wr[1][6]), "+v"(wr[1][7]),
                   "+v"(xf[0]), "+v"(xf[1]), "+v"(ep)
                 :: "memory");
    f32x4 qa[4];
#pragma unroll
    for (int jj = 0; jj < 4; ++jj) qa[jj] = f32x4{0.f, 0.f, 0.f, 0.f};
#pragma unroll
    for (int kk = 0; kk < 2; ++kk)
#pragma unroll
      for (int jj = 0; jj < 4; ++jj) {
        v4u a;
#pragma unroll
        for (int r = 0; r < 4; ++r) {
          const unsigned lo = wr[kk][2 * r][jj >> 1], hi = wr[kk][2 * r + 1][jj >> 1];
          a[r] = (jj & 1) ? perm_hi(lo, hi) : perm_lo(lo, hi);
        }
        qa[jj] = mfma16(a, xf[kk], qa[jj]);
      }
#pragma unroll
    for (int r = 0; r < 4; ++r)
      *(LAS f32x4*)(L + L_RING + (8 + r) * 8192 + w * 1024 + lane * 16) = f32x4{qa[0][r], qa[1][r], qa[2][r], qa[3][r]};
  } else {
    ld32_dev(ep, 0u, ws + W_EP + rb * 256);
    dma_unit(kwin + tile_off(w), lbase + L_RING + w * 8192, ko);
    asm volatile("s_waitcnt vmcnt(8)" : "+v"(ep) :: "memory");
  }
  const unsigned T = __builtin_amdgcn_readfirstlane(ep) + 1u;
  BARRIER();
  char* qx = uni(ws + W_QX + (size_t)(kvh * 4 + rb) * (8 * 4096));
  if (w < 4) {
    f32x4 q4 = *(LAS f32x4*)(L + L_RING + (8 + w) * 8192 + lane * 16);
#pragma unroll
    for (int ww = 1; ww < 7; ++ww) q4 += *(LAS f32x4*)(L + L_RING + (8 + w) * 8192 + ww * 1024 + lane * 16);
    st128_dev(cb * 4096 + w * 1024 + lane * 16, v4u{pack_bf16(q4[0], q4[1]), T, pack_bf16(q4[2], q4[3]), T}, qx);
  }
  v4u qu[2];
  const unsigned qgo = w * 4096 + (lane >> 5) * 1024 + (16 * ((lane >> 3) & 3) + 8 * (b & 1) + (lane & 7)) * 16;
  ld128_dev(qu[0], qgo, qx);
  ld128_dev(qu[1], qgo + 2048, qx);
  LGKM0();
  dma_unit(kwin + tile_off(8 + w), lbase + L_RING + (8 + w) * 8192, ko);
  asm volatile("s_waitcnt vmcnt(8)" : "+v"(qu[0]), "+v"(qu[1]) :: "memory");
  for (unsigned spin = 0; !wave_all(tags_ok(qu[0], T) && tags_ok(qu[1], T)) && spin < SPIN_CAP; ++spin) {
    ld128_dev(qu[0], qgo, qx);
    ld128_dev(qu[1], qgo + 2048, qx);
    asm volatile("s_waitcnt vmcnt(0)" : "+v"(qu[0]), "+v"(qu[1]) :: "memory");
  }
  {
    const int gq = (lane >> 3) & 3, q = lane & 7, h = w >> 2;
#pragma unroll
    for (int u = 0; u < 2; ++u) {
      const int reg = 2 * u + (lane >> 5);
      const int st = (w & 3) * 2 + (gq >> 1), gk = 2 * (gq & 1) + (reg >> 1);
      *(LAS v2u*)(L + L_QF + st * 1024 + (16 * gk + 8 * h + q) * 16 + (reg & 1) * 8) = v2u{qu[u].x, qu[u].z};
    }
  }
  BARRIER();
  v4u qf[8];
#pragma unroll
  for (int st = 0; st < 8; ++st) qf[st] = *(LAS v4u*)(L + L_QF + st * 1024 + lane * 16);

  f32x4 sc[4];
  auto qk_tile = [&](int slot) -> f32x4 {
    v4u kf[8];
#pragma unroll
    for (int st = 0; st < 8; ++st) kf[st] = *(LAS v4u*)(L + L_RING + slot * 8192 + c * 512 + (((4 * st + g) ^ c) << 4));
    f32x4 a = {0.f, 0.f, 0.f, 0.f};
#pragma unroll
    for (int st = 0; st < 8; ++st) a = mfma16(kf[st], qf[st], a);
    return a;
  };
  sc[0] = qk_tile(w);
  LGKM0();
  dma_unit(kwin + tile_off(16 + w), lbase + L_RING + w * 8192, ko);
  VMWAIT(8);
  sc[1] = qk_tile(8 + w);
  LGKM0();
  dma_unit(kwin + tile_off(24 + w), lbase + L_RING + (8 + w) * 8192, ko);
  VMWAIT(8);
  sc[2] = qk_tile(w);
  LGKM0();
  dma_unit(vwin + tile_off(w), lbase + L_RING + w * 8192, vo8);
  VMWAIT(8);
  sc[3] = qk_tile(8 + w);
  float mx = fmaxf(fmaxf(fmaxf(sc[0][0], sc[0][1]), fmaxf(sc[0][2], sc[0][3])), fmaxf(fmaxf(sc[1][0], sc[1][1]), fmaxf(sc[1][2], sc[1][3])));
  mx = fmaxf(mx, fmaxf(fmaxf(fmaxf(sc[2][0], sc[2][1]), fmaxf(sc[2][2], sc[2][3])), fmaxf(fmaxf(sc[3][0], sc[3][1]), fmaxf(sc[3][2], sc[3][3]))));
  mx = xg_max(mx);
  if (g == 0) *(LAS float*)(L + L_MX + (w * 16 + c) * 4) = mx;
  LGKM0();
  dma_unit(vwin + tile_off(8 + w), lbase + L_RING + (8 + w) * 8192, vo8);
  const int wc = w < 7 ? w : 6;
  v2u wv[4][8];
  const char* wob = wo + (size_t)(2 * kvh * 256 + 64 * s) * (DM * 2) + wc * 128;
  const unsigned wvo = (unsigned)(8 * g) * (DM * 2) + c * 8;
#pragma unroll
  for (int ks = 0; ks < 2; ++ks)
#pragma unroll
    for (int i = 0; i < 8; ++i) ld64(wv[ks][i], wvo + (unsigned)(32 * ks + i) * (DM * 2), wob);
  BARRIER();
  float mrow = *(LAS float*)(L + L_MX + c * 4);
#pragma unroll
  for (int ww = 1; ww < 8; ++ww) mrow = fmaxf(mrow, *(LAS float*)(L + L_MX + (ww * 16 + c) * 4));
  mrow *= C2;
  float lsum = 0.f;
#pragma unroll
  for (int t = 0; t < 4; ++t) {
    float p[4];
#pragma unroll
    for (int r = 0; r < 4; ++r) { p[r] = __builtin_amdgcn_exp2f(fmaf(sc[t][r], C2, -mrow)); lsum += p[r]; }
    *(LAS v2u*)(L + L_P + (4 * t + (w >> 1)) * 1024 + lane * 16 + (w & 1) * 8) = v2u{pack_bf16(p[0], p[1]), pack_bf16(p[2], p[3])};
  }
  lsum = xg_sum(lsum);
  if (g == 0) *(LAS float*)(L + L_LS + (w * 16 + c) * 4) = lsum;

  f32x4 oa[2] = {f32x4{0.f, 0.f, 0.f, 0.f}, f32x4{0.f, 0.f, 0.f, 0.f}};
  unsigned tra[2];
  {
    const int row = 4 * g + ((lane & 15) >> 2), p = lane & 3;
#pragma unroll
    for (int ct = 0; ct < 2; ++ct) tra[ct] = row * 512 + (((4 * w + 2 * ct + (p >> 1)) ^ ((row & 7) << 1)) << 4) + (p & 1) * 8;
  }
  auto pv_batch = [&](int n) {
#pragma unroll
    for (int u = 0; u < 4; ++u) {
      const v4u pf = *(LAS v4u*)(L + L_P + (4 * n + u) * 1024 + lane * 16);
      const int ub = L_RING + ((n & 1) * 8 + 2 * u) * 8192;
#pragma unroll
      for (int ct = 0; ct < 2; ++ct) {
        const s16x4 a0 = __builtin_amdgcn_ds_read_tr16_b64_v4i16((LAS s16x4*)(L + ub + tra[ct]));
        const s16x4 a1 = __builtin_amdgcn_ds_read_tr16_b64_v4i16((LAS s16x4*)(L + ub + 8192 + tra[ct]));
        const v2u x0 = __builtin_bit_cast(v2u, a0), x1 = __builtin_bit_cast(v2u, a1);
        oa[ct] = mfma16(v4u{x0.x, x0.y, x1.x, x1.y}, pf, oa[ct]);
      }
    }
  };
  const int bh = b * HKV + kvh;
  char* stb = uni(ws + W_ST + (size_t)bh * 1024);
  VMWAIT(24);
  BARRIER();
  if (w == 0) {
    float lt = *(LAS float*)(L + L_LS + c * 4);
#pragma unroll
    for (int ww = 1; ww < 8; ++ww) lt += *(LAS float*)(L + L_LS + (ww * 16 + c) * 4);
    if (g == 0) st128_dev((s * 16 + c) * 16, v4u{__float_as_uint(mrow), T, __float_as_uint(lt), T}, stb);
  }
  pv_batch(0);
  BARRIER();
  dma_unit(vwin + tile_off(16 + w), lbase + L_RING + w * 8192, vo8);
#pragma unroll
  for (int ks = 2; ks < 4; ++ks)
#pragma unroll
    for (int i = 0; i < 8; ++i) ld64(wv[ks][i], wvo + (unsigned)(256 + 32 * (ks - 2) + i) * (DM * 2), wob);
  VMWAIT(24);
  BARRIER();
  pv_batch(1);
  BARRIER();
  dma_unit(vwin + tile_off(24 + w), lbase + L_RING + (8 + w) * 8192, vo8);
  v4u stu;
  ld128_dev(stu, lane * 16, stb);
  asm volatile("s_waitcnt vmcnt(9)"
               : "+v"(wv[0][0]), "+v"(wv[0][1]), "+v"(wv[0][2]), "+v"(wv[0][3]), "+v"(wv[0][4]), "+v"(wv[0][5]), "+v"(wv[0][6]), "+v"(wv[0][7]),
                 "+v"(wv[1][0]), "+v"(wv[1][1]), "+v"(wv[1][2]), "+v"(wv[1][3]), "+v"(wv[1][4]), "+v"(wv[1][5]), "+v"(wv[1][6]), "+v"(wv[1][7])
               :: "memory");
  asm volatile("" : "+v"(wv[2][0]), "+v"(wv[2][1]), "+v"(wv[2][2]), "+v"(wv[2][3]), "+v"(wv[2][4]), "+v"(wv[2][5]), "+v"(wv[2][6]), "+v"(wv[2][7]),
                    "+v"(wv[3][0]), "+v"(wv[3][1]), "+v"(wv[3][2]), "+v"(wv[3][3]), "+v"(wv[3][4]), "+v"(wv[3][5]), "+v"(wv[3][6]), "+v"(wv[3][7])
               :: "memory");
  BARRIER();
  pv_batch(2);
  v4u wf[4][4];
#pragma unroll
  for (int ks = 0; ks < 4; ++ks)
#pragma unroll
    for (int jj = 0; jj < 4; ++jj)
#pragma unroll
      for (int r = 0; r < 4; ++r) {
        const unsigned lo = wv[ks][2 * r][jj >> 1], hi = wv[ks][2 * r + 1][jj >> 1];
        wf[ks][jj][r] = (jj & 1) ? perm_hi(lo, hi) : perm_lo(lo, hi);
      }
  asm volatile("s_waitcnt vmcnt(0)" : "+v"(stu) :: "memory");
  for (unsigned spin = 0; !wave_all(tags_ok(stu, T)) && spin < SPIN_CAP; ++spin) {
    ld128_dev(stu, lane * 16, stb);
    asm volatile("s_waitcnt vmcnt(0)" : "+v"(stu) :: "memory");
  }
  float fs;
  {
    const float ms = __uint_as_float(stu.x), ls = __uint_as_float(stu.z);
    const float M = xg_max(ms);
    const float Lt = xg_sum(__builtin_amdgcn_exp2f(ms - M) * ls);
    fs = __builtin_amdgcn_exp2f(mrow - M) / Lt;
  }
  BARRIER();
  pv_batch(3);

  char* h1 = uni(ws + W_H1 + (size_t)bh * (16 * 8192));
  {
    float ov[8];
#pragma unroll
    for (int ct = 0; ct < 2; ++ct)
#pragma unroll
      for (int r = 0; r < 4; ++r) ov[4 * ct + r] = oa[ct][r] * fs;
    const int dst = w >> 1, half = w & 1;
    if (dst != s) {
#pragma unroll
      for (int i = 0; i < 4; ++i)
        st128_dev(((dst * 4 + s) * 8 + half * 4 + i) * 1024 + lane * 16,
                  v4u{__float_as_uint(ov[2 * i]), T, __float_as_uint(ov[2 * i + 1]), T}, h1);
    } else {
#pragma unroll
      for (int i = 0; i < 4; ++i) *(LAS f32x2*)(L + L_OWN + (half * 4 + i) * 512 + lane * 8) = f32x2{ov[2 * i], ov[2 * i + 1]};
    }
  }
  {
    const int half = w & 1, i = w >> 1;
    v4u hu[3];
    unsigned ho[3];
#pragma unroll
    for (int k = 0; k < 3; ++k) {
      const int src = (s + 1 + k) & 3;
      ho[k] = ((s * 4 + src) * 8 + half * 4 + i) * 1024 + lane * 16;
      ld128_dev(hu[k], ho[k], h1);
    }
    BARRIER();
    f32x2 acc = *(LAS f32x2*)(L + L_OWN + (half * 4 + i) * 512 + lane * 8);
    asm volatile("s_waitcnt vmcnt(0)" : "+v"(hu[0]), "+v"(hu[1]), "+v"(hu[2]) :: "memory");
    for (unsigned spin = 0; !wave_all(tags_ok(hu[0], T) && tags_ok(hu[1], T) && tags_ok(hu[2], T)) && spin < SPIN_CAP; ++spin) {
#pragma unroll
      for (int k = 0; k < 3; ++k) ld128_dev(hu[k], ho[k], h1);
      asm volatile("s_waitcnt vmcnt(0)" : "+v"(hu[0]), "+v"(hu[1]), "+v"(hu[2]) :: "memory");
    }
    float a0 = 0.f, a1 = 0.f;
#pragma unroll
    for (int src = 0; src < 4; ++src) {
      const int k = (src - s - 1) & 3;
      const float v0 = k == 0 ? __uint_as_float(hu[0].x) : k == 1 ? __uint_as_float(hu[1].x) : k == 2 ? __uint_as_float(hu[2].x) : acc[0];
      const float v1 = k == 0 ? __uint_as_float(hu[0].z) : k == 1 ? __uint_as_float(hu[1].z) : k == 2 ? __uint_as_float(hu[2].z) : acc[1];
      a0 += v0; a1 += v1;
    }
    *(LAS unsigned*)(L + L_OB + c * 144 + (32 * half + 16 * (i >> 1) + 4 * g + 2 * (i & 1)) * 2) = pack_bf16(a0, a1);
  }
  BARRIER();

  f32x4 pa[4];
#pragma unroll
  for (int jj = 0; jj < 4; ++jj) pa[jj] = f32x4{0.f, 0.f, 0.f, 0.f};
#pragma unroll
  for (int ks = 0; ks < 4; ++ks) {
    const v4u af = *(LAS v4u*)(L + L_OB + (8 * (ks >> 1) + (c & 7)) * 144 + (32 * (ks & 1) + 8 * g) * 2);
#pragma unroll
    for (int jj = 0; jj < 4; ++jj) pa[jj] = mfma16(af, wf[ks][jj], pa[jj]);
  }
  const int j32 = kvh * 4 + s;
  char* h2 = uni(ws + W_H2 + (size_t)b * (32 * 8 * 224 * 16));
  if (w < 7) {
#pragma unroll
    for (int rr = 0; rr < 2; ++rr) {
      const int q = 4 * (g & 1) + 2 * (g >> 1) + rr;
#pragma unroll
      for (int p = 0; p < 2; ++p) {
        const float v0 = (g >> 1) ? pa[2 * p][2 + rr] : pa[2 * p][rr];
        const float v1 = (g >> 1) ? pa[2 * p + 1][2 + rr] : pa[2 * p + 1][rr];
        st128_dev(((j32 * 8 + q) * 224 + w * 32 + p * 16 + c) * 16, v4u{__float_as_uint(v0), T, __float_as_uint(v1), T}, h2);
      }
    }
  }
  {
    const int lu = lane < 56 ? lane : 55;
    v4u fu[4];
    unsigned fo[4];
#pragma unroll
    for (int k = 0; k < 4; ++k) {
      fo[k] = (((4 * w + k) * 8 + (j32 >> 2)) * 224 + 56 * (j32 & 3) + lu) * 16;
      ld128_dev(fu[k], fo[k], h2);
    }
    asm volatile("s_waitcnt vmcnt(0)" : "+v"(fu[0]), "+v"(fu[1]), "+v"(fu[2]), "+v"(fu[3]) :: "memory");
    for (unsigned spin = 0; !wave_all(tags_ok(fu[0], T) && tags_ok(fu[1], T) && tags_ok(fu[2], T) && tags_ok(fu[3], T)) && spin < SPIN_CAP; ++spin) {
#pragma unroll
      for (int k = 0; k < 4; ++k) ld128_dev(fu[k], fo[k], h2);
      asm volatile("s_waitcnt vmcnt(0)" : "+v"(fu[0]), "+v"(fu[1]), "+v"(fu[2]), "+v"(fu[3]) :: "memory");
    }
    const float r0 = (__uint_as_float(fu[0].x) + __uint_as_float(fu[1].x)) + (__uint_as_float(fu[2].x) + __uint_as_float(fu[3].x));
    const float r1 = (__uint_as_float(fu[0].z) + __uint_as_float(fu[1].z)) + (__uint_as_float(fu[2].z) + __uint_as_float(fu[3].z));
    *(LAS f32x2*)(L + L_RED + w * 512 + lane * 8) = f32x2{r0, r1};
  }
  BARRIER();
  if (w == 0) {
    if (lane < 56) {
      f32x2 t = *(LAS f32x2*)(L + L_RED + lane * 8);
#pragma unroll
      for (int ww = 1; ww < 8; ++ww) t += *(LAS f32x2*)(L + L_RED + ww * 512 + lane * 8);
      const int u = 56 * (j32 & 3) + lane;
      const int n = 64 * (u >> 5) + 4 * (u & 15) + 2 * ((u >> 4) & 1);
      out[((b * SQ + (j32 >> 2)) * DM + n) >> 1] = pack_bf16(t[0], t[1]);
    }
    if ((b & 1) == 0 && j32 == 0 && lane == 0) st32_dev(0u, T, ws + W_EP + rb * 256);
  }
}

}

extern "C" void kernel_launch(void* const* d_in, const int* in_sizes, int n_in, void* d_out, int out_size, void* d_ws,
                              size_t ws_size, hipStream_t stream) {
  (void)in_sizes; (void)n_in; (void)out_size;
  if (ws_size < W_END) return;
  (void)hipFuncSetAttribute((const void*)attn_fused_kernel, hipFuncAttributeMaxDynamicSharedMemorySize, LDS_BYTES);
  attn_fused_kernel<<<dim3(256), dim3(512), LDS_BYTES, stream>>>((const char*)d_in[0], (const char*)d_in[1], (const char*)d_in[2],
                                                                 (const char*)d_in[3], (const char*)d_in[4], (unsigned*)d_out, (char*)d_ws);
}
```

```cpp
#include <hip/hip_runtime.h>
#include <hip/hip_bf16.h>

namespace {

typedef unsigned v4u __attribute__((ext_vector_type(4)));
typedef unsigned v2u __attribute__((ext_vector_type(2)));
typedef float f32x4 __attribute__((ext_vector_type(4)));
typedef float f32x2 __attribute__((ext_vector_type(2)));
typedef __bf16 bf16x8 __attribute__((ext_vector_type(8)));
typedef __bf16 bf16x2 __attribute__((ext_vector_type(2)));
typedef short s16x4 __attribute__((ext_vector_type(4)));
#define LAS __attribute__((address_space(3)))

constexpr int NB = 8, SQ = 8, DM = 448, HKV = 8, SK = 8192, WIN = 2048;
constexpr int NQ = 4096;
constexpr float C2 = 0.0625f * 1.4426950408889634f;
constexpr unsigned SPIN_CAP = 1u << 18;

constexpr int L_RING = 0;
constexpr int L_WQ = L_RING + 65536;
constexpr int L_P = 131072;
constexpr int L_X = L_P;
constexpr int L_QF = L_P + 16384;
constexpr int L_HALF = L_QF;
constexpr int L_MX = L_QF + 8192;
constexpr int L_LS = L_MX + 512;
constexpr int L_QR = L_LS + 512;
constexpr int L_OB = L_QR + 4096;
constexpr int L_RED = L_P;
constexpr int LDS_BYTES = 163840;
static_assert(L_OB + 16 * 144 <= LDS_BYTES, "LDS map");

constexpr size_t W_EP = 0;
constexpr size_t W_QX = 4096;
constexpr size_t W_ST = W_QX + (size_t)8 * 4 * 8 * 4096;
constexpr size_t W_H1 = W_ST + (size_t)64 * 4 * 256;
constexpr size_t W_H2 = W_H1 + (size_t)64 * 16 * 4096;
constexpr size_t W_END = W_H2 + (size_t)8 * 32 * 8 * 112 * 16;

__device__ __forceinline__ f32x4 mfma16(v4u a, v4u b, f32x4 c) {
  return __builtin_amdgcn_mfma_f32_16x16x32_bf16(__builtin_bit_cast(bf16x8, a), __builtin_bit_cast(bf16x8, b), c, 0, 0, 0);
}
__device__ __forceinline__ unsigned pack_bf16(float lo, float hi) {
  bf16x2 r; r[0] = (__bf16)lo; r[1] = (__bf16)hi;
  return __builtin_bit_cast(unsigned, r);
}
__device__ __forceinline__ float xg_max(float x) {
  auto s = __builtin_amdgcn_permlane16_swap(__float_as_uint(x), __float_as_uint(x), false, false);
  x = fmaxf(__uint_as_float(s[0]), __uint_as_float(s[1]));
  auto t = __builtin_amdgcn_permlane32_swap(__float_as_uint(x), __float_as_uint(x), false, false);
  return fmaxf(__uint_as_float(t[0]), __uint_as_float(t[1]));
}
__device__ __forceinline__ float xg_sum(float x) {
  auto s = __builtin_amdgcn_permlane16_swap(__float_as_uint(x), __float_as_uint(x), false, false);
  x = __uint_as_float(s[0]) + __uint_as_float(s[1]);
  auto t = __builtin_amdgcn_permlane32_swap(__float_as_uint(x), __float_as_uint(x), false, false);
  return __uint_as_float(t[0]) + __uint_as_float(t[1]);
}
__device__ __forceinline__ unsigned perm_lo(unsigned a, unsigned b) { return __builtin_amdgcn_perm(b, a, 0x05040100u); }
__device__ __forceinline__ unsigned perm_hi(unsigned a, unsigned b) { return __builtin_amdgcn_perm(b, a, 0x07060302u); }

#define WPOL ""
#define VMWAIT(N) asm volatile("s_waitcnt vmcnt(" #N ")" ::: "memory")
#define VMWAIT_S(N) VMWAIT(N)
#define LGKM0() asm volatile("s_waitcnt lgkmcnt(0)" ::: "memory")
#define BARRIER() asm volatile("s_waitcnt lgkmcnt(0)\n\ts_barrier" ::: "memory")

__device__ __forceinline__ void dma_half(const char* gbase, unsigned lds_dst, unsigned v0, unsigned v1, unsigned v2, unsigned v3) {
  unsigned keep;
  asm volatile(
      "s_mov_b32 %[keep], m0\n\ts_mov_b32 m0, %[l0]\n\ts_nop 2\n\t"
      "global_load_lds_dwordx4 %[v0], %[sb] nt\n\t"
      "global_load_lds_dwordx4 %[v1], %[sb] offset:1024 nt\n\t"
      "global_load_lds_dwordx4 %[v2], %[sb] offset:2048 nt\n\t"
      "global_load_lds_dwordx4 %[v3], %[sb] offset:3072 nt\n\t"
      "s_mov_b32 m0, %[keep]"
      : [keep] "=&s"(keep)
      : [v0] "v"(v0), [v1] "v"(v1), [v2] "v"(v2), [v3] "v"(v3), [sb] "s"(gbase), [l0] "s"(lds_dst)
      : "memory");
}
__device__ __forceinline__ void dma1w(const char* sb, unsigned lds_dst, unsigned voff) {
  unsigned keep;
  asm volatile("s_mov_b32 %[keep], m0\n\ts_mov_b32 m0, %[l0]\n\ts_nop 2\n\tglobal_load_lds_dwordx4 %[v0], %[sb]" WPOL "\n\ts_mov_b32 m0, %[keep]"
               : [keep] "=&s"(keep) : [v0] "v"(voff), [sb] "s"(sb), [l0] "s"(lds_dst) : "memory");
}
__device__ __forceinline__ void dma1(const char* sb, unsigned lds_dst, unsigned voff) {
  unsigned keep;
  asm volatile("s_mov_b32 %[keep], m0\n\ts_mov_b32 m0, %[l0]\n\ts_nop 2\n\tglobal_load_lds_dwordx4 %[v0], %[sb] nt\n\ts_mov_b32 m0, %[keep]"
               : [keep] "=&s"(keep) : [v0] "v"(voff), [sb] "s"(sb), [l0] "s"(lds_dst) : "memory");
}
__device__ __forceinline__ void ld64(v2u& d, unsigned voff, const char* sb) {
  asm volatile("global_load_dwordx2 %0, %1, %2" WPOL : "=v"(d) : "v"(voff), "s"(sb) : "memory");
}
__device__ __forceinline__ void ld128_dev(v4u& d, unsigned voff, const char* sb) {
  asm volatile("global_load_dwordx4 %0, %1, %2 sc1" : "=v"(d) : "v"(voff), "s"(sb) : "memory");
}
__device__ __forceinline__ void ld32_dev(unsigned& d, unsigned voff, const char* sb) {
  asm volatile("global_load_dword %0, %1, %2 sc1" : "=v"(d) : "v"(voff), "s"(sb) : "memory");
}
__device__ __forceinline__ void st128_dev(unsigned voff, v4u v, char* sb) {
  asm volatile("global_store_dwordx4 %0, %1, %2 sc1\n\ts_nop 1" : : "v"(voff), "v"(v), "s"(sb) : "memory");
}
#define H1POL " sc1"
__device__ __forceinline__ void st128_h1(unsigned voff, v4u v, char* sb) {
  asm volatile("global_store_dwordx4 %0, %1, %2" H1POL "\n\ts_nop 1" : : "v"(voff), "v"(v), "s"(sb) : "memory");
}
__device__ __forceinline__ void st32_dev(unsigned voff, unsigned v, char* sb) {
  asm volatile("global_store_dword %0, %1, %2 sc1" : : "v"(voff), "v"(v), "s"(sb) : "memory");
}
__device__ __forceinline__ bool tags_ok(v4u u, unsigned T) { return u.y == T && u.w == T; }
constexpr unsigned TMASK = 0x3FFu;
__device__ __forceinline__ unsigned enc(float v, unsigned T) { return ((__float_as_uint(v) + 0x200u) & ~TMASK) | (T & TMASK); }
__device__ __forceinline__ float dec(unsigned u) { return __uint_as_float(u & ~TMASK); }
__device__ __forceinline__ v4u enc4(f32x4 a, unsigned T) { return v4u{enc(a[0], T), enc(a[1], T), enc(a[2], T), enc(a[3], T)}; }
__device__ __forceinline__ f32x4 dec4(v4u u) { return f32x4{dec(u.x), dec(u.y), dec(u.z), dec(u.w)}; }
__device__ __forceinline__ bool quad_ok(v4u u, unsigned T) { return (((u.x ^ T) | (u.y ^ T) | (u.z ^ T) | (u.w ^ T)) & TMASK) == 0u; }
template <class P> __device__ __forceinline__ P* uni(P* p) {
  const unsigned long long a = (unsigned long long)p;
  const unsigned lo = __builtin_amdgcn_readfirstlane((unsigned)a), hi = __builtin_amdgcn_readfirstlane((unsigned)(a >> 32));
  return (P*)(((unsigned long long)hi << 32) | lo);
}
__device__ __forceinline__ bool wave_all(bool ok) { return __builtin_amdgcn_ballot_w64(!ok) == 0ull; }
__device__ __forceinline__ void reload1(v4u& d0, unsigned o0, const char* sb) {
  asm volatile("global_load_dwordx4 %0, %1, %2 sc1\n\ts_waitcnt vmcnt(0)" : "=&v"(d0) : "v"(o0), "s"(sb) : "memory");
}
__device__ __forceinline__ void reload2(v4u& d0, v4u& d1, unsigned o0, unsigned o1, const char* sb) {
  asm volatile("global_load_dwordx4 %0, %2, %4 sc1\n\tglobal_load_dwordx4 %1, %3, %4 sc1\n\ts_waitcnt vmcnt(0)"
               : "=&v"(d0), "=&v"(d1) : "v"(o0), "v"(o1), "s"(sb) : "memory");
}

#define STAMP(i) do { } while (0)

__global__ void __launch_bounds__(512, 2)
attn_fused_kernel(const char* __restrict__ x, const char* __restrict__ kc, const char* __restrict__ vc,
                  const char* __restrict__ wq, const char* __restrict__ wo, unsigned* __restrict__ out, char* __restrict__ ws) {
  extern __shared__ __attribute__((aligned(16))) unsigned char lds_raw[];
  LAS unsigned char* L = (LAS unsigned char*)lds_raw;
  const unsigned lbase = (unsigned)(size_t)L;
  STAMP(0);
  const int tid = threadIdx.x, lane = tid & 63, w = __builtin_amdgcn_readfirstlane(tid >> 6);
  const int g = lane >> 4, c = lane & 15;
  const int bid = blockIdx.x, kvh = bid & 7, s = (bid >> 3) & 3, b = bid >> 5;
  const int rot = 8 * s + b;
  const int rb = b >> 1, cb = (b & 1) * 4 + s;
  asm volatile("" :: "s"(x), "s"(kc), "s"(vc), "s"(wq), "s"(wo), "s"(out), "s"(ws));

  unsigned ep;
  ld32_dev(ep, 0u, ws + W_EP + rb * 256);
  {
    const unsigned r8 = lane >> 3, f = ((r8 >> 1) & 1) | ((w & 1) << 1);
    const unsigned vo = r8 * (NQ * 2) + (((lane & 7) ^ (f << 1)) << 4);
    const char* wqb = wq + (size_t)(8 * w) * (NQ * 2) + (kvh * 512 + cb * 64) * 2;
#pragma unroll
    for (int i = 0; i < 7; ++i) dma1w(wqb + (size_t)(64 * i) * (NQ * 2), lbase + L_WQ + (8 * i + w) * 1024, vo);
    const char* xb = x + (size_t)(16 * rb) * (DM * 2);
#pragma unroll
    for (int i = 0; i < 2; ++i) {
      const int m = 8 * i + w;
      if (m < 14) {
        const unsigned p = 64 * m + lane, row = (p * 1171u) >> 16, ch = p - 56 * row;
        dma1w(xb, lbase + L_X + m * 1024, row * (DM * 2) + (((ch & ~7u) | ((ch & 7u) ^ ((row >> 1) & 7u))) << 4));
      }
    }
  }
  asm volatile("s_barrier" ::: "memory");
  const size_t headrow = (size_t)(b * HKV + kvh) * SK + (SK - WIN) + s * 512;
  const char* kwin = kc + headrow * 512;
  const char* vwin = vc + headrow * 512;
  unsigned ko[8];
  {
    const unsigned hi = lane >> 5, l31 = lane & 31;
    const unsigned laneK = (hi << 9) | ((l31 ^ hi) << 4);
#pragma unroll
    for (int q = 0; q < 8; ++q) ko[q] = (laneK ^ (unsigned)(q << 5)) + ((q >> 2) << 12);
  }
  auto tile_off = [&](int unit) -> size_t { return (size_t)((unit + rot) & 31) * 8192; };
  auto k_half = [&](int t, int h) {
    dma_half(kwin + tile_off(8 * t + w), lbase + L_RING + ((t & 1) * 8 + w) * 8192 + h * 4096, ko[4 * h], ko[4 * h + 1], ko[4 * h + 2], ko[4 * h + 3]);
  };
  k_half(0, 0);
  asm volatile("s_waitcnt vmcnt(4)" : "+v"(ep) :: "memory");
  const unsigned T = __builtin_amdgcn_readfirstlane(ep) + 1u;
  STAMP(1);
  BARRIER();
  char* qx = uni(ws + W_QX + (size_t)(kvh * 4 + rb) * (8 * 4096));
  {
    const int nt = w & 3, kh = w >> 2;
    const int q4 = (lane & 15) >> 2, p4 = lane & 3;
    const unsigned fa = (unsigned)(q4 >> 1) | ((unsigned)(g & 1) << 1);
    const unsigned ta = L_WQ + (224 * kh + 8 * g + q4) * 128 + (((2 * nt + (p4 >> 1)) ^ (fa << 1)) << 4) + (p4 & 1) * 8;
    f32x4 qa = {0.f, 0.f, 0.f, 0.f};
#pragma unroll
    for (int i = 0; i < 7; ++i) {
      const s16x4 a0 = __builtin_amdgcn_ds_read_tr16_b64_v4i16((LAS s16x4*)(L + ta + i * 4096));
      const s16x4 a1 = __builtin_amdgcn_ds_read_tr16_b64_v4i16((LAS s16x4*)(L + ta + i * 4096 + 512));
      const unsigned ch = 4 * (7 * kh + i) + g;
      const v4u xb = *(LAS v4u*)(L + L_X + c * 896 + (((ch & ~7u) | ((ch & 7u) ^ ((c >> 1) & 7u))) << 4));
      const v2u x0 = __builtin_bit_cast(v2u, a0), x1 = __builtin_bit_cast(v2u, a1);
      qa = mfma16(v4u{x0.x, x0.y, x1.x, x1.y}, xb, qa);
    }
    if (kh == 1) *(LAS f32x4*)(L + L_QR + nt * 1024 + lane * 16) = qa;
    STAMP(17);
    BARRIER();
    if (kh == 0) {
      qa += *(LAS f32x4*)(L + L_QR + nt * 1024 + lane * 16);
      st128_dev(cb * 4096 + nt * 1024 + lane * 16, v4u{pack_bf16(qa[0], qa[1]), T, pack_bf16(qa[2], qa[3]), T}, qx);
    }
  }
  STAMP(18);
  k_half(0, 1);
  v4u qu[2];
  const unsigned qgo = w * 4096 + (lane >> 5) * 1024 + (16 * ((lane >> 3) & 3) + 8 * (b & 1) + (lane & 7)) * 16;
  ld128_dev(qu[0], qgo, qx);
  ld128_dev(qu[1], qgo + 2048, qx);
  STAMP(19);
  k_half(1, 0);
  STAMP(2);
  asm volatile("s_waitcnt vmcnt(4)" : "+v"(qu[0]), "+v"(qu[1]) :: "memory");
  STAMP(3);
  for (unsigned spin = 0; !wave_all(tags_ok(qu[0], T) && tags_ok(qu[1], T)) && spin < SPIN_CAP; ++spin) reload2(qu[0], qu[1], qgo, qgo + 2048, qx);
  {
    const int gq = (lane >> 3) & 3, q = lane & 7, h = w >> 2;
#pragma unroll
    for (int u = 0; u < 2; ++u) {
      const int nt = 2 * u + (lane >> 5);
      const int st = (w & 3) * 2 + (nt >> 1), gk = 2 * (nt & 1) + (gq >> 1);
      *(LAS v2u*)(L + L_QF + st * 1024 + (16 * gk + 8 * h + q) * 16 + (gq & 1) * 8) = v2u{qu[u].x, qu[u].z};
    }
  }
  STAMP(4);
  k_half(1, 1);
  BARRIER();
  v4u qf[8];
#pragma unroll
  for (int st = 0; st < 8; ++st) qf[st] = *(LAS v4u*)(L + L_QF + st * 1024 + lane * 16);

  f32x4 sc[4];
  auto qk_tile = [&](int slot) -> f32x4 {
    v4u kf[8];
#pragma unroll
    for (int st = 0; st < 8; ++st) kf[st] = *(LAS v4u*)(L + L_RING + slot * 8192 + c * 512 + (((4 * st + g) ^ c) << 4));
    f32x4 a = {0.f, 0.f, 0.f, 0.f};
#pragma unroll
    for (int st = 0; st < 8; ++st) a = mfma16(kf[st], qf[st], a);
    return a;
  };
  const unsigned vlane = (lane >> 3) * 512 + (((lane & 7) ^ (((lane >> 4) & 3) << 1)) << 4);
  auto v_piece = [&](int p, int q) {
    dma1(vwin + tile_off(4 * w + (q >> 1)) + (q & 1) * 4096 + ((s + 1 + p) & 3) * 128,
         lbase + L_RING + ((p & 1) * 8 + w) * 8192 + q * 1024, vlane);
  };
  const int wc = w < 7 ? w : 6;
  v2u wv[4][8];
  const char* wob = wo + (size_t)(2 * kvh * 256 + 64 * s) * (DM * 2) + wc * 128;
  const unsigned wvo = (unsigned)(8 * g) * (DM * 2) + c * 8;
#define WO_N(a, b) (b)
  auto wo_load = [&](int ks, int i) { ld64(wv[ks][i], wvo + (unsigned)(256 * (ks >> 1) + 32 * (ks & 1) + i) * (DM * 2), wob); };

  auto wo_loads = [&](int n0, int n1) {
#pragma unroll
    for (int n = n0; n < n1; ++n) wo_load(n >> 3, n & 7);
  };
  auto v_half = [&](int p, int h) {
#pragma unroll
    for (int q = 4 * h; q < 4 * h + 4; ++q) v_piece(p, q);
  };
  sc[0] = qk_tile(w);
  LGKM0();
  wo_loads(0, 4); k_half(2, 0);
  VMWAIT_S(8);
  sc[1] = qk_tile(8 + w);
  LGKM0();
  wo_loads(4, 7); k_half(2, 1); wo_loads(7, 10); k_half(3, 0);
  VMWAIT_S(7);
  sc[2] = qk_tile(w);
  LGKM0();
  wo_loads(10, 13); k_half(3, 1); wo_loads(13, 16); v_half(0, 0);
  VMWAIT_S(7);
  sc[3] = qk_tile(8 + w);
  LGKM0();
  v_half(0, 1);
  STAMP(5);
  float mx = fmaxf(fmaxf(fmaxf(sc[0][0], sc[0][1]), fmaxf(sc[0][2], sc[0][3])), fmaxf(fmaxf(sc[1][0], sc[1][1]), fmaxf(sc[1][2], sc[1][3])));
  mx = fmaxf(mx, fmaxf(fmaxf(fmaxf(sc[2][0], sc[2][1]), fmaxf(sc[2][2], sc[2][3])), fmaxf(fmaxf(sc[3][0], sc[3][1]), fmaxf(sc[3][2], sc[3][3]))));
  mx = xg_max(mx);
  if (g == 0) *(LAS float*)(L + L_MX + (w * 16 + c) * 4) = mx;
  BARRIER();
  float mrow = *(LAS float*)(L + L_MX + c * 4);
#pragma unroll
  for (int ww = 1; ww < 8; ++ww) mrow = fmaxf(mrow, *(LAS float*)(L + L_MX + (ww * 16 + c) * 4));
  mrow *= C2;
  float lsum = 0.f;
#pragma unroll
  for (int t = 0; t < 4; ++t) {
    float p[4];
#pragma unroll
    for (int r = 0; r < 4; ++r) { p[r] = __builtin_amdgcn_exp2f(fmaf(sc[t][r], C2, -mrow)); lsum += p[r]; }
    *(LAS v2u*)(L + L_P + (4 * t + (w >> 1)) * 1024 + lane * 16 + (w & 1) * 8) = v2u{pack_bf16(p[0], p[1]), pack_bf16(p[2], p[3])};
  }
  lsum = xg_sum(lsum);
  if (g == 0) *(LAS float*)(L + L_LS + (w * 16 + c) * 4) = lsum;
  wo_loads(16, 20); v_half(1, 0);

  const int ct = w & 3, kh = w >> 2;
  unsigned tra;
  {
    const int q4 = (lane & 15) >> 2, p4 = lane & 3;
    tra = (4 * g + q4) * 128 + (((2 * ct + (p4 >> 1)) ^ (((2 * g + (q4 >> 1)) & 3) << 1)) << 4) + (p4 & 1) * 8 + kh * 32768;
  }
  auto pv_pass = [&](int p) -> f32x4 {
    f32x4 a = {0.f, 0.f, 0.f, 0.f};
#pragma unroll
    for (int i = 0; i < 8; ++i) {
      const v4u pf = *(LAS v4u*)(L + L_P + (8 * kh + i) * 1024 + lane * 16);
      const int ub = L_RING + (p & 1) * 65536 + i * 4096;
      const s16x4 a0 = __builtin_amdgcn_ds_read_tr16_b64_v4i16((LAS s16x4*)(L + ub + tra));
      const s16x4 a1 = __builtin_amdgcn_ds_read_tr16_b64_v4i16((LAS s16x4*)(L + ub + 2048 + tra));
      const v2u x0 = __builtin_bit_cast(v2u, a0), x1 = __builtin_bit_cast(v2u, a1);
      a = mfma16(v4u{x0.x, x0.y, x1.x, x1.y}, pf, a);
    }
    return a;
  };
  const int bh = b * HKV + kvh;
  char* stb = uni(ws + W_ST + (size_t)bh * 1024);
  char* h1 = uni(ws + W_H1 + (size_t)bh * (16 * 4096));
  auto reduce_pass = [&](f32x4 a) -> f32x4 {
    if (kh == 1) *(LAS f32x4*)(L + L_HALF + ct * 1024 + lane * 16) = a;
    BARRIER();
    if (kh == 0) a += *(LAS f32x4*)(L + L_HALF + ct * 1024 + lane * 16);
    return a;
  };
  auto publish_pass = [&](int p, f32x4 a) {
    if (kh == 0) st128_h1(((((s + 1 + p) & 3) * 4 + s) * 4 + ct) * 1024 + lane * 16, enc4(a, T), h1);
  };
  STAMP(6);
  VMWAIT_S(8);
  BARRIER();
  STAMP(7);
  {
    const f32x4 a = reduce_pass(pv_pass(0));
    wo_loads(20, 23); v_half(1, 1);
    asm volatile("s_barrier" ::: "memory");
    wo_loads(23, 26); v_half(2, 0);
    publish_pass(0, a);
    if (w == 7) {
      float lt = *(LAS float*)(L + L_LS + c * 4);
#pragma unroll
      for (int ww = 1; ww < 8; ++ww) lt += *(LAS float*)(L + L_LS + (ww * 16 + c) * 4);
      if (g == 0) st128_dev((s * 16 + c) * 16, v4u{__float_as_uint(mrow), T, __float_as_uint(lt), T}, stb);
    }
    if (kh == 0 || w == 7) VMWAIT_S(8); else VMWAIT_S(7);
  }
  BARRIER();
  v4u stu;
  {
    const f32x4 a = reduce_pass(pv_pass(1));
    wo_loads(26, 32); v_half(2, 1);
    asm volatile("s_barrier" ::: "memory");
    v_half(3, 0);
    publish_pass(1, a);
  }
  ld128_dev(stu, lane * 16, stb);
  if (kh == 0) VMWAIT(6); else VMWAIT(5);
  asm volatile(""
               : "+v"(wv[0][0]), "+v"(wv[0][1]), "+v"(wv[0][2]), "+v"(wv[0][3]), "+v"(wv[0][4]), "+v"(wv[0][5]), "+v"(wv[0][6]), "+v"(wv[0][7]),
                 "+v"(wv[1][0]), "+v"(wv[1][1]), "+v"(wv[1][2]), "+v"(wv[1][3]), "+v"(wv[1][4]), "+v"(wv[1][5]), "+v"(wv[1][6]), "+v"(wv[1][7])
               :: "memory");
  asm volatile("" : "+v"(wv[2][0]), "+v"(wv[2][1]), "+v"(wv[2][2]), "+v"(wv[2][3]), "+v"(wv[2][4]), "+v"(wv[2][5]), "+v"(wv[2][6]), "+v"(wv[2][7]),
                    "+v"(wv[3][0]), "+v"(wv[3][1]), "+v"(wv[3][2]), "+v"(wv[3][3]), "+v"(wv[3][4]), "+v"(wv[3][5]), "+v"(wv[3][6]), "+v"(wv[3][7])
               :: "memory");
  BARRIER();
  publish_pass(2, reduce_pass(pv_pass(2)));
  v_half(3, 1);
  v4u hu[2];
  const int srcA = (s + (kh ? 3 : 1)) & 3, srcB = (s + (kh ? 3 : 2)) & 3;
  const unsigned hoA = ((s * 4 + srcA) * 4 + ct) * 1024 + lane * 16, hoB = ((s * 4 + srcB) * 4 + ct) * 1024 + lane * 16;
  ld128_dev(hu[0], hoA, h1);
  ld128_dev(hu[1], hoB, h1);
  v4u wf[4][4];
#pragma unroll
  for (int ks = 0; ks < 4; ++ks)
#pragma unroll
    for (int jj = 0; jj < 4; ++jj)
#pragma unroll
      for (int r = 0; r < 4; ++r) {
        const unsigned lo = wv[ks][2 * r][jj >> 1], hi = wv[ks][2 * r + 1][jj >> 1];
        wf[ks][jj][r] = (jj & 1) ? perm_hi(lo, hi) : perm_lo(lo, hi);
      }
  asm volatile("s_waitcnt vmcnt(6)" : "+v"(stu) :: "memory");
  for (unsigned spin = 0; !wave_all(tags_ok(stu, T)) && spin < SPIN_CAP; ++spin) reload1(stu, lane * 16, stb);
  float fS, fA, fB;
  {
    const float ms = __uint_as_float(stu.x), ls = __uint_as_float(stu.z);
    const float M = xg_max(ms);
    const float e = __builtin_amdgcn_exp2f(ms - M);
    const float fw = e / xg_sum(e * ls);
    fS = __shfl(fw, 16 * s + c); fA = __shfl(fw, 16 * srcA + c); fB = kh ? 0.f : __shfl(fw, 16 * srcB + c);
  }
  asm volatile("s_waitcnt vmcnt(0)" : "+v"(hu[0]), "+v"(hu[1]) :: "memory");
  STAMP(8);
  BARRIER();
  f32x4 own = pv_pass(3);
  STAMP(9);
  for (unsigned spin = 0; !wave_all(quad_ok(hu[0], T) && quad_ok(hu[1], T)) && spin < SPIN_CAP; ++spin) reload2(hu[0], hu[1], hoA, hoB, h1);
  STAMP(11);
  {
    own = own * fS + dec4(hu[0]) * fA + dec4(hu[1]) * fB;
    if (kh == 1) *(LAS f32x4*)(L + L_HALF + ct * 1024 + lane * 16) = own;
    BARRIER();
    if (kh == 0) {
      own += *(LAS f32x4*)(L + L_HALF + ct * 1024 + lane * 16);
      *(LAS v2u*)(L + L_OB + c * 144 + (16 * ct + 4 * g) * 2) = v2u{pack_bf16(own[0], own[1]), pack_bf16(own[2], own[3])};
    }
  }
  BARRIER();

  f32x4 pa[4];
#pragma unroll
  for (int jj = 0; jj < 4; ++jj) pa[jj] = f32x4{0.f, 0.f, 0.f, 0.f};
#pragma unroll
  for (int ks = 0; ks < 4; ++ks) {
    const v4u af = *(LAS v4u*)(L + L_OB + (8 * (ks >> 1) + (c & 7)) * 144 + (32 * (ks & 1) + 8 * g) * 2);
#pragma unroll
    for (int jj = 0; jj < 4; ++jj) pa[jj] = mfma16(af, wf[ks][jj], pa[jj]);
  }
  const int j32 = kvh * 4 + s;
  char* h2 = uni(ws + W_H2 + (size_t)b * (32 * 8 * 112 * 16));
  if (w < 7) {
#pragma unroll
    for (int rr = 0; rr < 2; ++rr) {
      const int q = 4 * (g & 1) + 2 * (g >> 1) + rr;
      const f32x4 v = (g >> 1) ? f32x4{pa[0][2 + rr], pa[1][2 + rr], pa[2][2 + rr], pa[3][2 + rr]} : f32x4{pa[0][rr], pa[1][rr], pa[2][rr], pa[3][rr]};
      st128_dev(((j32 * 8 + q) * 112 + w * 16 + c) * 16, enc4(v, T), h2);
    }
  }
  STAMP(12);
  {
    const int l31 = lane & 31, lu = l31 < 28 ? l31 : 27;
    v4u fu[2];
    unsigned fo[2];
#pragma unroll
    for (int k = 0; k < 2; ++k) {
      fo[k] = (((4 * w + 2 * k + (lane >> 5)) * 8 + (j32 >> 2)) * 112 + 28 * (j32 & 3) + lu) * 16;
      ld128_dev(fu[k], fo[k], h2);
    }
    asm volatile("s_waitcnt vmcnt(0)" : "+v"(fu[0]), "+v"(fu[1]) :: "memory");
    for (unsigned spin = 0; !wave_all(quad_ok(fu[0], T) && quad_ok(fu[1], T)) && spin < SPIN_CAP; ++spin) reload2(fu[0], fu[1], fo[0], fo[1], h2);
    STAMP(13);
    *(LAS f32x4*)(L + L_RED + w * 1024 + lane * 16) = dec4(fu[0]) + dec4(fu[1]);
  }
  BARRIER();
  if (w == 0) {
    const int l31 = lane & 31, hf = lane >> 5;
    f32x4 t = *(LAS f32x4*)(L + L_RED + (4 * hf) * 1024 + l31 * 16);
    t += *(LAS f32x4*)(L + L_RED + (4 * hf) * 1024 + 512 + l31 * 16);
#pragma unroll
    for (int ww = 1; ww < 4; ++ww) {
      t += *(LAS f32x4*)(L + L_RED + (4 * hf + ww) * 1024 + l31 * 16);
      t += *(LAS f32x4*)(L + L_RED + (4 * hf + ww) * 1024 + 512 + l31 * 16);
    }
#pragma unroll
    for (int e = 0; e < 4; ++e) {
      auto sw = __builtin_amdgcn_permlane32_swap(__float_as_uint(t[e]), __float_as_uint(t[e]), false, false);
      t[e] = __uint_as_float(sw[0]) + __uint_as_float(sw[1]);
    }
    if (lane < 28)
      *(v2u*)(out + (((b * SQ + (j32 >> 2)) * DM + 112 * (j32 & 3) + 4 * lane) >> 1)) = v2u{pack_bf16(t[0], t[1]), pack_bf16(t[2], t[3])};
    if ((b & 1) == 0 && j32 == 0 && lane == 0) st32_dev(0u, T, ws + W_EP + rb * 256);
  }
}

}

extern "C" void kernel_launch(void* const* d_in, const int* in_sizes, int n_in, void* d_out, int out_size, void* d_ws,
                              size_t ws_size, hipStream_t stream) {
  (void)in_sizes; (void)n_in; (void)out_size;
  if (ws_size < W_END) return;
  (void)hipFuncSetAttribute((const void*)attn_fused_kernel, hipFuncAttributeMaxDynamicSharedMemorySize, LDS_BYTES);
  attn_fused_kernel<<<dim3(256), dim3(512), LDS_BYTES, stream>>>((const char*)d_in[0], (const char*)d_in[1], (const char*)d_in[2],
                                                                 (const char*)d_in[3], (const char*)d_in[4], (unsigned*)d_out, (char*)d_ws);
}
```

```cpp
#define WPOL ""
#include <hip/hip_runtime.h>
#include <hip/hip_bf16.h>

namespace {

typedef unsigned v4u __attribute__((ext_vector_type(4)));
typedef unsigned v2u __attribute__((ext_vector_type(2)));
typedef float f32x4 __attribute__((ext_vector_type(4)));
typedef float f32x2 __attribute__((ext_vector_type(2)));
typedef __bf16 bf16x8 __attribute__((ext_vector_type(8)));
typedef __bf16 bf16x2 __attribute__((ext_vector_type(2)));
typedef short s16x4 __attribute__((ext_vector_type(4)));
#define LAS __attribute__((address_space(3)))

constexpr int NB = 8, SQ = 8, DM = 448, HKV = 8, SK = 8192, WIN = 2048;
constexpr int NQ = 4096;
constexpr float C2 = 0.0625f * 1.4426950408889634f;
constexpr unsigned SPIN_CAP = 1u << 18;

constexpr int L_RING = 0;
constexpr int L_WQ = L_RING + 65536;
constexpr int L_P = 131072;
constexpr int L_X = L_P;
constexpr int L_QF = L_P + 16384;
constexpr int L_HALF = L_QF;
constexpr int L_MX = L_QF + 8192;
constexpr int L_LS = L_MX + 512;
constexpr int L_QR = L_LS + 512;
constexpr int L_OB = L_QR + 4096;
constexpr int L_RED = L_P;
constexpr int LDS_BYTES = 163840;
static_assert(L_OB + 16 * 144 <= LDS_BYTES, "LDS map");

constexpr size_t W_EP = 0;
constexpr size_t W_QX = 4096;
constexpr size_t W_ST = W_QX + (size_t)8 * 4 * 8 * 4096;
constexpr size_t W_H1 = W_ST + (size_t)64 * 4 * 256;
constexpr size_t W_H2 = W_H1 + (size_t)64 * 16 * 4096;
constexpr size_t W_END = W_H2 + (size_t)8 * 32 * 8 * 112 * 16;

__device__ __forceinline__ f32x4 mfma16(v4u a, v4u b, f32x4 c) {
  return __builtin_amdgcn_mfma_f32_16x16x32_bf16(__builtin_bit_cast(bf16x8, a), __builtin_bit_cast(bf16x8, b), c, 0, 0, 0);
}
__device__ __forceinline__ unsigned pack_bf16(float lo, float hi) {
  bf16x2 r; r[0] = (__bf16)lo; r[1] = (__bf16)hi;
  return __builtin_bit_cast(unsigned, r);
}
__device__ __forceinline__ float xg_max(float x) {
  auto s = __builtin_amdgcn_permlane16_swap(__float_as_uint(x), __float_as_uint(x), false, false);
  x = fmaxf(__uint_as_float(s[0]), __uint_as_float(s[1]));
  auto t = __builtin_amdgcn_permlane32_swap(__float_as_uint(x), __float_as_uint(x), false, false);
  return fmaxf(__uint_as_float(t[0]), __uint_as_float(t[1]));
}
__device__ __forceinline__ float xg_sum(float x) {
  auto s = __builtin_amdgcn_permlane16_swap(__float_as_uint(x), __float_as_uint(x), false, false);
  x = __uint_as_float(s[0]) + __uint_as_float(s[1]);
  auto t = __builtin_amdgcn_permlane32_swap(__float_as_uint(x), __float_as_uint(x), false, false);
  return __uint_as_float(t[0]) + __uint_as_float(t[1]);
}
__device__ __forceinline__ unsigned perm_lo(unsigned a, unsigned b) { return __builtin_amdgcn_perm(b, a, 0x05040100u); }
__device__ __forceinline__ unsigned perm_hi(unsigned a, unsigned b) { return __builtin_amdgcn_perm(b, a, 0x07060302u); }

#define KPOL " nt"
#define VPOL " nt"
#define VMWAIT(N) asm volatile("s_waitcnt vmcnt(" #N ")" ::: "memory")
#define VMWAIT_S(N) VMWAIT(N)
#define LGKM0() asm volatile("s_waitcnt lgkmcnt(0)" ::: "memory")
#define BARRIER() asm volatile("s_waitcnt lgkmcnt(0)\n\ts_barrier" ::: "memory")

__device__ __forceinline__ void dma_half(const char* gbase, unsigned lds_dst, unsigned v0, unsigned v1, unsigned v2, unsigned v3) {
  unsigned keep;
  asm volatile(
      "s_mov_b32 %[keep], m0\n\ts_mov_b32 m0, %[l0]\n\ts_nop 2\n\t"
      "global_load_lds_dwordx4 %[v0], %[sb]" KPOL "\n\t"
      "global_load_lds_dwordx4 %[v1], %[sb] offset:1024" KPOL "\n\t"
      "global_load_lds_dwordx4 %[v2], %[sb] offset:2048" KPOL "\n\t"
      "global_load_lds_dwordx4 %[v3], %[sb] offset:3072" KPOL "\n\t"
      "s_mov_b32 m0, %[keep]"
      : [keep] "=&s"(keep)
      : [v0] "v"(v0), [v1] "v"(v1), [v2] "v"(v2), [v3] "v"(v3), [sb] "s"(gbase), [l0] "s"(lds_dst)
      : "memory");
}
__device__ __forceinline__ void dma1w(const char* sb, unsigned lds_dst, unsigned voff) {
  unsigned keep;
  asm volatile("s_mov_b32 %[keep], m0\n\ts_mov_b32 m0, %[l0]\n\ts_nop 2\n\tglobal_load_lds_dwordx4 %[v0], %[sb]" WPOL "\n\ts_mov_b32 m0, %[keep]"
               : [keep] "=&s"(keep) : [v0] "v"(voff), [sb] "s"(sb), [l0] "s"(lds_dst) : "memory");
}
__device__ __forceinline__ void dma1(const char* sb, unsigned lds_dst, unsigned voff) {
  unsigned keep;
  asm volatile("s_mov_b32 %[keep], m0\n\ts_mov_b32 m0, %[l0]\n\ts_nop 2\n\tglobal_load_lds_dwordx4 %[v0], %[sb]" VPOL "\n\ts_mov_b32 m0, %[keep]"
               : [keep] "=&s"(keep) : [v0] "v"(voff), [sb] "s"(sb), [l0] "s"(lds_dst) : "memory");
}
__device__ __forceinline__ void ld64(v2u& d, unsigned voff, const char* sb) {
  asm volatile("global_load_dwordx2 %0, %1, %2" WPOL : "=v"(d) : "v"(voff), "s"(sb) : "memory");
}
__device__ __forceinline__ void ld128_dev(v4u& d, unsigned voff, const char* sb) {
  asm volatile("global_load_dwordx4 %0, %1, %2 sc1" : "=v"(d) : "v"(voff), "s"(sb) : "memory");
}
__device__ __forceinline__ void ld32_dev(unsigned& d, unsigned voff, const char* sb) {
  asm volatile("global_load_dword %0, %1, %2 sc1" : "=v"(d) : "v"(voff), "s"(sb) : "memory");
}
__device__ __forceinline__ void st128_dev(unsigned voff, v4u v, char* sb) {
  asm volatile("global_store_dwordx4 %0, %1, %2 sc1\n\ts_nop 1" : : "v"(voff), "v"(v), "s"(sb) : "memory");
}
#define H1POL " sc1"
__device__ __forceinline__ void st128_h1(unsigned voff, v4u v, char* sb) {
  asm volatile("global_store_dwordx4 %0, %1, %2" H1POL "\n\ts_nop 1" : : "v"(voff), "v"(v), "s"(sb) : "memory");
}
__device__ __forceinline__ void st32_dev(unsigned voff, unsigned v, char* sb) {
  asm volatile("global_store_dword %0, %1, %2 sc1" : : "v"(voff), "v"(v), "s"(sb) : "memory");
}
__device__ __forceinline__ bool tags_ok(v4u u, unsigned T) { return u.y == T && u.w == T; }
constexpr unsigned TMASK = 0x3FFu;
__device__ __forceinline__ unsigned enc(float v, unsigned T) { return ((__float_as_uint(v) + 0x200u) & ~TMASK) | (T & TMASK); }
__device__ __forceinline__ float dec(unsigned u) { return __uint_as_float(u & ~TMASK); }
__device__ __forceinline__ v4u enc4(f32x4 a, unsigned T) { return v4u{enc(a[0], T), enc(a[1], T), enc(a[2], T), enc(a[3], T)}; }
__device__ __forceinline__ f32x4 dec4(v4u u) { return f32x4{dec(u.x), dec(u.y), dec(u.z), dec(u.w)}; }
__device__ __forceinline__ bool quad_ok(v4u u, unsigned T) { return (((u.x ^ T) | (u.y ^ T) | (u.z ^ T) | (u.w ^ T)) & TMASK) == 0u; }
template <class P> __device__ __forceinline__ P* uni(P* p) {
  const unsigned long long a = (unsigned long long)p;
  const unsigned lo = __builtin_amdgcn_readfirstlane((unsigned)a), hi = __builtin_amdgcn_readfirstlane((unsigned)(a >> 32));
  return (P*)(((unsigned long long)hi << 32) | lo);
}
__device__ __forceinline__ bool wave_all(bool ok) { return __builtin_amdgcn_ballot_w64(!ok) == 0ull; }
__device__ __forceinline__ void reload1(v4u& d0, unsigned o0, const char* sb) {
  asm volatile("global_load_dwordx4 %0, %1, %2 sc1\n\ts_waitcnt vmcnt(0)" : "=&v"(d0) : "v"(o0), "s"(sb) : "memory");
}
__device__ __forceinline__ void reload2(v4u& d0, v4u& d1, unsigned o0, unsigned o1, const char* sb) {
  asm volatile("global_load_dwordx4 %0, %2, %4 sc1\n\tglobal_load_dwordx4 %1, %3, %4 sc1\n\ts_waitcnt vmcnt(0)"
               : "=&v"(d0), "=&v"(d1) : "v"(o0), "v"(o1), "s"(sb) : "memory");
}

#define STAMP(i) do { } while (0)

__global__ void __launch_bounds__(512, 2)
attn_fused_kernel(const char* __restrict__ x, const char* __restrict__ kc, const char* __restrict__ vc,
                  const char* __restrict__ wq, const char* __restrict__ wo, unsigned* __restrict__ out, char* __restrict__ ws) {
  extern __shared__ __attribute__((aligned(16))) unsigned char lds_raw[];
  LAS unsigned char* L = (LAS unsigned char*)lds_raw;
  const unsigned lbase = (unsigned)(size_t)L;
  STAMP(0);
  const int tid = threadIdx.x, lane = tid & 63, w = __builtin_amdgcn_readfirstlane(tid >> 6);
  const int g = lane >> 4, c = lane & 15;
  const int bid = blockIdx.x, kvh = bid & 7, s = (bid >> 3) & 3, b = bid >> 5;
  const int rot = 8 * s + b;
  const int rb = b >> 1, cb = (b & 1) * 4 + s;
  asm volatile("" :: "s"(x), "s"(kc), "s"(vc), "s"(wq), "s"(wo), "s"(out), "s"(ws));

  unsigned ep;
  ld32_dev(ep, 0u, ws + W_EP + rb * 256);
  {
    const unsigned r8 = lane >> 3, f = ((r8 >> 1) & 1) | ((w & 1) << 1);
    const unsigned vo = r8 * (NQ * 2) + (((lane & 7) ^ (f << 1)) << 4);
    const char* wqb = wq + (size_t)(8 * w) * (NQ * 2) + (kvh * 512 + cb * 64) * 2;
#pragma unroll
    for (int i = 0; i < 7; ++i) dma1w(wqb + (size_t)(64 * i) * (NQ * 2), lbase + L_WQ + (8 * i + w) * 1024, vo);
    const char* xb = x + (size_t)(16 * rb) * (DM * 2);
#pragma unroll
    for (int i = 0; i < 2; ++i) {
      const int m = 8 * i + w;
      if (m < 14) {
        const unsigned p = 64 * m + lane, row = (p * 1171u) >> 16, ch = p - 56 * row;
        dma1w(xb, lbase + L_X + m * 1024, row * (DM * 2) + (((ch & ~7u) | ((ch & 7u) ^ ((row >> 1) & 7u))) << 4));
      }
    }
  }
  asm volatile("s_barrier" ::: "memory");
  const size_t headrow = (size_t)(b * HKV + kvh) * SK + (SK - WIN) + s * 512;
  const char* kwin = kc + headrow * 512;
  const char* vwin = vc + headrow * 512;
  unsigned ko[8];
  {
    const unsigned hi = lane >> 5, l31 = lane & 31;
    const unsigned laneK = (hi << 9) | ((l31 ^ hi) << 4);
#pragma unroll
    for (int q = 0; q < 8; ++q) ko[q] = (laneK ^ (unsigned)(q << 5)) + ((q >> 2) << 12);
  }
  auto tile_off = [&](int unit) -> size_t { return (size_t)((unit + rot) & 31) * 8192; };
  auto k_half = [&](int t, int h) {
    dma_half(kwin + tile_off(8 * t + w), lbase + L_RING + ((t & 1) * 8 + w) * 8192 + h * 4096, ko[4 * h], ko[4 * h + 1], ko[4 * h + 2], ko[4 * h + 3]);
  };
  k_half(0, 0);
  asm volatile("s_waitcnt vmcnt(4)" : "+v"(ep) :: "memory");
  const unsigned T = __builtin_amdgcn_readfirstlane(ep) + 1u;
  STAMP(1);
  BARRIER();
  char* qx = uni(ws + W_QX + (size_t)(kvh * 4 + rb) * (8 * 4096));
  {
    const int nt = w & 3, kh = w >> 2;
    const int q4 = (lane & 15) >> 2, p4 = lane & 3;
    const unsigned fa = (unsigned)(q4 >> 1) | ((unsigned)(g & 1) << 1);
    const unsigned ta = L_WQ + (224 * kh + 8 * g + q4) * 128 + (((2 * nt + (p4 >> 1)) ^ (fa << 1)) << 4) + (p4 & 1) * 8;
    f32x4 qa = {0.f, 0.f, 0.f, 0.f};
#pragma unroll
    for (int i = 0; i < 7; ++i) {
      const s16x4 a0 = __builtin_amdgcn_ds_read_tr16_b64_v4i16((LAS s16x4*)(L + ta + i * 4096));
      const s16x4 a1 = __builtin_amdgcn_ds_read_tr16_b64_v4i16((LAS s16x4*)(L + ta + i * 4096 + 512));
      const unsigned ch = 4 * (7 * kh + i) + g;
      const v4u xb = *(LAS v4u*)(L + L_X + c * 896 + (((ch & ~7u) | ((ch & 7u) ^ ((c >> 1) & 7u))) << 4));
      const v2u x0 = __builtin_bit_cast(v2u, a0), x1 = __builtin_bit_cast(v2u, a1);
      qa = mfma16(v4u{x0.x, x0.y, x1.x, x1.y}, xb, qa);
    }
    if (kh == 1) *(LAS f32x4*)(L + L_QR + nt * 1024 + lane * 16) = qa;
    STAMP(17);
    BARRIER();
    if (kh == 0) {
      qa += *(LAS f32x4*)(L + L_QR + nt * 1024 + lane * 16);
      st128_dev(cb * 4096 + nt * 1024 + lane * 16, v4u{pack_bf16(qa[0], qa[1]), T, pack_bf16(qa[2], qa[3]), T}, qx);
    }
  }
  STAMP(18);
  k_half(0, 1);
  v4u qu[2];
  const unsigned qgo = w * 4096 + (lane >> 5) * 1024 + (16 * ((lane >> 3) & 3) + 8 * (b & 1) + (lane & 7)) * 16;
  ld128_dev(qu[0], qgo, qx);
  ld128_dev(qu[1], qgo + 2048, qx);
  STAMP(19);
  k_half(1, 0);
  STAMP(2);
  asm volatile("s_waitcnt vmcnt(4)" : "+v"(qu[0]), "+v"(qu[1]) :: "memory");
  STAMP(3);
  for (unsigned spin = 0; !wave_all(tags_ok(qu[0], T) && tags_ok(qu[1], T)) && spin < SPIN_CAP; ++spin) reload2(qu[0], qu[1], qgo, qgo + 2048, qx);
  {
    const int gq = (lane >> 3) & 3, q = lane & 7, h = w >> 2;
#pragma unroll
    for (int u = 0; u < 2; ++u) {
      const int nt = 2 * u + (lane >> 5);
      const int st = (w & 3) * 2 + (nt >> 1), gk = 2 * (nt & 1) + (gq >> 1);
      *(LAS v2u*)(L + L_QF + st * 1024 + (16 * gk + 8 * h + q) * 16 + (gq & 1) * 8) = v2u{qu[u].x, qu[u].z};
    }
  }
  STAMP(4);
  k_half(1, 1);
  BARRIER();
  v4u qf[8];
#pragma unroll
  for (int st = 0; st < 8; ++st) qf[st] = *(LAS v4u*)(L + L_QF + st * 1024 + lane * 16);

  f32x4 sc[4];
  auto qk_tile = [&](int slot) -> f32x4 {
    v4u kf[8];
#pragma unroll
    for (int st = 0; st < 8; ++st) kf[st] = *(LAS v4u*)(L + L_RING + slot * 8192 + c * 512 + (((4 * st + g) ^ c) << 4));
    f32x4 a = {0.f, 0.f, 0.f, 0.f};
#pragma unroll
    for (int st = 0; st < 8; ++st) a = mfma16(kf[st], qf[st], a);
    return a;
  };
  const unsigned vlane = (lane >> 3) * 512 + (((lane & 7) ^ (((lane >> 4) & 3) << 1)) << 4);
  auto v_piece = [&](int p, int q) {
    dma1(vwin + tile_off(4 * w + (q >> 1)) + (q & 1) * 4096 + ((s + 1 + p) & 3) * 128,
         lbase + L_RING + ((p & 1) * 8 + w) * 8192 + q * 1024, vlane);
  };
  const int wc = w < 7 ? w : 6;
  v2u wv[4][8];
  const char* wob = wo + (size_t)(2 * kvh * 256 + 64 * s) * (DM * 2) + wc * 128;
  const unsigned wvo = (unsigned)(8 * g) * (DM * 2) + c * 8;
#define WO_N(a, b) (b)
  auto wo_load = [&](int ks, int i) { ld64(wv[ks][i], wvo + (unsigned)(256 * (ks >> 1) + 32 * (ks & 1) + i) * (DM * 2), wob); };

  auto wo_loads = [&](int n0, int n1) {
#pragma unroll
    for (int n = n0; n < n1; ++n) wo_load(n >> 3, n & 7);
  };
  auto v_half = [&](int p, int h) {
#pragma unroll
    for (int q = 4 * h; q < 4 * h + 4; ++q) v_piece(p, q);
  };
  sc[0] = qk_tile(w);
  LGKM0();
  wo_loads(0, 4); k_half(2, 0);
  VMWAIT_S(8);
  sc[1] = qk_tile(8 + w);
  LGKM0();
  wo_loads(4, 7); k_half(2, 1); wo_loads(7, 10); k_half(3, 0);
  VMWAIT_S(7);
  sc[2] = qk_tile(w);
  LGKM0();
  wo_loads(10, 13); k_half(3, 1); wo_loads(13, 16); v_half(0, 0);
  VMWAIT_S(7);
  sc[3] = qk_tile(8 + w);
  LGKM0();
  v_half(0, 1);
  STAMP(5);
  float mx = fmaxf(fmaxf(fmaxf(sc[0][0], sc[0][1]), fmaxf(sc[0][2], sc[0][3])), fmaxf(fmaxf(sc[1][0], sc[1][1]), fmaxf(sc[1][2], sc[1][3])));
  mx = fmaxf(mx, fmaxf(fmaxf(fmaxf(sc[2][0], sc[2][1]), fmaxf(sc[2][2], sc[2][3])), fmaxf(fmaxf(sc[3][0], sc[3][1]), fmaxf(sc[3][2], sc[3][3]))));
  mx = xg_max(mx);
  if (g == 0) *(LAS float*)(L + L_MX + (w * 16 + c) * 4) = mx;
  BARRIER();
  float mrow = *(LAS float*)(L + L_MX + c * 4);
#pragma unroll
  for (int ww = 1; ww < 8; ++ww) mrow = fmaxf(mrow, *(LAS float*)(L + L_MX + (ww * 16 + c) * 4));
  mrow *= C2;
  float lsum = 0.f;
#pragma unroll
  for (int t = 0; t < 4; ++t) {
    float p[4];
#pragma unroll
    for (int r = 0; r < 4; ++r) { p[r] = __builtin_amdgcn_exp2f(fmaf(sc[t][r], C2, -mrow)); lsum += p[r]; }
    *(LAS v2u*)(L + L_P + (4 * t + (w >> 1)) * 1024 + lane * 16 + (w & 1) * 8) = v2u{pack_bf16(p[0], p[1]), pack_bf16(p[2], p[3])};
  }
  lsum = xg_sum(lsum);
  if (g == 0) *(LAS float*)(L + L_LS + (w * 16 + c) * 4) = lsum;
  wo_loads(16, 20); v_half(1, 0);

  const int ct = w & 3, kh = w >> 2;
  unsigned tra;
  {
    const int q4 = (lane & 15) >> 2, p4 = lane & 3;
    tra = (4 * g + q4) * 128 + (((2 * ct + (p4 >> 1)) ^ (((2 * g + (q4 >> 1)) & 3) << 1)) << 4) + (p4 & 1) * 8 + kh * 32768;
  }
  auto pv_pass = [&](int p) -> f32x4 {
    f32x4 a = {0.f, 0.f, 0.f, 0.f};
#pragma unroll
    for (int i = 0; i < 8; ++i) {
      const v4u pf = *(LAS v4u*)(L + L_P + (8 * kh + i) * 1024 + lane * 16);
      const int ub = L_RING + (p & 1) * 65536 + i * 4096;
      const s16x4 a0 = __builtin_amdgcn_ds_read_tr16_b64_v4i16((LAS s16x4*)(L + ub + tra));
      const s16x4 a1 = __builtin_amdgcn_ds_read_tr16_b64_v4i16((LAS s16x4*)(L + ub + 2048 + tra));
      const v2u x0 = __builtin_bit_cast(v2u, a0), x1 = __builtin_bit_cast(v2u, a1);
      a = mfma16(v4u{x0.x, x0.y, x1.x, x1.y}, pf, a);
    }
    return a;
  };
  const int bh = b * HKV + kvh;
  char* stb = uni(ws + W_ST + (size_t)bh * 1024);
  char* h1 = uni(ws + W_H1 + (size_t)bh * (16 * 4096));
  auto reduce_pass = [&](f32x4 a) -> f32x4 {
    if (kh == 1) *(LAS f32x4*)(L + L_HALF + ct * 1024 + lane * 16) = a;
    BARRIER();
    if (kh == 0) a += *(LAS f32x4*)(L + L_HALF + ct * 1024 + lane * 16);
    return a;
  };
  auto publish_pass = [&](int p, f32x4 a) {
    if (kh == 0) st128_h1(((((s + 1 + p) & 3) * 4 + s) * 4 + ct) * 1024 + lane * 16, enc4(a, T), h1);
  };
  STAMP(6);
  VMWAIT_S(8);
  BARRIER();
  STAMP(7);
  {
    const f32x4 a = reduce_pass(pv_pass(0));
    wo_loads(20, 23); v_half(1, 1);
    asm volatile("s_barrier" ::: "memory");
    wo_loads(23, 26); v_half(2, 0);
    publish_pass(0, a);
    if (w == 7) {
      float lt = *(LAS float*)(L + L_LS + c * 4);
#pragma unroll
      for (int ww = 1; ww < 8; ++ww) lt += *(LAS float*)(L + L_LS + (ww * 16 + c) * 4);
      if (g == 0) st128_dev((s * 16 + c) * 16, v4u{__float_as_uint(mrow), T, __float_as_uint(lt), T}, stb);
    }
    if (kh == 0 || w == 7) VMWAIT_S(8); else VMWAIT_S(7);
  }
  BARRIER();
  v4u stu;
  {
    const f32x4 a = reduce_pass(pv_pass(1));
    wo_loads(26, 32); v_half(2, 1);
    asm volatile("s_barrier" ::: "memory");
    v_half(3, 0);
    publish_pass(1, a);
  }
  ld128_dev(stu, lane * 16, stb);
  if (kh == 0) VMWAIT(6); else VMWAIT(5);
  asm volatile(""
               : "+v"(wv[0][0]), "+v"(wv[0][1]), "+v"(wv[0][2]), "+v"(wv[0][3]), "+v"(wv[0][4]), "+v"(wv[0][5]), "+v"(wv[0][6]), "+v"(wv[0][7]),
                 "+v"(wv[1][0]), "+v"(wv[1][1]), "+v"(wv[1][2]), "+v"(wv[1][3]), "+v"(wv[1][4]), "+v"(wv[1][5]), "+v"(wv[1][6]), "+v"(wv[1][7])
               :: "memory");
  asm volatile("" : "+v"(wv[2][0]), "+v"(wv[2][1]), "+v"(wv[2][2]), "+v"(wv[2][3]), "+v"(wv[2][4]), "+v"(wv[2][5]), "+v"(wv[2][6]), "+v"(wv[2][7]),
                    "+v"(wv[3][0]), "+v"(wv[3][1]), "+v"(wv[3][2]), "+v"(wv[3][3]), "+v"(wv[3][4]), "+v"(wv[3][5]), "+v"(wv[3][6]), "+v"(wv[3][7])
               :: "memory");
  BARRIER();
  publish_pass(2, reduce_pass(pv_pass(2)));
  v_half(3, 1);
  v4u hu[2];
  const int srcA = (s + (kh ? 3 : 1)) & 3, srcB = (s + (kh ? 3 : 2)) & 3;
  const unsigned hoA = ((s * 4 + srcA) * 4 + ct) * 1024 + lane * 16, hoB = ((s * 4 + srcB) * 4 + ct) * 1024 + lane * 16;
  ld128_dev(hu[0], hoA, h1);
  ld128_dev(hu[1], hoB, h1);
  v4u wf[4][4];
#pragma unroll
  for (int ks = 0; ks < 4; ++ks)
#pragma unroll
    for (int jj = 0; jj < 4; ++jj)
#pragma unroll
      for (int r = 0; r < 4; ++r) {
        const unsigned lo = wv[ks][2 * r][jj >> 1], hi = wv[ks][2 * r + 1][jj >> 1];
        wf[ks][jj][r] = (jj & 1) ? perm_hi(lo, hi) : perm_lo(lo, hi);
      }
  asm volatile("s_waitcnt vmcnt(6)" : "+v"(stu) :: "memory");
  for (unsigned spin = 0; !wave_all(tags_ok(stu, T)) && spin < SPIN_CAP; ++spin) reload1(stu, lane * 16, stb);
  float fS, fA, fB;
  {
    const float ms = __uint_as_float(stu.x), ls = __uint_as_float(stu.z);
    const float M = xg_max(ms);
    const float e = __builtin_amdgcn_exp2f(ms - M);
    const float fw = e / xg_sum(e * ls);
    fS = __shfl(fw, 16 * s + c); fA = __shfl(fw, 16 * srcA + c); fB = kh ? 0.f : __shfl(fw, 16 * srcB + c);
  }
  asm volatile("s_waitcnt vmcnt(0)" : "+v"(hu[0]), "+v"(hu[1]) :: "memory");
  STAMP(8);
  BARRIER();
  f32x4 own = pv_pass(3);
  STAMP(9);
  for (unsigned spin = 0; !wave_all(quad_ok(hu[0], T) && quad_ok(hu[1], T)) && spin < SPIN_CAP; ++spin) reload2(hu[0], hu[1], hoA, hoB, h1);
  STAMP(11);
  {
    own = own * fS + dec4(hu[0]) * fA + dec4(hu[1]) * fB;
    if (kh == 1) *(LAS f32x4*)(L + L_HALF + ct * 1024 + lane * 16) = own;
    BARRIER();
    if (kh == 0) {
      own += *(LAS f32x4*)(L + L_HALF + ct * 1024 + lane * 16);
      *(LAS v2u*)(L + L_OB + c * 144 + (16 * ct + 4 * g) * 2) = v2u{pack_bf16(own[0], own[1]), pack_bf16(own[2], own[3])};
    }
  }
  BARRIER();

  f32x4 pa[4];
#pragma unroll
  for (int jj = 0; jj < 4; ++jj) pa[jj] = f32x4{0.f, 0.f, 0.f, 0.f};
#pragma unroll
  for (int ks = 0; ks < 4; ++ks) {
    const v4u af = *(LAS v4u*)(L + L_OB + (8 * (ks >> 1) + (c & 7)) * 144 + (32 * (ks & 1) + 8 * g) * 2);
#pragma unroll
    for (int jj = 0; jj < 4; ++jj) pa[jj] = mfma16(af, wf[ks][jj], pa[jj]);
  }
  const int j32 = kvh * 4 + s;
  char* h2 = uni(ws + W_H2 + (size_t)b * (32 * 8 * 112 * 16));
  if (w < 7) {
#pragma unroll
    for (int rr = 0; rr < 2; ++rr) {
      const int q = 4 * (g & 1) + 2 * (g >> 1) + rr;
      const f32x4 v = (g >> 1) ? f32x4{pa[0][2 + rr], pa[1][2 + rr], pa[2][2 + rr], pa[3][2 + rr]} : f32x4{pa[0][rr], pa[1][rr], pa[2][rr], pa[3][rr]};
      st128_dev(((j32 * 8 + q) * 112 + w * 16 + c) * 16, enc4(v, T), h2);
    }
  }
  STAMP(12);
  {
    const int l31 = lane & 31, lu = l31 < 28 ? l31 : 27;
    v4u fu[2];
    unsigned fo[2];
#pragma unroll
    for (int k = 0; k < 2; ++k) {
      fo[k] = (((4 * w + 2 * k + (lane >> 5)) * 8 + (j32 >> 2)) * 112 + 28 * (j32 & 3) + lu) * 16;
      ld128_dev(fu[k], fo[k], h2);
    }
    asm volatile("s_waitcnt vmcnt(0)" : "+v"(fu[0]), "+v"(fu[1]) :: "memory");
    for (unsigned spin = 0; !wave_all(quad_ok(fu[0], T) && quad_ok(fu[1], T)) && spin < SPIN_CAP; ++spin) reload2(fu[0], fu[1], fo[0], fo[1], h2);
    STAMP(13);
    *(LAS f32x4*)(L + L_RED + w * 1024 + lane * 16) = dec4(fu[0]) + dec4(fu[1]);
  }
  BARRIER();
  if (w == 0) {
    const int l31 = lane & 31, hf = lane >> 5;
    f32x4 t = *(LAS f32x4*)(L + L_RED + (4 * hf) * 1024 + l31 * 16);
    t += *(LAS f32x4*)(L + L_RED + (4 * hf) * 1024 + 512 + l31 * 16);
#pragma unroll
    for (int ww = 1; ww < 4; ++ww) {
      t += *(LAS f32x4*)(L + L_RED + (4 * hf + ww) * 1024 + l31 * 16);
      t += *(LAS f32x4*)(L + L_RED + (4 * hf + ww) * 1024 + 512 + l31 * 16);
    }
#pragma unroll
    for (int e = 0; e < 4; ++e) {
      auto sw = __builtin_amdgcn_permlane32_swap(__float_as_uint(t[e]), __float_as_uint(t[e]), false, false);
      t[e] = __uint_as_float(sw[0]) + __uint_as_float(sw[1]);
    }
    if (lane < 28)
      *(v2u*)(out + (((b * SQ + (j32 >> 2)) * DM + 112 * (j32 & 3) + 4 * lane) >> 1)) = v2u{pack_bf16(t[0], t[1]), pack_bf16(t[2], t[3])};
    if ((b & 1) == 0 && j32 == 0 && lane == 0) st32_dev(0u, T, ws + W_EP + rb * 256);
  }
}

}

extern "C" void kernel_launch(void* const* d_in, const int* in_sizes, int n_in, void* d_out, int out_size, void* d_ws,
                              size_t ws_size, hipStream_t stream) {
  (void)in_sizes; (void)n_in; (void)out_size;
  if (ws_size < W_END) return;
  (void)hipFuncSetAttribute((const void*)attn_fused_kernel, hipFuncAttributeMaxDynamicSharedMemorySize, LDS_BYTES);
  attn_fused_kernel<<<dim3(256), dim3(512), LDS_BYTES, stream>>>((const char*)d_in[0], (const char*)d_in[1], (const char*)d_in[2],
                                                                 (const char*)d_in[3], (const char*)d_in[4], (unsigned*)d_out, (char*)d_ws);
}
```

```cpp
#define SPINT 6000
#include <hip/hip_runtime.h>
#include <hip/hip_bf16.h>

namespace {

typedef unsigned v4u __attribute__((ext_vector_type(4)));
typedef unsigned v2u __attribute__((ext_vector_type(2)));
typedef float f32x4 __attribute__((ext_vector_type(4)));
typedef float f32x2 __attribute__((ext_vector_type(2)));
typedef __bf16 bf16x8 __attribute__((ext_vector_type(8)));
typedef __bf16 bf16x2 __attribute__((ext_vector_type(2)));
typedef short s16x4 __attribute__((ext_vector_type(4)));
#define LAS __attribute__((address_space(3)))

constexpr int NB = 8, SQ = 8, DM = 448, HKV = 8, SK = 8192, WIN = 2048;
constexpr int NQ = 4096;
constexpr float C2 = 0.0625f * 1.4426950408889634f;
constexpr unsigned SPIN_CAP = 1u << 18;

constexpr int L_RING = 0;
constexpr int L_WQ = L_RING + 65536;
constexpr int L_P = 131072;
constexpr int L_X = L_P;
constexpr int L_QF = L_P + 16384;
constexpr int L_HALF = L_QF;
constexpr int L_MX = L_QF + 8192;
constexpr int L_LS = L_MX + 512;
constexpr int L_QR = L_LS + 512;
constexpr int L_OB = L_QR + 4096;
constexpr int L_RED = L_P;
constexpr int LDS_BYTES = 163840;
static_assert(L_OB + 16 * 144 <= LDS_BYTES, "LDS map");

constexpr size_t W_EP = 0;
constexpr size_t W_QX = 4096;
constexpr size_t W_ST = W_QX + (size_t)8 * 4 * 8 * 4096;
constexpr size_t W_H1 = W_ST + (size_t)64 * 4 * 256;
constexpr size_t W_H2 = W_H1 + (size_t)64 * 16 * 4096;
constexpr size_t W_END = W_H2 + (size_t)8 * 32 * 8 * 112 * 16;

__device__ __forceinline__ f32x4 mfma16(v4u a, v4u b, f32x4 c) {
  return __builtin_amdgcn_mfma_f32_16x16x32_bf16(__builtin_bit_cast(bf16x8, a), __builtin_bit_cast(bf16x8, b), c, 0, 0, 0);
}
__device__ __forceinline__ unsigned pack_bf16(float lo, float hi) {
  bf16x2 r; r[0] = (__bf16)lo; r[1] = (__bf16)hi;
  return __builtin_bit_cast(unsigned, r);
}
__device__ __forceinline__ float xg_max(float x) {
  auto s = __builtin_amdgcn_permlane16_swap(__float_as_uint(x), __float_as_uint(x), false, false);
  x = fmaxf(__uint_as_float(s[0]), __uint_as_float(s[1]));
  auto t = __builtin_amdgcn_permlane32_swap(__float_as_uint(x), __float_as_uint(x), false, false);
  return fmaxf(__uint_as_float(t[0]), __uint_as_float(t[1]));
}
__device__ __forceinline__ float xg_sum(float x) {
  auto s = __builtin_amdgcn_permlane16_swap(__float_as_uint(x), __float_as_uint(x), false, false);
  x = __uint_as_float(s[0]) + __uint_as_float(s[1]);
  auto t = __builtin_amdgcn_permlane32_swap(__float_as_uint(x), __float_as_uint(x), false, false);
  return __uint_as_float(t[0]) + __uint_as_float(t[1]);
}
__device__ __forceinline__ unsigned perm_lo(unsigned a, unsigned b) { return __builtin_amdgcn_perm(b, a, 0x05040100u); }
__device__ __forceinline__ unsigned perm_hi(unsigned a, unsigned b) { return __builtin_amdgcn_perm(b, a, 0x07060302u); }

#define KPOL " nt"
#define VPOL " nt"
#define WPOL " nt"
#define VMWAIT(N) asm volatile("s_waitcnt vmcnt(" #N ")" ::: "memory")
#define VMWAIT_S(N) VMWAIT(N)
#define LGKM0() asm volatile("s_waitcnt lgkmcnt(0)" ::: "memory")
#define BARRIER() asm volatile("s_waitcnt lgkmcnt(0)\n\ts_barrier" ::: "memory")

__device__ __forceinline__ void dma_half(const char* gbase, unsigned lds_dst, unsigned v0, unsigned v1, unsigned v2, unsigned v3) {
  unsigned keep;
  asm volatile(
      "s_mov_b32 %[keep], m0\n\ts_mov_b32 m0, %[l0]\n\ts_nop 2\n\t"
      "global_load_lds_dwordx4 %[v0], %[sb]" KPOL "\n\t"
      "global_load_lds_dwordx4 %[v1], %[sb] offset:1024" KPOL "\n\t"
      "global_load_lds_dwordx4 %[v2], %[sb] offset:2048" KPOL "\n\t"
      "global_load_lds_dwordx4 %[v3], %[sb] offset:3072" KPOL "\n\t"
      "s_mov_b32 m0, %[keep]"
      : [keep] "=&s"(keep)
      : [v0] "v"(v0), [v1] "v"(v1), [v2] "v"(v2), [v3] "v"(v3), [sb] "s"(gbase), [l0] "s"(lds_dst)
      : "memory");
}
__device__ __forceinline__ void dma1w(const char* sb, unsigned lds_dst, unsigned voff) {
  unsigned keep;
  asm volatile("s_mov_b32 %[keep], m0\n\ts_mov_b32 m0, %[l0]\n\ts_nop 2\n\tglobal_load_lds_dwordx4 %[v0], %[sb]" WPOL "\n\ts_mov_b32 m0, %[keep]"
               : [keep] "=&s"(keep) : [v0] "v"(voff), [sb] "s"(sb), [l0] "s"(lds_dst) : "memory");
}
__device__ __forceinline__ void dma1(const char* sb, unsigned lds_dst, unsigned voff) {
  unsigned keep;
  asm volatile("s_mov_b32 %[keep], m0\n\ts_mov_b32 m0, %[l0]\n\ts_nop 2\n\tglobal_load_lds_dwordx4 %[v0], %[sb]" VPOL "\n\ts_mov_b32 m0, %[keep]"
               : [keep] "=&s"(keep) : [v0] "v"(voff), [sb] "s"(sb), [l0] "s"(lds_dst) : "memory");
}
__device__ __forceinline__ void ld64(v2u& d, unsigned voff, const char* sb) {
  asm volatile("global_load_dwordx2 %0, %1, %2" WPOL : "=v"(d) : "v"(voff), "s"(sb) : "memory");
}
__device__ __forceinline__ void ld128_dev(v4u& d, unsigned voff, const char* sb) {
  asm volatile("global_load_dwordx4 %0, %1, %2 sc1" : "=v"(d) : "v"(voff), "s"(sb) : "memory");
}
__device__ __forceinline__ void ld32_dev(unsigned& d, unsigned voff, const char* sb) {
  asm volatile("global_load_dword %0, %1, %2 sc1" : "=v"(d) : "v"(voff), "s"(sb) : "memory");
}
__device__ __forceinline__ void st128_dev(unsigned voff, v4u v, char* sb) {
  asm volatile("global_store_dwordx4 %0, %1, %2 sc1\n\ts_nop 1" : : "v"(voff), "v"(v), "s"(sb) : "memory");
}
#define H1POL " sc1"
__device__ __forceinline__ void st128_h1(unsigned voff, v4u v, char* sb) {
  asm volatile("global_store_dwordx4 %0, %1, %2" H1POL "\n\ts_nop 1" : : "v"(voff), "v"(v), "s"(sb) : "memory");
}
__device__ __forceinline__ void st32_dev(unsigned voff, unsigned v, char* sb) {
  asm volatile("global_store_dword %0, %1, %2 sc1" : : "v"(voff), "v"(v), "s"(sb) : "memory");
}
__device__ __forceinline__ bool tags_ok(v4u u, unsigned T) { return u.y == T && u.w == T; }
constexpr unsigned TMASK = 0x3FFu;
__device__ __forceinline__ unsigned enc(float v, unsigned T) { return ((__float_as_uint(v) + 0x200u) & ~TMASK) | (T & TMASK); }
__device__ __forceinline__ float dec(unsigned u) { return __uint_as_float(u & ~TMASK); }
__device__ __forceinline__ v4u enc4(f32x4 a, unsigned T) { return v4u{enc(a[0], T), enc(a[1], T), enc(a[2], T), enc(a[3], T)}; }
__device__ __forceinline__ f32x4 dec4(v4u u) { return f32x4{dec(u.x), dec(u.y), dec(u.z), dec(u.w)}; }
__device__ __forceinline__ bool quad_ok(v4u u, unsigned T) { return (((u.x ^ T) | (u.y ^ T) | (u.z ^ T) | (u.w ^ T)) & TMASK) == 0u; }
template <class P> __device__ __forceinline__ P* uni(P* p) {
  const unsigned long long a = (unsigned long long)p;
  const unsigned lo = __builtin_amdgcn_readfirstlane((unsigned)a), hi = __builtin_amdgcn_readfirstlane((unsigned)(a >> 32));
  return (P*)(((unsigned long long)hi << 32) | lo);
}
__device__ __forceinline__ bool wave_all(bool ok) { return __builtin_amdgcn_ballot_w64(!ok) == 0ull; }
__device__ __forceinline__ void reload1(v4u& d0, unsigned o0, const char* sb) {
  asm volatile("global_load_dwordx4 %0, %1, %2 sc1\n\ts_waitcnt vmcnt(0)" : "=&v"(d0) : "v"(o0), "s"(sb) : "memory");
}
__device__ __forceinline__ void reload2(v4u& d0, v4u& d1, unsigned o0, unsigned o1, const char* sb) {
  asm volatile("global_load_dwordx4 %0, %2, %4 sc1\n\tglobal_load_dwordx4 %1, %3, %4 sc1\n\ts_waitcnt vmcnt(0)"
               : "=&v"(d0), "=&v"(d1) : "v"(o0), "v"(o1), "s"(sb) : "memory");
}

#define STAMP(i) do { } while (0)

__global__ void __launch_bounds__(512, 2)
attn_fused_kernel(const char* __restrict__ x, const char* __restrict__ kc, const char* __restrict__ vc,
                  const char* __restrict__ wq, const char* __restrict__ wo, unsigned* __restrict__ out, char* __restrict__ ws) {
  extern __shared__ __attribute__((aligned(16))) unsigned char lds_raw[];
  LAS unsigned char* L = (LAS unsigned char*)lds_raw;
  const unsigned lbase = (unsigned)(size_t)L;
  STAMP(0);
  const unsigned long long tbeg = __builtin_amdgcn_s_memrealtime();
  const int tid = threadIdx.x, lane = tid & 63, w = __builtin_amdgcn_readfirstlane(tid >> 6);
  const int g = lane >> 4, c = lane & 15;
  const int bid = blockIdx.x, kvh = bid & 7, s = (bid >> 3) & 3, b = bid >> 5;
  const int rot = 8 * s + b;
  const int rb = b >> 1, cb = (b & 1) * 4 + s;
  asm volatile("" :: "s"(x), "s"(kc), "s"(vc), "s"(wq), "s"(wo), "s"(out), "s"(ws));

  unsigned ep;
  ld32_dev(ep, 0u, ws + W_EP + rb * 256);
  {
    const unsigned r8 = lane >> 3, f = ((r8 >> 1) & 1) | ((w & 1) << 1);
    const unsigned vo = r8 * (NQ * 2) + (((lane & 7) ^ (f << 1)) << 4);
    const char* wqb = wq + (size_t)(8 * w) * (NQ * 2) + (kvh * 512 + cb * 64) * 2;
#pragma unroll
    for (int i = 0; i < 7; ++i) dma1w(wqb + (size_t)(64 * i) * (NQ * 2), lbase + L_WQ + (8 * i + w) * 1024, vo);
    const char* xb = x + (size_t)(16 * rb) * (DM * 2);
#pragma unroll
    for (int i = 0; i < 2; ++i) {
      const int m = 8 * i + w;
      if (m < 14) {
        const unsigned p = 64 * m + lane, row = (p * 1171u) >> 16, ch = p - 56 * row;
        dma1w(xb, lbase + L_X + m * 1024, row * (DM * 2) + (((ch & ~7u) | ((ch & 7u) ^ ((row >> 1) & 7u))) << 4));
      }
    }
  }
  asm volatile("s_barrier" ::: "memory");
  const size_t headrow = (size_t)(b * HKV + kvh) * SK + (SK - WIN) + s * 512;
  const char* kwin = kc + headrow * 512;
  const char* vwin = vc + headrow * 512;
  unsigned ko[8];
  {
    const unsigned hi = lane >> 5, l31 = lane & 31;
    const unsigned laneK = (hi << 9) | ((l31 ^ hi) << 4);
#pragma unroll
    for (int q = 0; q < 8; ++q) ko[q] = (laneK ^ (unsigned)(q << 5)) + ((q >> 2) << 12);
  }
  auto tile_off = [&](int unit) -> size_t { return (size_t)((unit + rot) & 31) * 8192; };
  auto k_half = [&](int t, int h) {
    dma_half(kwin + tile_off(8 * t + w), lbase + L_RING + ((t & 1) * 8 + w) * 8192 + h * 4096, ko[4 * h], ko[4 * h + 1], ko[4 * h + 2], ko[4 * h + 3]);
  };
  k_half(0, 0);
  asm volatile("s_waitcnt vmcnt(4)" : "+v"(ep) :: "memory");
  const unsigned T = __builtin_amdgcn_readfirstlane(ep) + 1u;
  STAMP(1);
  BARRIER();
  char* qx = uni(ws + W_QX + (size_t)(kvh * 4 + rb) * (8 * 4096));
  {
    const int nt = w & 3, kh = w >> 2;
    const int q4 = (lane & 15) >> 2, p4 = lane & 3;
    const unsigned fa = (unsigned)(q4 >> 1) | ((unsigned)(g & 1) << 1);
    const unsigned ta = L_WQ + (224 * kh + 8 * g + q4) * 128 + (((2 * nt + (p4 >> 1)) ^ (fa << 1)) << 4) + (p4 & 1) * 8;
    f32x4 qa = {0.f, 0.f, 0.f, 0.f};
#pragma unroll
    for (int i = 0; i < 7; ++i) {
      const s16x4 a0 = __builtin_amdgcn_ds_read_tr16_b64_v4i16((LAS s16x4*)(L + ta + i * 4096));
      const s16x4 a1 = __builtin_amdgcn_ds_read_tr16_b64_v4i16((LAS s16x4*)(L + ta + i * 4096 + 512));
      const unsigned ch = 4 * (7 * kh + i) + g;
      const v4u xb = *(LAS v4u*)(L + L_X + c * 896 + (((ch & ~7u) | ((ch & 7u) ^ ((c >> 1) & 7u))) << 4));
      const v2u x0 = __builtin_bit_cast(v2u, a0), x1 = __builtin_bit_cast(v2u, a1);
      qa = mfma16(v4u{x0.x, x0.y, x1.x, x1.y}, xb, qa);
    }
    if (kh == 1) *(LAS f32x4*)(L + L_QR + nt * 1024 + lane * 16) = qa;
    STAMP(17);
    BARRIER();
    if (kh == 0) {
      qa += *(LAS f32x4*)(L + L_QR + nt * 1024 + lane * 16);
      st128_dev(cb * 4096 + nt * 1024 + lane * 16, v4u{pack_bf16(qa[0], qa[1]), T, pack_bf16(qa[2], qa[3]), T}, qx);
    }
  }
  STAMP(18);
  k_half(0, 1);
  v4u qu[2];
  const unsigned qgo = w * 4096 + (lane >> 5) * 1024 + (16 * ((lane >> 3) & 3) + 8 * (b & 1) + (lane & 7)) * 16;
  ld128_dev(qu[0], qgo, qx);
  ld128_dev(qu[1], qgo + 2048, qx);
  STAMP(19);
  k_half(1, 0);
  STAMP(2);
  asm volatile("s_waitcnt vmcnt(4)" : "+v"(qu[0]), "+v"(qu[1]) :: "memory");
  STAMP(3);
  for (unsigned spin = 0; !wave_all(tags_ok(qu[0], T) && tags_ok(qu[1], T)) && spin < SPIN_CAP; ++spin) reload2(qu[0], qu[1], qgo, qgo + 2048, qx);
  {
    const int gq = (lane >> 3) & 3, q = lane & 7, h = w >> 2;
#pragma unroll
    for (int u = 0; u < 2; ++u) {
      const int nt = 2 * u + (lane >> 5);
      const int st = (w & 3) * 2 + (nt >> 1), gk = 2 * (nt & 1) + (gq >> 1);
      *(LAS v2u*)(L + L_QF + st * 1024 + (16 * gk + 8 * h + q) * 16 + (gq & 1) * 8) = v2u{qu[u].x, qu[u].z};
    }
  }
  STAMP(4);
  k_half(1, 1);
  BARRIER();
  v4u qf[8];
#pragma unroll
  for (int st = 0; st < 8; ++st) qf[st] = *(LAS v4u*)(L + L_QF + st * 1024 + lane * 16);

  f32x4 sc[4];
  auto qk_tile = [&](int slot) -> f32x4 {
    v4u kf[8];
#pragma unroll
    for (int st = 0; st < 8; ++st) kf[st] = *(LAS v4u*)(L + L_RING + slot * 8192 + c * 512 + (((4 * st + g) ^ c) << 4));
    f32x4 a = {0.f, 0.f, 0.f, 0.f};
#pragma unroll
    for (int st = 0; st < 8; ++st) a = mfma16(kf[st], qf[st], a);
    return a;
  };
  const unsigned vlane = (lane >> 3) * 512 + (((lane & 7) ^ (((lane >> 4) & 3) << 1)) << 4);
  auto v_piece = [&](int p, int q) {
    dma1(vwin + tile_off(4 * w + (q >> 1)) + (q & 1) * 4096 + ((s + 1 + p) & 3) * 128,
         lbase + L_RING + ((p & 1) * 8 + w) * 8192 + q * 1024, vlane);
  };
  const int wc = w < 7 ? w : 6;
  v2u wv[4][8];
  const char* wob = wo + (size_t)(2 * kvh * 256 + 64 * s) * (DM * 2) + wc * 128;
  const unsigned wvo = (unsigned)(8 * g) * (DM * 2) + c * 8;
#define WO_N(a, b) (b)
  auto wo_load = [&](int ks, int i) { ld64(wv[ks][i], wvo + (unsigned)(256 * (ks >> 1) + 32 * (ks & 1) + i) * (DM * 2), wob); };

  auto wo_loads = [&](int n0, int n1) {
#pragma unroll
    for (int n = n0; n < n1; ++n) wo_load(n >> 3, n & 7);
  };
  auto v_half = [&](int p, int h) {
#pragma unroll
    for (int q = 4 * h; q < 4 * h + 4; ++q) v_piece(p, q);
  };
  sc[0] = qk_tile(w);
  LGKM0();
  wo_loads(0, 4); k_half(2, 0);
  VMWAIT_S(8);
  sc[1] = qk_tile(8 + w);
  LGKM0();
  wo_loads(4, 7); k_half(2, 1); wo_loads(7, 10); k_half(3, 0);
  VMWAIT_S(7);
  sc[2] = qk_tile(w);
  LGKM0();
  wo_loads(10, 13); k_half(3, 1); wo_loads(13, 16); v_half(0, 0);
  VMWAIT_S(7);
  sc[3] = qk_tile(8 + w);
  LGKM0();
  v_half(0, 1);
  STAMP(5);
  float mx = fmaxf(fmaxf(fmaxf(sc[0][0], sc[0][1]), fmaxf(sc[0][2], sc[0][3])), fmaxf(fmaxf(sc[1][0], sc[1][1]), fmaxf(sc[1][2], sc[1][3])));
  mx = fmaxf(mx, fmaxf(fmaxf(fmaxf(sc[2][0], sc[2][1]), fmaxf(sc[2][2], sc[2][3])), fmaxf(fmaxf(sc[3][0], sc[3][1]), fmaxf(sc[3][2], sc[3][3]))));
  mx = xg_max(mx);
  if (g == 0) *(LAS float*)(L + L_MX + (w * 16 + c) * 4) = mx;
  BARRIER();
  float mrow = *(LAS float*)(L + L_MX + c * 4);
#pragma unroll
  for (int ww = 1; ww < 8; ++ww) mrow = fmaxf(mrow, *(LAS float*)(L + L_MX + (ww * 16 + c) * 4));
  mrow *= C2;
  float lsum = 0.f;
#pragma unroll
  for (int t = 0; t < 4; ++t) {
    float p[4];
#pragma unroll
    for (int r = 0; r < 4; ++r) { p[r] = __builtin_amdgcn_exp2f(fmaf(sc[t][r], C2, -mrow)); lsum += p[r]; }
    *(LAS v2u*)(L + L_P + (4 * t + (w >> 1)) * 1024 + lane * 16 + (w & 1) * 8) = v2u{pack_bf16(p[0], p[1]), pack_bf16(p[2], p[3])};
  }
  lsum = xg_sum(lsum);
  if (g == 0) *(LAS float*)(L + L_LS + (w * 16 + c) * 4) = lsum;
  wo_loads(16, 20); v_half(1, 0);

  const int ct = w & 3, kh = w >> 2;
  unsigned tra;
  {
    const int q4 = (lane & 15) >> 2, p4 = lane & 3;
    tra = (4 * g + q4) * 128 + (((2 * ct + (p4 >> 1)) ^ (((2 * g + (q4 >> 1)) & 3) << 1)) << 4) + (p4 & 1) * 8 + kh * 32768;
  }
  auto pv_pass = [&](int p) -> f32x4 {
    f32x4 a = {0.f, 0.f, 0.f, 0.f};
#pragma unroll
    for (int i = 0; i < 8; ++i) {
      const v4u pf = *(LAS v4u*)(L + L_P + (8 * kh + i) * 1024 + lane * 16);
      const int ub = L_RING + (p & 1) * 65536 + i * 4096;
      const s16x4 a0 = __builtin_amdgcn_ds_read_tr16_b64_v4i16((LAS s16x4*)(L + ub + tra));
      const s16x4 a1 = __builtin_amdgcn_ds_read_tr16_b64_v4i16((LAS s16x4*)(L + ub + 2048 + tra));
      const v2u x0 = __builtin_bit_cast(v2u, a0), x1 = __builtin_bit_cast(v2u, a1);
      a = mfma16(v4u{x0.x, x0.y, x1.x, x1.y}, pf, a);
    }
    return a;
  };
  const int bh = b * HKV + kvh;
  char* stb = uni(ws + W_ST + (size_t)bh * 1024);
  char* h1 = uni(ws + W_H1 + (size_t)bh * (16 * 4096));
  auto reduce_pass = [&](f32x4 a) -> f32x4 {
    if (kh == 1) *(LAS f32x4*)(L + L_HALF + ct * 1024 + lane * 16) = a;
    BARRIER();
    if (kh == 0) a += *(LAS f32x4*)(L + L_HALF + ct * 1024 + lane * 16);
    return a;
  };
  auto publish_pass = [&](int p, f32x4 a) {
    if (kh == 0) st128_h1(((((s + 1 + p) & 3) * 4 + s) * 4 + ct) * 1024 + lane * 16, enc4(a, T), h1);
  };
  STAMP(6);
  VMWAIT_S(8);
  BARRIER();
  STAMP(7);
  {
    const f32x4 a = reduce_pass(pv_pass(0));
    wo_loads(20, 23); v_half(1, 1);
    asm volatile("s_barrier" ::: "memory");
    wo_loads(23, 26); v_half(2, 0);
    publish_pass(0, a);
    if (w == 7) {
      float lt = *(LAS float*)(L + L_LS + c * 4);
#pragma unroll
      for (int ww = 1; ww < 8; ++ww) lt += *(LAS float*)(L + L_LS + (ww * 16 + c) * 4);
      if (g == 0) st128_dev((s * 16 + c) * 16, v4u{__float_as_uint(mrow), T, __float_as_uint(lt), T}, stb);
    }
    if (kh == 0 || w == 7) VMWAIT_S(8); else VMWAIT_S(7);
  }
  BARRIER();
  v4u stu;
  {
    const f32x4 a = reduce_pass(pv_pass(1));
    wo_loads(26, 32); v_half(2, 1);
    asm volatile("s_barrier" ::: "memory");
    v_half(3, 0);
    publish_pass(1, a);
  }
  ld128_dev(stu, lane * 16, stb);
  if (kh == 0) VMWAIT(6); else VMWAIT(5);
  asm volatile(""
               : "+v"(wv[0][0]), "+v"(wv[0][1]), "+v"(wv[0][2]), "+v"(wv[0][3]), "+v"(wv[0][4]), "+v"(wv[0][5]), "+v"(wv[0][6]), "+v"(wv[0][7]),
                 "+v"(wv[1][0]), "+v"(wv[1][1]), "+v"(wv[1][2]), "+v"(wv[1][3]), "+v"(wv[1][4]), "+v"(wv[1][5]), "+v"(wv[1][6]), "+v"(wv[1][7])
               :: "memory");
  asm volatile("" : "+v"(wv[2][0]), "+v"(wv[2][1]), "+v"(wv[2][2]), "+v"(wv[2][3]), "+v"(wv[2][4]), "+v"(wv[2][5]), "+v"(wv[2][6]), "+v"(wv[2][7]),
                    "+v"(wv[3][0]), "+v"(wv[3][1]), "+v"(wv[3][2]), "+v"(wv[3][3]), "+v"(wv[3][4]), "+v"(wv[3][5]), "+v"(wv[3][6]), "+v"(wv[3][7])
               :: "memory");
  BARRIER();
  publish_pass(2, reduce_pass(pv_pass(2)));
  v_half(3, 1);
  v4u hu[2];
  const int srcA = (s + (kh ? 3 : 1)) & 3, srcB = (s + (kh ? 3 : 2)) & 3;
  const unsigned hoA = ((s * 4 + srcA) * 4 + ct) * 1024 + lane * 16, hoB = ((s * 4 + srcB) * 4 + ct) * 1024 + lane * 16;
  ld128_dev(hu[0], hoA, h1);
  ld128_dev(hu[1], hoB, h1);
  v4u wf[4][4];
#pragma unroll
  for (int ks = 0; ks < 4; ++ks)
#pragma unroll
    for (int jj = 0; jj < 4; ++jj)
#pragma unroll
      for (int r = 0; r < 4; ++r) {
        const unsigned lo = wv[ks][2 * r][jj >> 1], hi = wv[ks][2 * r + 1][jj >> 1];
        wf[ks][jj][r] = (jj & 1) ? perm_hi(lo, hi) : perm_lo(lo, hi);
      }
  asm volatile("s_waitcnt vmcnt(6)" : "+v"(stu) :: "memory");
  for (unsigned spin = 0; !wave_all(tags_ok(stu, T)) && spin < SPIN_CAP; ++spin) reload1(stu, lane * 16, stb);
  float fS, fA, fB;
  {
    const float ms = __uint_as_float(stu.x), ls = __uint_as_float(stu.z);
    const float M = xg_max(ms);
    const float e = __builtin_amdgcn_exp2f(ms - M);
    const float fw = e / xg_sum(e * ls);
    fS = __shfl(fw, 16 * s + c); fA = __shfl(fw, 16 * srcA + c); fB = kh ? 0.f : __shfl(fw, 16 * srcB + c);
  }
  asm volatile("s_waitcnt vmcnt(0)" : "+v"(hu[0]), "+v"(hu[1]) :: "memory");
  STAMP(8);
  BARRIER();
  f32x4 own = pv_pass(3);
  STAMP(9);
  for (unsigned spin = 0; !wave_all(quad_ok(hu[0], T) && quad_ok(hu[1], T)) && spin < SPIN_CAP; ++spin) reload2(hu[0], hu[1], hoA, hoB, h1);
  STAMP(11);
  {
    own = own * fS + dec4(hu[0]) * fA + dec4(hu[1]) * fB;
    if (kh == 1) *(LAS f32x4*)(L + L_HALF + ct * 1024 + lane * 16) = own;
    BARRIER();
    if (kh == 0) {
      own += *(LAS f32x4*)(L + L_HALF + ct * 1024 + lane * 16);
      *(LAS v2u*)(L + L_OB + c * 144 + (16 * ct + 4 * g) * 2) = v2u{pack_bf16(own[0], own[1]), pack_bf16(own[2], own[3])};
    }
  }
  BARRIER();

  f32x4 pa[4];
#pragma unroll
  for (int jj = 0; jj < 4; ++jj) pa[jj] = f32x4{0.f, 0.f, 0.f, 0.f};
#pragma unroll
  for (int ks = 0; ks < 4; ++ks) {
    const v4u af = *(LAS v4u*)(L + L_OB + (8 * (ks >> 1) + (c & 7)) * 144 + (32 * (ks & 1) + 8 * g) * 2);
#pragma unroll
    for (int jj = 0; jj < 4; ++jj) pa[jj] = mfma16(af, wf[ks][jj], pa[jj]);
  }
  const int j32 = kvh * 4 + s;
  char* h2 = uni(ws + W_H2 + (size_t)b * (32 * 8 * 112 * 16));
  if (w < 7) {
#pragma unroll
    for (int rr = 0; rr < 2; ++rr) {
      const int q = 4 * (g & 1) + 2 * (g >> 1) + rr;
      const f32x4 v = (g >> 1) ? f32x4{pa[0][2 + rr], pa[1][2 + rr], pa[2][2 + rr], pa[3][2 + rr]} : f32x4{pa[0][rr], pa[1][rr], pa[2][rr], pa[3][rr]};
      st128_dev(((j32 * 8 + q) * 112 + w * 16 + c) * 16, enc4(v, T), h2);
    }
  }
  STAMP(12);
  {
    const int l31 = lane & 31, lu = l31 < 28 ? l31 : 27;
    v4u fu[2];
    unsigned fo[2];
#pragma unroll
    for (int k = 0; k < 2; ++k) {
      fo[k] = (((4 * w + 2 * k + (lane >> 5)) * 8 + (j32 >> 2)) * 112 + 28 * (j32 & 3) + lu) * 16;
      ld128_dev(fu[k], fo[k], h2);
    }
    asm volatile("s_waitcnt vmcnt(0)" : "+v"(fu[0]), "+v"(fu[1]) :: "memory");
    for (unsigned spin = 0; !wave_all(quad_ok(fu[0], T) && quad_ok(fu[1], T)) && spin < SPIN_CAP; ++spin) reload2(fu[0], fu[1], fo[0], fo[1], h2);
    STAMP(13);
    *(LAS f32x4*)(L + L_RED + w * 1024 + lane * 16) = dec4(fu[0]) + dec4(fu[1]);
  }
  BARRIER();
  if (w == 0) {
    const int l31 = lane & 31, hf = lane >> 5;
    f32x4 t = *(LAS f32x4*)(L + L_RED + (4 * hf) * 1024 + l31 * 16);
    t += *(LAS f32x4*)(L + L_RED + (4 * hf) * 1024 + 512 + l31 * 16);
#pragma unroll
    for (int ww = 1; ww < 4; ++ww) {
      t += *(LAS f32x4*)(L + L_RED + (4 * hf + ww) * 1024 + l31 * 16);
      t += *(LAS f32x4*)(L + L_RED + (4 * hf + ww) * 1024 + 512 + l31 * 16);
    }
#pragma unroll
    for (int e = 0; e < 4; ++e) {
      auto sw = __builtin_amdgcn_permlane32_swap(__float_as_uint(t[e]), __float_as_uint(t[e]), false, false);
      t[e] = __uint_as_float(sw[0]) + __uint_as_float(sw[1]);
    }
    if (lane < 28)
      *(v2u*)(out + (((b * SQ + (j32 >> 2)) * DM + 112 * (j32 & 3) + 4 * lane) >> 1)) = v2u{pack_bf16(t[0], t[1]), pack_bf16(t[2], t[3])};
    if ((b & 1) == 0 && j32 == 0 && lane == 0) st32_dev(0u, T, ws + W_EP + rb * 256);
  }
  while ((bid == 0) && __builtin_amdgcn_s_memrealtime() - tbeg < SPINT) __builtin_amdgcn_s_sleep(4);
}

}

extern "C" void kernel_launch(void* const* d_in, const int* in_sizes, int n_in, void* d_out, int out_size, void* d_ws,
                              size_t ws_size, hipStream_t stream) {
  (void)in_sizes; (void)n_in; (void)out_size;
  if (ws_size < W_END) return;
  (void)hipFuncSetAttribute((const void*)attn_fused_kernel, hipFuncAttributeMaxDynamicSharedMemorySize, LDS_BYTES);
  attn_fused_kernel<<<dim3(256), dim3(512), LDS_BYTES, stream>>>((const char*)d_in[0], (const char*)d_in[1], (const char*)d_in[2],
                                                                 (const char*)d_in[3], (const char*)d_in[4], (unsigned*)d_out, (char*)d_ws);
}
```

```cpp
#include <hip/hip_runtime.h>
#include <hip/hip_bf16.h>

namespace {

typedef unsigned v4u __attribute__((ext_vector_type(4)));
typedef unsigned v2u __attribute__((ext_vector_type(2)));
typedef float f32x4 __attribute__((ext_vector_type(4)));
typedef float f32x2 __attribute__((ext_vector_type(2)));
typedef __bf16 bf16x8 __attribute__((ext_vector_type(8)));
typedef __bf16 bf16x2 __attribute__((ext_vector_type(2)));
typedef short s16x4 __attribute__((ext_vector_type(4)));
#define LAS __attribute__((address_space(3)))

constexpr int NB = 8, SQ = 8, DM = 448, HKV = 8, SK = 8192, WIN = 2048;
constexpr int NQ = 4096;
constexpr float C2 = 0.0625f * 1.4426950408889634f;
constexpr unsigned SPIN_CAP = 1u << 18;

constexpr int L_RING = 0;
constexpr int L_WQ = L_RING + 65536;
constexpr int L_P = 131072;
constexpr int L_X = L_P;
constexpr int L_QF = L_P + 16384;
constexpr int L_HALF = L_QF;
constexpr int L_MX = L_QF + 8192;
constexpr int L_LS = L_MX + 512;
constexpr int L_QR = L_LS + 512;
constexpr int L_FLAG = L_QR + 4096;
constexpr int L_OB = L_FLAG + 16;
constexpr int L_RED = L_P;
constexpr int LDS_BYTES = 163840;
static_assert(L_OB + 16 * 144 <= LDS_BYTES, "LDS map");

constexpr size_t W_EP = 0;
constexpr size_t W_QX = 4096;
constexpr size_t W_ST = W_QX + (size_t)8 * 4 * 8 * 4096;
constexpr size_t W_H1 = W_ST + (size_t)64 * 4 * 256;
constexpr size_t W_H2 = W_H1 + (size_t)64 * 16 * 4096;
constexpr size_t W_END = W_H2 + (size_t)8 * 32 * 8 * 112 * 16;

__device__ __forceinline__ f32x4 mfma16(v4u a, v4u b, f32x4 c) {
  return __builtin_amdgcn_mfma_f32_16x16x32_bf16(__builtin_bit_cast(bf16x8, a), __builtin_bit_cast(bf16x8, b), c, 0, 0, 0);
}
__device__ __forceinline__ unsigned pack_bf16(float lo, float hi) {
  bf16x2 r; r[0] = (__bf16)lo; r[1] = (__bf16)hi;
  return __builtin_bit_cast(unsigned, r);
}
__device__ __forceinline__ float xg_max(float x) {
  auto s = __builtin_amdgcn_permlane16_swap(__float_as_uint(x), __float_as_uint(x), false, false);
  x = fmaxf(__uint_as_float(s[0]), __uint_as_float(s[1]));
  auto t = __builtin_amdgcn_permlane32_swap(__float_as_uint(x), __float_as_uint(x), false, false);
  return fmaxf(__uint_as_float(t[0]), __uint_as_float(t[1]));
}
__device__ __forceinline__ float xg_sum(float x) {
  auto s = __builtin_amdgcn_permlane16_swap(__float_as_uint(x), __float_as_uint(x), false, false);
  x = __uint_as_float(s[0]) + __uint_as_float(s[1]);
  auto t = __builtin_amdgcn_permlane32_swap(__float_as_uint(x), __float_as_uint(x), false, false);
  return __uint_as_float(t[0]) + __uint_as_float(t[1]);
}
__device__ __forceinline__ unsigned perm_lo(unsigned a, unsigned b) { return __builtin_amdgcn_perm(b, a, 0x05040100u); }
__device__ __forceinline__ unsigned perm_hi(unsigned a, unsigned b) { return __builtin_amdgcn_perm(b, a, 0x07060302u); }

#define KPOL " nt"
#define VPOL " nt"
#define WPOL ""
#define VMWAIT(N) asm volatile("s_waitcnt vmcnt(" #N ")" ::: "memory")
#define VMWAIT_S(N) VMWAIT(N)
#define LGKM0() asm volatile("s_waitcnt lgkmcnt(0)" ::: "memory")
#define BARRIER() asm volatile("s_waitcnt lgkmcnt(0)\n\ts_barrier" ::: "memory")

__device__ __forceinline__ void dma_half(const char* gbase, unsigned lds_dst, unsigned v0, unsigned v1, unsigned v2, unsigned v3) {
  unsigned keep;
  asm volatile(
      "s_mov_b32 %[keep], m0\n\ts_mov_b32 m0, %[l0]\n\ts_nop 2\n\t"
      "global_load_lds_dwordx4 %[v0], %[sb]" KPOL "\n\t"
      "global_load_lds_dwordx4 %[v1], %[sb] offset:1024" KPOL "\n\t"
      "global_load_lds_dwordx4 %[v2], %[sb] offset:2048" KPOL "\n\t"
      "global_load_lds_dwordx4 %[v3], %[sb] offset:3072" KPOL "\n\t"
      "s_mov_b32 m0, %[keep]"
      : [keep] "=&s"(keep)
      : [v0] "v"(v0), [v1] "v"(v1), [v2] "v"(v2), [v3] "v"(v3), [sb] "s"(gbase), [l0] "s"(lds_dst)
      : "memory");
}
__device__ __forceinline__ void dma1w(const char* sb, unsigned lds_dst, unsigned voff) {
  unsigned keep;
  asm volatile("s_mov_b32 %[keep], m0\n\ts_mov_b32 m0, %[l0]\n\ts_nop 2\n\tglobal_load_lds_dwordx4 %[v0], %[sb]" WPOL "\n\ts_mov_b32 m0, %[keep]"
               : [keep] "=&s"(keep) : [v0] "v"(voff), [sb] "s"(sb), [l0] "s"(lds_dst) : "memory");
}
__device__ __forceinline__ void dma1(const char* sb, unsigned lds_dst, unsigned voff) {
  unsigned keep;
  asm volatile("s_mov_b32 %[keep], m0\n\ts_mov_b32 m0, %[l0]\n\ts_nop 2\n\tglobal_load_lds_dwordx4 %[v0], %[sb]" VPOL "\n\ts_mov_b32 m0, %[keep]"
               : [keep] "=&s"(keep) : [v0] "v"(voff), [sb] "s"(sb), [l0] "s"(lds_dst) : "memory");
}
__device__ __forceinline__ void ld64(v2u& d, unsigned voff, const char* sb) {
  asm volatile("global_load_dwordx2 %0, %1, %2" WPOL : "=v"(d) : "v"(voff), "s"(sb) : "memory");
}
__device__ __forceinline__ void ld128_dev(v4u& d, unsigned voff, const char* sb) {
  asm volatile("global_load_dwordx4 %0, %1, %2 sc1" : "=v"(d) : "v"(voff), "s"(sb) : "memory");
}
__device__ __forceinline__ void ld32_dev(unsigned& d, unsigned voff, const char* sb) {
  asm volatile("global_load_dword %0, %1, %2 sc1" : "=v"(d) : "v"(voff), "s"(sb) : "memory");
}
__device__ __forceinline__ void st128_dev(unsigned voff, v4u v, char* sb) {
  asm volatile("global_store_dwordx4 %0, %1, %2 sc1\n\ts_nop 1" : : "v"(voff), "v"(v), "s"(sb) : "memory");
}
#define H1POL " sc1"
__device__ __forceinline__ void st128_h1(unsigned voff, v4u v, char* sb) {
  asm volatile("global_store_dwordx4 %0, %1, %2" H1POL "\n\ts_nop 1" : : "v"(voff), "v"(v), "s"(sb) : "memory");
}
__device__ __forceinline__ void st32_dev(unsigned voff, unsigned v, char* sb) {
  asm volatile("global_store_dword %0, %1, %2 sc1" : : "v"(voff), "v"(v), "s"(sb) : "memory");
}
__device__ __forceinline__ bool tags_ok(v4u u, unsigned T) { return u.y == T && u.w == T; }
constexpr unsigned TMASK = 0x3FFu;
__device__ __forceinline__ unsigned enc(float v, unsigned T) { return ((__float_as_uint(v) + 0x200u) & ~TMASK) | (T & TMASK); }
__device__ __forceinline__ float dec(unsigned u) { return __uint_as_float(u & ~TMASK); }
__device__ __forceinline__ v4u enc4(f32x4 a, unsigned T) { return v4u{enc(a[0], T), enc(a[1], T), enc(a[2], T), enc(a[3], T)}; }
__device__ __forceinline__ f32x4 dec4(v4u u) { return f32x4{dec(u.x), dec(u.y), dec(u.z), dec(u.w)}; }
__device__ __forceinline__ bool quad_ok(v4u u, unsigned T) { return (((u.x ^ T) | (u.y ^ T) | (u.z ^ T) | (u.w ^ T)) & TMASK) == 0u; }
template <class P> __device__ __forceinline__ P* uni(P* p) {
  const unsigned long long a = (unsigned long long)p;
  const unsigned lo = __builtin_amdgcn_readfirstlane((unsigned)a), hi = __builtin_amdgcn_readfirstlane((unsigned)(a >> 32));
  return (P*)(((unsigned long long)hi << 32) | lo);
}
__device__ __forceinline__ bool wave_all(bool ok) { return __builtin_amdgcn_ballot_w64(!ok) == 0ull; }
__device__ __forceinline__ void reload1(v4u& d0, unsigned o0, const char* sb) {
  asm volatile("global_load_dwordx4 %0, %1, %2 sc1\n\ts_waitcnt vmcnt(0)" : "=&v"(d0) : "v"(o0), "s"(sb) : "memory");
}
__device__ __forceinline__ void reload2(v4u& d0, v4u& d1, unsigned o0, unsigned o1, const char* sb) {
  asm volatile("global_load_dwordx4 %0, %2, %4 sc1\n\tglobal_load_dwordx4 %1, %3, %4 sc1\n\ts_waitcnt vmcnt(0)"
               : "=&v"(d0), "=&v"(d1) : "v"(o0), "v"(o1), "s"(sb) : "memory");
}

__device__ __forceinline__ void reload4(v4u& d0, v4u& d1, v4u& d2, v4u& d3, unsigned o0, unsigned o1, unsigned o2, unsigned o3, const char* sb) {
  asm volatile("global_load_dwordx4 %0, %4, %8 sc1\n\tglobal_load_dwordx4 %1, %5, %8 sc1\n\t"
               "global_load_dwordx4 %2, %6, %8 sc1\n\tglobal_load_dwordx4 %3, %7, %8 sc1\n\ts_waitcnt vmcnt(0)"
               : "=&v"(d0), "=&v"(d1), "=&v"(d2), "=&v"(d3) : "v"(o0), "v"(o1), "v"(o2), "v"(o3), "s"(sb) : "memory");
}

#define STAMP(i) do { } while (0)

__global__ void __launch_bounds__(512, 2)
attn_fused_kernel(const char* __restrict__ x, const char* __restrict__ kc, const char* __restrict__ vc,
                  const char* __restrict__ wq, const char* __restrict__ wo, unsigned* __restrict__ out, char* __restrict__ ws) {
  extern __shared__ __attribute__((aligned(16))) unsigned char lds_raw[];
  LAS unsigned char* L = (LAS unsigned char*)lds_raw;
  const unsigned lbase = (unsigned)(size_t)L;
  STAMP(0);
  const int tid = threadIdx.x, lane = tid & 63, w = __builtin_amdgcn_readfirstlane(tid >> 6);
  const int g = lane >> 4, c = lane & 15;
  const int bid = blockIdx.x, kvh = bid & 7, s = (bid >> 3) & 3, b = bid >> 5;
  const int rot = 8 * s + b;
  const int rb = b >> 1, cb = (b & 1) * 4 + s;
  asm volatile("" :: "s"(x), "s"(kc), "s"(vc), "s"(wq), "s"(wo), "s"(out), "s"(ws));
  if (tid == 0) *(LAS unsigned*)(L + L_FLAG) = 0u;
  __builtin_amdgcn_s_setprio(3);

  unsigned ep;
  ld32_dev(ep, 0u, ws + W_EP + rb * 256);
  {
    const unsigned r8 = lane >> 3, f = ((r8 >> 1) & 1) | ((w & 1) << 1);
    const unsigned vo = r8 * (NQ * 2) + (((lane & 7) ^ (f << 1)) << 4);
    const char* wqb = wq + (size_t)(8 * w) * (NQ * 2) + (kvh * 512 + cb * 64) * 2;
#pragma unroll
    for (int i = 0; i < 7; ++i) dma1w(wqb + (size_t)(64 * i) * (NQ * 2), lbase + L_WQ + (8 * i + w) * 1024, vo);
    const char* xb = x + (size_t)(16 * rb) * (DM * 2);
#pragma unroll
    for (int i = 0; i < 2; ++i) {
      const int m = 8 * i + w;
      if (m < 14) {
        const unsigned p = 64 * m + lane, row = (p * 1171u) >> 16, ch = p - 56 * row;
        dma1w(xb, lbase + L_X + m * 1024, row * (DM * 2) + (((ch & ~7u) | ((ch & 7u) ^ ((row >> 1) & 7u))) << 4));
      }
    }
  }
  asm volatile("s_barrier" ::: "memory");
  const size_t headrow = (size_t)(b * HKV + kvh) * SK + (SK - WIN) + s * 512;
  const char* kwin = kc + headrow * 512;
  const char* vwin = vc + headrow * 512;
  unsigned ko[8];
  {
    const unsigned hi = lane >> 5, l31 = lane & 31;
    const unsigned laneK = (hi << 9) | ((l31 ^ hi) << 4);
#pragma unroll
    for (int q = 0; q < 8; ++q) ko[q] = (laneK ^ (unsigned)(q << 5)) + ((q >> 2) << 12);
  }
  auto tile_off = [&](int unit) -> size_t { return (size_t)((unit + rot) & 31) * 8192; };
  auto k_half = [&](int t, int h) {
    dma_half(kwin + tile_off(8 * t + w), lbase + L_RING + ((t & 1) * 8 + w) * 8192 + h * 4096, ko[4 * h], ko[4 * h + 1], ko[4 * h + 2], ko[4 * h + 3]);
  };
  k_half(0, 0);
  asm volatile("s_waitcnt vmcnt(4)" : "+v"(ep) :: "memory");
  const unsigned T = __builtin_amdgcn_readfirstlane(ep) + 1u;
  STAMP(1);
  BARRIER();
  char* qx = uni(ws + W_QX + (size_t)(kvh * 4 + rb) * (8 * 4096));
  {
    const int nt = w & 3, kh = w >> 2;
    const int q4 = (lane & 15) >> 2, p4 = lane & 3;
    const unsigned fa = (unsigned)(q4 >> 1) | ((unsigned)(g & 1) << 1);
    const unsigned ta = L_WQ + (224 * kh + 8 * g + q4) * 128 + (((2 * nt + (p4 >> 1)) ^ (fa << 1)) << 4) + (p4 & 1) * 8;
    f32x4 qa = {0.f, 0.f, 0.f, 0.f};
#pragma unroll
    for (int i = 0; i < 7; ++i) {
      const s16x4 a0 = __builtin_amdgcn_ds_read_tr16_b64_v4i16((LAS s16x4*)(L + ta + i * 4096));
      const s16x4 a1 = __builtin_amdgcn_ds_read_tr16_b64_v4i16((LAS s16x4*)(L + ta + i * 4096 + 512));
      const unsigned ch = 4 * (7 * kh + i) + g;
      const v4u xb = *(LAS v4u*)(L + L_X + c * 896 + (((ch & ~7u) | ((ch & 7u) ^ ((c >> 1) & 7u))) << 4));
      const v2u x0 = __builtin_bit_cast(v2u, a0), x1 = __builtin_bit_cast(v2u, a1);
      qa = mfma16(v4u{x0.x, x0.y, x1.x, x1.y}, xb, qa);
    }
    if (kh == 1) *(LAS f32x4*)(L + L_QR + nt * 1024 + lane * 16) = qa;
    STAMP(17);
    BARRIER();
    if (kh == 0) {
      qa += *(LAS f32x4*)(L + L_QR + nt * 1024 + lane * 16);
      st128_dev(cb * 4096 + nt * 1024 + lane * 16, v4u{pack_bf16(qa[0], qa[1]), T, pack_bf16(qa[2], qa[3]), T}, qx);
    }
  }
  STAMP(18);
  k_half(0, 1);
  k_half(1, 0);
  if (w < 4) {
    v4u qu[4];
    const unsigned qgo = w * 4096 + (lane >> 5) * 1024 + (16 * ((lane >> 3) & 3) + 8 * (b & 1) + (lane & 7)) * 16;
    STAMP(19);
    reload4(qu[0], qu[1], qu[2], qu[3], qgo, qgo + 2048, qgo + 16384, qgo + 18432, qx);
    STAMP(3);
    for (unsigned spin = 0; !wave_all(tags_ok(qu[0], T) && tags_ok(qu[1], T)) && spin < SPIN_CAP; ++spin) reload2(qu[0], qu[1], qgo, qgo + 2048, qx);
    for (unsigned spin = 0; !wave_all(tags_ok(qu[2], T) && tags_ok(qu[3], T)) && spin < SPIN_CAP; ++spin) reload2(qu[2], qu[3], qgo + 16384, qgo + 18432, qx);
    const int gq = (lane >> 3) & 3, q = lane & 7;
#pragma unroll
    for (int u = 0; u < 4; ++u) {
      const int nt = 2 * (u & 1) + (lane >> 5);
      const int st = w * 2 + (nt >> 1), gk = 2 * (nt & 1) + (gq >> 1);
      *(LAS v2u*)(L + L_QF + st * 1024 + (16 * gk + 8 * (u >> 1) + q) * 16 + (gq & 1) * 8) = v2u{qu[u].x, qu[u].z};
    }
    LGKM0();
    if (lane == 0) __hip_atomic_fetch_add((LAS unsigned*)(L + L_FLAG), 1u, __ATOMIC_RELAXED, __HIP_MEMORY_SCOPE_WORKGROUP);
  }
  STAMP(4);
  k_half(1, 1);
  STAMP(2);
  for (unsigned spin = 0; *(volatile LAS unsigned*)(L + L_FLAG) < 4u && spin < (SPIN_CAP << 6); ++spin) __builtin_amdgcn_s_sleep(1);
  asm volatile("" ::: "memory");
  VMWAIT(8);
  v4u qf[8];
#pragma unroll
  for (int st = 0; st < 8; ++st) qf[st] = *(LAS v4u*)(L + L_QF + st * 1024 + lane * 16);

  f32x4 sc[4];
  auto qk_tile = [&](int slot) -> f32x4 {
    v4u kf[8];
#pragma unroll
    for (int st = 0; st < 8; ++st) kf[st] = *(LAS v4u*)(L + L_RING + slot * 8192 + c * 512 + (((4 * st + g) ^ c) << 4));
    f32x4 a = {0.f, 0.f, 0.f, 0.f};
#pragma unroll
    for (int st = 0; st < 8; ++st) a = mfma16(kf[st], qf[st], a);
    return a;
  };
  const unsigned vlane = (lane >> 3) * 512 + (((lane & 7) ^ (((lane >> 4) & 3) << 1)) << 4);
  auto v_piece = [&](int p, int q) {
    dma1(vwin + tile_off(4 * w + (q >> 1)) + (q & 1) * 4096 + ((s + 1 + p) & 3) * 128,
         lbase + L_RING + ((p & 1) * 8 + w) * 8192 + q * 1024, vlane);
  };
  const int wc = w < 7 ? w : 6;
  v2u wv[4][8];
  const char* wob = wo + (size_t)(2 * kvh * 256 + 64 * s) * (DM * 2) + wc * 128;
  const unsigned wvo = (unsigned)(8 * g) * (DM * 2) + c * 8;
#define WO_N(a, b) (b)
  auto wo_load = [&](int ks, int i) { ld64(wv[ks][i], wvo + (unsigned)(256 * (ks >> 1) + 32 * (ks & 1) + i) * (DM * 2), wob); };

  auto wo_loads = [&](int n0, int n1) {
#pragma unroll
    for (int n = n0; n < n1; ++n) wo_load(n >> 3, n & 7);
  };
  auto v_half = [&](int p, int h) {
#pragma unroll
    for (int q = 4 * h; q < 4 * h + 4; ++q) v_piece(p, q);
  };
#define PRIO(n) __builtin_amdgcn_s_setprio(n)
  sc[0] = qk_tile(w);
  LGKM0();
  PRIO(2);
  wo_loads(0, 4); k_half(2, 0);
  VMWAIT_S(8);
  sc[1] = qk_tile(8 + w);
  LGKM0();
  PRIO(1);
  wo_loads(4, 7); k_half(2, 1); wo_loads(7, 10); k_half(3, 0);
  VMWAIT_S(7);
  sc[2] = qk_tile(w);
  LGKM0();
  PRIO(0);
  wo_loads(10, 13); k_half(3, 1); wo_loads(13, 16); v_half(0, 0);
  VMWAIT_S(7);
  sc[3] = qk_tile(8 + w);
  LGKM0();
  v_half(0, 1);
  STAMP(5);
  float mx = fmaxf(fmaxf(fmaxf(sc[0][0], sc[0][1]), fmaxf(sc[0][2], sc[0][3])), fmaxf(fmaxf(sc[1][0], sc[1][1]), fmaxf(sc[1][2], sc[1][3])));
  mx = fmaxf(mx, fmaxf(fmaxf(fmaxf(sc[2][0], sc[2][1]), fmaxf(sc[2][2], sc[2][3])), fmaxf(fmaxf(sc[3][0], sc[3][1]), fmaxf(sc[3][2], sc[3][3]))));
  mx = xg_max(mx);
  if (g == 0) *(LAS float*)(L + L_MX + (w * 16 + c) * 4) = mx;
  BARRIER();
  float mrow = *(LAS float*)(L + L_MX + c * 4);
#pragma unroll
  for (int ww = 1; ww < 8; ++ww) mrow = fmaxf(mrow, *(LAS float*)(L + L_MX + (ww * 16 + c) * 4));
  mrow *= C2;
  float lsum = 0.f;
#pragma unroll
  for (int t = 0; t < 4; ++t) {
    float p[4];
#pragma unroll
    for (int r = 0; r < 4; ++r) { p[r] = __builtin_amdgcn_exp2f(fmaf(sc[t][r], C2, -mrow)); lsum += p[r]; }
    *(LAS v2u*)(L + L_P + (4 * t + (w >> 1)) * 1024 + lane * 16 + (w & 1) * 8) = v2u{pack_bf16(p[0], p[1]), pack_bf16(p[2], p[3])};
  }
  lsum = xg_sum(lsum);
  if (g == 0) *(LAS float*)(L + L_LS + (w * 16 + c) * 4) = lsum;
  wo_loads(16, 20); v_half(1, 0);

  const int ct = w & 3, kh = w >> 2;
  unsigned tra;
  {
    const int q4 = (lane & 15) >> 2, p4 = lane & 3;
    tra = (4 * g + q4) * 128 + (((2 * ct + (p4 >> 1)) ^ (((2 * g + (q4 >> 1)) & 3) << 1)) << 4) + (p4 & 1) * 8 + kh * 32768;
  }
  auto pv_pass = [&](int p) -> f32x4 {
    f32x4 a = {0.f, 0.f, 0.f, 0.f};
#pragma unroll
    for (int i = 0; i < 8; ++i) {
      const v4u pf = *(LAS v4u*)(L + L_P + (8 * kh + i) * 1024 + lane * 16);
      const int ub = L_RING + (p & 1) * 65536 + i * 4096;
      const s16x4 a0 = __builtin_amdgcn_ds_read_tr16_b64_v4i16((LAS s16x4*)(L + ub + tra));
      const s16x4 a1 = __builtin_amdgcn_ds_read_tr16_b64_v4i16((LAS s16x4*)(L + ub + 2048 + tra));
      const v2u x0 = __builtin_bit_cast(v2u, a0), x1 = __builtin_bit_cast(v2u, a1);
      a = mfma16(v4u{x0.x, x0.y, x1.x, x1.y}, pf, a);
    }
    return a;
  };
  const int bh = b * HKV + kvh;
  char* stb = uni(ws + W_ST + (size_t)bh * 1024);
  char* h1 = uni(ws + W_H1 + (size_t)bh * (16 * 4096));
  auto reduce_pass = [&](f32x4 a) -> f32x4 {
    if (kh == 1) *(LAS f32x4*)(L + L_HALF + ct * 1024 + lane * 16) = a;
    BARRIER();
    if (kh == 0) a += *(LAS f32x4*)(L + L_HALF + ct * 1024 + lane * 16);
    return a;
  };
  auto publish_pass = [&](int p, f32x4 a) {
    if (kh == 0) st128_h1(((((s + 1 + p) & 3) * 4 + s) * 4 + ct) * 1024 + lane * 16, enc4(a, T), h1);
  };
  STAMP(6);
  VMWAIT_S(8);
  BARRIER();
  STAMP(7);
  {
    const f32x4 a = reduce_pass(pv_pass(0));
    wo_loads(20, 23); v_half(1, 1);
    asm volatile("s_barrier" ::: "memory");
    wo_loads(23, 26); v_half(2, 0);
    publish_pass(0, a);
    if (w == 7) {
      float lt = *(LAS float*)(L + L_LS + c * 4);
#pragma unroll
      for (int ww = 1; ww < 8; ++ww) lt += *(LAS float*)(L + L_LS + (ww * 16 + c) * 4);
      if (g == 0) st128_dev((s * 16 + c) * 16, v4u{__float_as_uint(mrow), T, __float_as_uint(lt), T}, stb);
    }
    if (kh == 0 || w == 7) VMWAIT_S(8); else VMWAIT_S(7);
  }
  BARRIER();
  v4u stu;
  {
    const f32x4 a = reduce_pass(pv_pass(1));
    wo_loads(26, 32); v_half(2, 1);
    asm volatile("s_barrier" ::: "memory");
    v_half(3, 0);
    publish_pass(1, a);
  }
  ld128_dev(stu, lane * 16, stb);
  if (kh == 0) VMWAIT(6); else VMWAIT(5);
  asm volatile(""
               : "+v"(wv[0][0]), "+v"(wv[0][1]), "+v"(wv[0][2]), "+v"(wv[0][3]), "+v"(wv[0][4]), "+v"(wv[0][5]), "+v"(wv[0][6]), "+v"(wv[0][7]),
                 "+v"(wv[1][0]), "+v"(wv[1][1]), "+v"(wv[1][2]), "+v"(wv[1][3]), "+v"(wv[1][4]), "+v"(wv[1][5]), "+v"(wv[1][6]), "+v"(wv[1][7])
               :: "memory");
  asm volatile("" : "+v"(wv[2][0]), "+v"(wv[2][1]), "+v"(wv[2][2]), "+v"(wv[2][3]), "+v"(wv[2][4]), "+v"(wv[2][5]), "+v"(wv[2][6]), "+v"(wv[2][7]),
                    "+v"(wv[3][0]), "+v"(wv[3][1]), "+v"(wv[3][2]), "+v"(wv[3][3]), "+v"(wv[3][4]), "+v"(wv[3][5]), "+v"(wv[3][6]), "+v"(wv[3][7])
               :: "memory");
  BARRIER();
  publish_pass(2, reduce_pass(pv_pass(2)));
  v_half(3, 1);
  v4u hu[2];
  const int srcA = (s + (kh ? 3 : 1)) & 3, srcB = (s + (kh ? 3 : 2)) & 3;
  const unsigned hoA = ((s * 4 + srcA) * 4 + ct) * 1024 + lane * 16, hoB = ((s * 4 + srcB) * 4 + ct) * 1024 + lane * 16;
  ld128_dev(hu[0], hoA, h1);
  ld128_dev(hu[1], hoB, h1);
  v4u wf[4][4];
#pragma unroll
  for (int ks = 0; ks < 4; ++ks)
#pragma unroll
    for (int jj = 0; jj < 4; ++jj)
#pragma unroll
      for (int r = 0; r < 4; ++r) {
        const unsigned lo = wv[ks][2 * r][jj >> 1], hi = wv[ks][2 * r + 1][jj >> 1];
        wf[ks][jj][r] = (jj & 1) ? perm_hi(lo, hi) : perm_lo(lo, hi);
      }
  asm volatile("s_waitcnt vmcnt(6)" : "+v"(stu) :: "memory");
  for (unsigned spin = 0; !wave_all(tags_ok(stu, T)) && spin < SPIN_CAP; ++spin) reload1(stu, lane * 16, stb);
  float fS, fA, fB;
  {
    const float ms = __uint_as_float(stu.x), ls = __uint_as_float(stu.z);
    const float M = xg_max(ms);
    const float e = __builtin_amdgcn_exp2f(ms - M);
    const float fw = e / xg_sum(e * ls);
    fS = __shfl(fw, 16 * s + c); fA = __shfl(fw, 16 * srcA + c); fB = kh ? 0.f : __shfl(fw, 16 * srcB + c);
  }
  asm volatile("s_waitcnt vmcnt(0)" : "+v"(hu[0]), "+v"(hu[1]) :: "memory");
  STAMP(8);
  BARRIER();
  f32x4 own = pv_pass(3);
  STAMP(9);
  for (unsigned spin = 0; !wave_all(quad_ok(hu[0], T) && quad_ok(hu[1], T)) && spin < SPIN_CAP; ++spin) reload2(hu[0], hu[1], hoA, hoB, h1);
  STAMP(11);
  {
    own = own * fS + dec4(hu[0]) * fA + dec4(hu[1]) * fB;
    if (kh == 1) *(LAS f32x4*)(L + L_HALF + ct * 1024 + lane * 16) = own;
    BARRIER();
    if (kh == 0) {
      own += *(LAS f32x4*)(L + L_HALF + ct * 1024 + lane * 16);
      *(LAS v2u*)(L + L_OB + c * 144 + (16 * ct + 4 * g) * 2) = v2u{pack_bf16(own[0], own[1]), pack_bf16(own[2], own[3])};
    }
  }
  BARRIER();

  f32x4 pa[4];
#pragma unroll
  for (int jj = 0; jj < 4; ++jj) pa[jj] = f32x4{0.f, 0.f, 0.f, 0.f};
#pragma unroll
  for (int ks = 0; ks < 4; ++ks) {
    const v4u af = *(LAS v4u*)(L + L_OB + (8 * (ks >> 1) + (c & 7)) * 144 + (32 * (ks & 1) + 8 * g) * 2);
#pragma unroll
    for (int jj = 0; jj < 4; ++jj) pa[jj] = mfma16(af, wf[ks][jj], pa[jj]);
  }
  const int j32 = kvh * 4 + s;
  char* h2 = uni(ws + W_H2 + (size_t)b * (32 * 8 * 112 * 16));
  if (w < 7) {
#pragma unroll
    for (int rr = 0; rr < 2; ++rr) {
      const int q = 4 * (g & 1) + 2 * (g >> 1) + rr;
      const f32x4 v = (g >> 1) ? f32x4{pa[0][2 + rr], pa[1][2 + rr], pa[2][2 + rr], pa[3][2 + rr]} : f32x4{pa[0][rr], pa[1][rr], pa[2][rr], pa[3][rr]};
      st128_dev(((j32 * 8 + q) * 112 + w * 16 + c) * 16, enc4(v, T), h2);
    }
  }
  STAMP(12);
  {
    const int l31 = lane & 31, lu = l31 < 28 ? l31 : 27;
    v4u fu[2];
    unsigned fo[2];
#pragma unroll
    for (int k = 0; k < 2; ++k) {
      fo[k] = (((4 * w + 2 * k + (lane >> 5)) * 8 + (j32 >> 2)) * 112 + 28 * (j32 & 3) + lu) * 16;
      ld128_dev(fu[k], fo[k], h2);
    }
    asm volatile("s_waitcnt vmcnt(0)" : "+v"(fu[0]), "+v"(fu[1]) :: "memory");
    for (unsigned spin = 0; !wave_all(quad_ok(fu[0], T) && quad_ok(fu[1], T)) && spin < SPIN_CAP; ++spin) reload2(fu[0], fu[1], fo[0], fo[1], h2);
    STAMP(13);
    *(LAS f32x4*)(L + L_RED + w * 1024 + lane * 16) = dec4(fu[0]) + dec4(fu[1]);
  }
  BARRIER();
  if (w == 0) {
    const int l31 = lane & 31, hf = lane >> 5;
    f32x4 t = *(LAS f32x4*)(L + L_RED + (4 * hf) * 1024 + l31 * 16);
    t += *(LAS f32x4*)(L + L_RED + (4 * hf) * 1024 + 512 + l31 * 16);
#pragma unroll
    for (int ww = 1; ww < 4; ++ww) {
      t += *(LAS f32x4*)(L + L_RED + (4 * hf + ww) * 1024 + l31 * 16);
      t += *(LAS f32x4*)(L + L_RED + (4 * hf + ww) * 1024 + 512 + l31 * 16);
    }
#pragma unroll
    for (int e = 0; e < 4; ++e) {
      auto sw = __builtin_amdgcn_permlane32_swap(__float_as_uint(t[e]), __float_as_uint(t[e]), false, false);
      t[e] = __uint_as_float(sw[0]) + __uint_as_float(sw[1]);
    }
    if (lane < 28)
      *(v2u*)(out + (((b * SQ + (j32 >> 2)) * DM + 112 * (j32 & 3) + 4 * lane) >> 1)) = v2u{pack_bf16(t[0], t[1]), pack_bf16(t[2], t[3])};
    if ((b & 1) == 0 && j32 == 0 && lane == 0) st32_dev(0u, T, ws + W_EP + rb * 256);
  }
}

}

extern "C" void kernel_launch(void* const* d_in, const int* in_sizes, int n_in, void* d_out, int out_size, void* d_ws,
                              size_t ws_size, hipStream_t stream) {
  (void)in_sizes; (void)n_in; (void)out_size;
  if (ws_size < W_END) return;
  (void)hipFuncSetAttribute((const void*)attn_fused_kernel, hipFuncAttributeMaxDynamicSharedMemorySize, LDS_BYTES);
  attn_fused_kernel<<<dim3(256), dim3(512), LDS_BYTES, stream>>>((const char*)d_in[0], (const char*)d_in[1], (const char*)d_in[2],
                                                                 (const char*)d_in[3], (const char*)d_in[4], (unsigned*)d_out, (char*)d_ws);
}
```

```cpp
#include <hip/hip_runtime.h>
#include <hip/hip_bf16.h>

namespace {

typedef unsigned v4u __attribute__((ext_vector_type(4)));
typedef unsigned v2u __attribute__((ext_vector_type(2)));
typedef float f32x4 __attribute__((ext_vector_type(4)));
typedef float f32x2 __attribute__((ext_vector_type(2)));
typedef __bf16 bf16x8 __attribute__((ext_vector_type(8)));
typedef __bf16 bf16x2 __attribute__((ext_vector_type(2)));
typedef short s16x4 __attribute__((ext_vector_type(4)));
#define LAS __attribute__((address_space(3)))

constexpr int NB = 8, SQ = 8, DM = 448, HKV = 8, SK = 8192, WIN = 2048;
constexpr int NQ = 4096;
constexpr float C2 = 0.0625f * 1.4426950408889634f;
constexpr unsigned SPIN_CAP = 1u << 18;

constexpr int L_RING = 0;
constexpr int L_WQ = L_RING + 65536;
constexpr int L_P = 131072;
constexpr int L_X = L_P;
constexpr int L_QF = L_P + 16384;
constexpr int L_HALF = L_QF;
constexpr int L_MX = L_QF + 8192;
constexpr int L_LS = L_MX + 512;
constexpr int L_QR = L_LS + 512;
constexpr int L_FLAG = L_QR + 4096;
constexpr int L_OB = L_FLAG + 16;
constexpr int L_RED = L_P;
constexpr int LDS_BYTES = 163840;
static_assert(L_OB + 16 * 144 <= LDS_BYTES, "LDS map");

constexpr size_t W_EP = 0;
constexpr size_t W_QX = 4096;
constexpr size_t W_ST = W_QX + (size_t)8 * 4 * 8 * 4096;
constexpr size_t W_H1 = W_ST + (size_t)64 * 4 * 256;
constexpr size_t W_H2 = W_H1 + (size_t)64 * 16 * 4096;
constexpr size_t W_END = W_H2 + (size_t)8 * 32 * 8 * 112 * 16;

__device__ __forceinline__ f32x4 mfma16(v4u a, v4u b, f32x4 c) {
  return __builtin_amdgcn_mfma_f32_16x16x32_bf16(__builtin_bit_cast(bf16x8, a), __builtin_bit_cast(bf16x8, b), c, 0, 0, 0);
}
__device__ __forceinline__ unsigned pack_bf16(float lo, float hi) {
  bf16x2 r; r[0] = (__bf16)lo; r[1] = (__bf16)hi;
  return __builtin_bit_cast(unsigned, r);
}
__device__ __forceinline__ float xg_max(float x) {
  auto s = __builtin_amdgcn_permlane16_swap(__float_as_uint(x), __float_as_uint(x), false, false);
  x = fmaxf(__uint_as_float(s[0]), __uint_as_float(s[1]));
  auto t = __builtin_amdgcn_permlane32_swap(__float_as_uint(x), __float_as_uint(x), false, false);
  return fmaxf(__uint_as_float(t[0]), __uint_as_float(t[1]));
}
__device__ __forceinline__ float xg_sum(float x) {
  auto s = __builtin_amdgcn_permlane16_swap(__float_as_uint(x), __float_as_uint(x), false, false);
  x = __uint_as_float(s[0]) + __uint_as_float(s[1]);
  auto t = __builtin_amdgcn_permlane32_swap(__float_as_uint(x), __float_as_uint(x), false, false);
  return __uint_as_float(t[0]) + __uint_as_float(t[1]);
}
__device__ __forceinline__ unsigned perm_lo(unsigned a, unsigned b) { return __builtin_amdgcn_perm(b, a, 0x05040100u); }
__device__ __forceinline__ unsigned perm_hi(unsigned a, unsigned b) { return __builtin_amdgcn_perm(b, a, 0x07060302u); }

#define T2REG 1
#define KPOL " nt"
#define VPOL " nt"
#define WPOL ""
#define VMWAIT(N) asm volatile("s_waitcnt vmcnt(" #N ")" ::: "memory")
#define LGKM0() asm volatile("s_waitcnt lgkmcnt(0)" ::: "memory")
#define BARRIER() asm volatile("s_waitcnt lgkmcnt(0)\n\ts_barrier" ::: "memory")

__device__ __forceinline__ void dma_half(const char* gbase, unsigned lds_dst, unsigned v0, unsigned v1, unsigned v2, unsigned v3) {
  unsigned keep;
  asm volatile(
      "s_mov_b32 %[keep], m0\n\ts_mov_b32 m0, %[l0]\n\ts_nop 2\n\t"
      "global_load_lds_dwordx4 %[v0], %[sb]" KPOL "\n\t"
      "global_load_lds_dwordx4 %[v1], %[sb] offset:1024" KPOL "\n\t"
      "global_load_lds_dwordx4 %[v2], %[sb] offset:2048" KPOL "\n\t"
      "global_load_lds_dwordx4 %[v3], %[sb] offset:3072" KPOL "\n\t"
      "s_mov_b32 m0, %[keep]"
      : [keep] "=&s"(keep)
      : [v0] "v"(v0), [v1] "v"(v1), [v2] "v"(v2), [v3] "v"(v3), [sb] "s"(gbase), [l0] "s"(lds_dst)
      : "memory");
}
__device__ __forceinline__ void dma1w(const char* sb, unsigned lds_dst, unsigned voff) {
  unsigned keep;
  asm volatile("s_mov_b32 %[keep], m0\n\ts_mov_b32 m0, %[l0]\n\ts_nop 2\n\tglobal_load_lds_dwordx4 %[v0], %[sb]" WPOL "\n\ts_mov_b32 m0, %[keep]"
               : [keep] "=&s"(keep) : [v0] "v"(voff), [sb] "s"(sb), [l0] "s"(lds_dst) : "memory");
}
__device__ __forceinline__ void dma1(const char* sb, unsigned lds_dst, unsigned voff) {
  unsigned keep;
  asm volatile("s_mov_b32 %[keep], m0\n\ts_mov_b32 m0, %[l0]\n\ts_nop 2\n\tglobal_load_lds_dwordx4 %[v0], %[sb]" VPOL "\n\ts_mov_b32 m0, %[keep]"
               : [keep] "=&s"(keep) : [v0] "v"(voff), [sb] "s"(sb), [l0] "s"(lds_dst) : "memory");
}
__device__ __forceinline__ void ld128_nt(v4u& d, unsigned voff, const char* sb) {
  asm volatile("global_load_dwordx4 %0, %1, %2" KPOL : "=v"(d) : "v"(voff), "s"(sb) : "memory");
}
__device__ __forceinline__ void ld64(v2u& d, unsigned voff, const char* sb) {
  asm volatile("global_load_dwordx2 %0, %1, %2" WPOL : "=v"(d) : "v"(voff), "s"(sb) : "memory");
}
__device__ __forceinline__ void ld128_dev(v4u& d, unsigned voff, const char* sb) {
  asm volatile("global_load_dwordx4 %0, %1, %2 sc1" : "=v"(d) : "v"(voff), "s"(sb) : "memory");
}
__device__ __forceinline__ void ld32_dev(unsigned& d, unsigned voff, const char* sb) {
  asm volatile("global_load_dword %0, %1, %2 sc1" : "=v"(d) : "v"(voff), "s"(sb) : "memory");
}
__device__ __forceinline__ void st128_dev(unsigned voff, v4u v, char* sb) {
  asm volatile("global_store_dwordx4 %0, %1, %2 sc1\n\ts_nop 1" : : "v"(voff), "v"(v), "s"(sb) : "memory");
}
__device__ __forceinline__ void st32_dev(unsigned voff, unsigned v, char* sb) {
  asm volatile("global_store_dword %0, %1, %2 sc1" : : "v"(voff), "v"(v), "s"(sb) : "memory");
}
__device__ __forceinline__ bool tags_ok(v4u u, unsigned T) { return u.y == T && u.w == T; }
constexpr unsigned TMASK = 0x3FFu;
__device__ __forceinline__ unsigned enc(float v, unsigned T) { return ((__float_as_uint(v) + 0x200u) & ~TMASK) | (T & TMASK); }
__device__ __forceinline__ float dec(unsigned u) { return __uint_as_float(u & ~TMASK); }
__device__ __forceinline__ v4u enc4(f32x4 a, unsigned T) { return v4u{enc(a[0], T), enc(a[1], T), enc(a[2], T), enc(a[3], T)}; }
__device__ __forceinline__ f32x4 dec4(v4u u) { return f32x4{dec(u.x), dec(u.y), dec(u.z), dec(u.w)}; }
__device__ __forceinline__ bool quad_ok(v4u u, unsigned T) { return (((u.x ^ T) | (u.y ^ T) | (u.z ^ T) | (u.w ^ T)) & TMASK) == 0u; }
template <class P> __device__ __forceinline__ P* uni(P* p) {
  const unsigned long long a = (unsigned long long)p;
  const unsigned lo = __builtin_amdgcn_readfirstlane((unsigned)a), hi = __builtin_amdgcn_readfirstlane((unsigned)(a >> 32));
  return (P*)(((unsigned long long)hi << 32) | lo);
}
__device__ __forceinline__ bool wave_all(bool ok) { return __builtin_amdgcn_ballot_w64(!ok) == 0ull; }
__device__ __forceinline__ void reload1(v4u& d0, unsigned o0, const char* sb) {
  asm volatile("global_load_dwordx4 %0, %1, %2 sc1\n\ts_waitcnt vmcnt(0)" : "=&v"(d0) : "v"(o0), "s"(sb) : "memory");
}
__device__ __forceinline__ void reload2(v4u& d0, v4u& d1, unsigned o0, unsigned o1, const char* sb) {
  asm volatile("global_load_dwordx4 %0, %2, %4 sc1\n\tglobal_load_dwordx4 %1, %3, %4 sc1\n\ts_waitcnt vmcnt(0)"
               : "=&v"(d0), "=&v"(d1) : "v"(o0), "v"(o1), "s"(sb) : "memory");
}

__device__ __forceinline__ void reload4(v4u& d0, v4u& d1, v4u& d2, v4u& d3, unsigned o0, unsigned o1, unsigned o2, unsigned o3, const char* sb) {
  asm volatile("global_load_dwordx4 %0, %4, %8 sc1\n\tglobal_load_dwordx4 %1, %5, %8 sc1\n\t"
               "global_load_dwordx4 %2, %6, %8 sc1\n\tglobal_load_dwordx4 %3, %7, %8 sc1\n\ts_waitcnt vmcnt(0)"
               : "=&v"(d0), "=&v"(d1), "=&v"(d2), "=&v"(d3) : "v"(o0), "v"(o1), "v"(o2), "v"(o3), "s"(sb) : "memory");
}

#define STAMP(i) do { } while (0)

__global__ void __launch_bounds__(512, 2)
attn_fused_kernel(const char* __restrict__ x, const char* __restrict__ kc, const char* __restrict__ vc,
                  const char* __restrict__ wq, const char* __restrict__ wo, unsigned* __restrict__ out, char* __restrict__ ws) {
  extern __shared__ __attribute__((aligned(16))) unsigned char lds_raw[];
  LAS unsigned char* L = (LAS unsigned char*)lds_raw;
  const unsigned lbase = (unsigned)(size_t)L;
  STAMP(0);
  const int tid = threadIdx.x, lane = tid & 63, w = __builtin_amdgcn_readfirstlane(tid >> 6);
  const int g = lane >> 4, c = lane & 15;
  const int bid = blockIdx.x, kvh = bid & 7, s = (bid >> 3) & 3, b = bid >> 5;
  const int rot = 8 * s + b;
  const int rb = b >> 1, cb = (b & 1) * 4 + s;
  asm volatile("" :: "s"(x), "s"(kc), "s"(vc), "s"(wq), "s"(wo), "s"(out), "s"(ws));
  if (tid == 0) *(LAS unsigned*)(L + L_FLAG) = 0u;
  __builtin_amdgcn_s_setprio(3);

  unsigned ep;
  ld32_dev(ep, 0u, ws + W_EP + rb * 256);
  {
    const unsigned r8 = lane >> 3, f = ((r8 >> 1) & 1) | ((w & 1) << 1);
    const unsigned vo = r8 * (NQ * 2) + (((lane & 7) ^ (f << 1)) << 4);
    const char* wqb = wq + (size_t)(8 * w) * (NQ * 2) + (kvh * 512 + cb * 64) * 2;
#pragma unroll
    for (int i = 0; i < 7; ++i) dma1w(wqb + (size_t)(64 * i) * (NQ * 2), lbase + L_WQ + (8 * i + w) * 1024, vo);
    const char* xb = x + (size_t)(16 * rb) * (DM * 2);
#pragma unroll
    for (int i = 0; i < 2; ++i) {
      const int m = 8 * i + w;
      if (m < 14) {
        const unsigned p = 64 * m + lane, row = (p * 1171u) >> 16, ch = p - 56 * row;
        dma1w(xb, lbase + L_X + m * 1024, row * (DM * 2) + (((ch & ~7u) | ((ch & 7u) ^ ((row >> 1) & 7u))) << 4));
      }
    }
  }
  asm volatile("s_barrier" ::: "memory");
  const size_t headrow = (size_t)(b * HKV + kvh) * SK + (SK - WIN) + s * 512;
  const char* kwin = kc + headrow * 512;
  const char* vwin = vc + headrow * 512;
  unsigned ko[8];
  {
    const unsigned hi = lane >> 5, l31 = lane & 31;
    const unsigned laneK = (hi << 9) | ((l31 ^ hi) << 4);
#pragma unroll
    for (int q = 0; q < 8; ++q) ko[q] = (laneK ^ (unsigned)(q << 5)) + ((q >> 2) << 12);
  }
  auto tile_off = [&](int unit) -> size_t { return (size_t)((unit + rot) & 31) * 8192; };
  auto k_half = [&](int t, int h) {
    dma_half(kwin + tile_off(8 * t + w), lbase + L_RING + ((t & 1) * 8 + w) * 8192 + h * 4096, ko[4 * h], ko[4 * h + 1], ko[4 * h + 2], ko[4 * h + 3]);
  };
  k_half(0, 0);
  asm volatile("s_waitcnt vmcnt(4)" : "+v"(ep) :: "memory");
  const unsigned T = __builtin_amdgcn_readfirstlane(ep) + 1u;
  STAMP(1);
  BARRIER();
  char* qx = uni(ws + W_QX + (size_t)(kvh * 4 + rb) * (8 * 4096));
  {
    const int nt = w & 3, kh = w >> 2;
    const int q4 = (lane & 15) >> 2, p4 = lane & 3;
    const unsigned fa = (unsigned)(q4 >> 1) | ((unsigned)(g & 1) << 1);
    const unsigned ta = L_WQ + (224 * kh + 8 * g + q4) * 128 + (((2 * nt + (p4 >> 1)) ^ (fa << 1)) << 4) + (p4 & 1) * 8;
    f32x4 qa = {0.f, 0.f, 0.f, 0.f};
#pragma unroll
    for (int i = 0; i < 7; ++i) {
      const s16x4 a0 = __builtin_amdgcn_ds_read_tr16_b64_v4i16((LAS s16x4*)(L + ta + i * 4096));
      const s16x4 a1 = __builtin_amdgcn_ds_read_tr16_b64_v4i16((LAS s16x4*)(L + ta + i * 4096 + 512));
      const unsigned ch = 4 * (7 * kh + i) + g;
      const v4u xb = *(LAS v4u*)(L + L_X + c * 896 + (((ch & ~7u) | ((ch & 7u) ^ ((c >> 1) & 7u))) << 4));
      const v2u x0 = __builtin_bit_cast(v2u, a0), x1 = __builtin_bit_cast(v2u, a1);
      qa = mfma16(v4u{x0.x, x0.y, x1.x, x1.y}, xb, qa);
    }
    if (kh == 1) *(LAS f32x4*)(L + L_QR + nt * 1024 + lane * 16) = qa;
    STAMP(17);
    BARRIER();
    if (kh == 0) {
      qa += *(LAS f32x4*)(L + L_QR + nt * 1024 + lane * 16);
      st128_dev(cb * 4096 + nt * 1024 + lane * 16, v4u{pack_bf16(qa[0], qa[1]), T, pack_bf16(qa[2], qa[3]), T}, qx);
    }
  }
  STAMP(18);
  k_half(0, 1);
  k_half(1, 0);
  if (w < 4) {
    v4u qu[4];
    const unsigned qgo = w * 4096 + (lane >> 5) * 1024 + (16 * ((lane >> 3) & 3) + 8 * (b & 1) + (lane & 7)) * 16;
    STAMP(19);
    reload4(qu[0], qu[1], qu[2], qu[3], qgo, qgo + 2048, qgo + 16384, qgo + 18432, qx);
    STAMP(3);
    for (unsigned spin = 0; !wave_all(tags_ok(qu[0], T) && tags_ok(qu[1], T)) && spin < SPIN_CAP; ++spin) reload2(qu[0], qu[1], qgo, qgo + 2048, qx);
    for (unsigned spin = 0; !wave_all(tags_ok(qu[2], T) && tags_ok(qu[3], T)) && spin < SPIN_CAP; ++spin) reload2(qu[2], qu[3], qgo + 16384, qgo + 18432, qx);
    const int gq = (lane >> 3) & 3, q = lane & 7;
#pragma unroll
    for (int u = 0; u < 4; ++u) {
      const int nt = 2 * (u & 1) + (lane >> 5);
      const int st = w * 2 + (nt >> 1), gk = 2 * (nt & 1) + (gq >> 1);
      *(LAS v2u*)(L + L_QF + st * 1024 + (16 * gk + 8 * (u >> 1) + q) * 16 + (gq & 1) * 8) = v2u{qu[u].x, qu[u].z};
    }
    LGKM0();
    if (lane == 0) __hip_atomic_fetch_add((LAS unsigned*)(L + L_FLAG), 1u, __ATOMIC_RELAXED, __HIP_MEMORY_SCOPE_WORKGROUP);
  }
  STAMP(4);
  k_half(1, 1);
  v4u kreg[8];
  {
    const char* t2b = kwin + tile_off(16 + w);
#pragma unroll
    for (int q = 0; q < 8; ++q) ld128_nt(kreg[q], q * 1024 + lane * 16, t2b);
  }
  STAMP(2);
  for (unsigned spin = 0; *(volatile LAS unsigned*)(L + L_FLAG) < 4u && spin < (SPIN_CAP << 6); ++spin) __builtin_amdgcn_s_sleep(1);
  asm volatile("" ::: "memory");
  VMWAIT(16);
  v4u qf[8];
#pragma unroll
  for (int st = 0; st < 8; ++st) qf[st] = *(LAS v4u*)(L + L_QF + st * 1024 + lane * 16);

  f32x4 sc[4];
  auto qk_tile = [&](int slot) -> f32x4 {
    v4u kf[8];
#pragma unroll
    for (int st = 0; st < 8; ++st) kf[st] = *(LAS v4u*)(L + L_RING + slot * 8192 + c * 512 + (((4 * st + g) ^ c) << 4));
    f32x4 a = {0.f, 0.f, 0.f, 0.f};
#pragma unroll
    for (int st = 0; st < 8; ++st) a = mfma16(kf[st], qf[st], a);
    return a;
  };
  const unsigned vlane = (lane >> 3) * 512 + (((lane & 7) ^ (((lane >> 4) & 3) << 1)) << 4);
  auto v_piece = [&](int p, int q) {
    dma1(vwin + tile_off(4 * w + (q >> 1)) + (q & 1) * 4096 + ((s + 1 + p) & 3) * 128,
         lbase + L_RING + ((p & 1) * 8 + w) * 8192 + q * 1024, vlane);
  };
  const int wc = w < 7 ? w : 6;
  v2u wv[4][8];
  const char* wob = wo + (size_t)(2 * kvh * 256 + 64 * s) * (DM * 2) + wc * 128;
  const unsigned wvo = (unsigned)(8 * g) * (DM * 2) + c * 8;
  auto wo_load = [&](int ks, int i) { ld64(wv[ks][i], wvo + (unsigned)(256 * (ks >> 1) + 32 * (ks & 1) + i) * (DM * 2), wob); };

  auto wo_loads = [&](int n0, int n1) {
#pragma unroll
    for (int n = n0; n < n1; ++n) wo_load(n >> 3, n & 7);
  };
  auto v_half = [&](int p, int h) {
#pragma unroll
    for (int q = 4 * h; q < 4 * h + 4; ++q) v_piece(p, q);
  };
#define PRIO(n) __builtin_amdgcn_s_setprio(n)
  sc[0] = qk_tile(w);
  VMWAIT(8);
  sc[1] = qk_tile(8 + w);
  LGKM0();
  PRIO(2);
  wo_loads(0, 4); k_half(3, 0); wo_loads(4, 7);
  asm volatile("s_waitcnt vmcnt(11)"
               : "+v"(kreg[0]), "+v"(kreg[1]), "+v"(kreg[2]), "+v"(kreg[3]), "+v"(kreg[4]), "+v"(kreg[5]), "+v"(kreg[6]), "+v"(kreg[7]) :: "memory");
#pragma unroll
  for (int q = 0; q < 8; ++q) *(LAS v4u*)(L + L_RING + w * 8192 + ko[q] + (q & 3) * 1024) = kreg[q];
  sc[2] = qk_tile(w);
  LGKM0();
  PRIO(1);
  wo_loads(7, 10); k_half(3, 1); wo_loads(10, 13); v_half(0, 0);
  VMWAIT(7);
  sc[3] = qk_tile(8 + w);
  LGKM0();
  PRIO(0);
  wo_loads(13, 16); v_half(0, 1);
  STAMP(5);
  float mx = fmaxf(fmaxf(fmaxf(sc[0][0], sc[0][1]), fmaxf(sc[0][2], sc[0][3])), fmaxf(fmaxf(sc[1][0], sc[1][1]), fmaxf(sc[1][2], sc[1][3])));
  mx = fmaxf(mx, fmaxf(fmaxf(fmaxf(sc[2][0], sc[2][1]), fmaxf(sc[2][2], sc[2][3])), fmaxf(fmaxf(sc[3][0], sc[3][1]), fmaxf(sc[3][2], sc[3][3]))));
  mx = xg_max(mx);
  if (g == 0) *(LAS float*)(L + L_MX + (w * 16 + c) * 4) = mx;
  BARRIER();
  float mrow = *(LAS float*)(L + L_MX + c * 4);
#pragma unroll
  for (int ww = 1; ww < 8; ++ww) mrow = fmaxf(mrow, *(LAS float*)(L + L_MX + (ww * 16 + c) * 4));
  mrow *= C2;
  float lsum = 0.f;
#pragma unroll
  for (int t = 0; t < 4; ++t) {
    float p[4];
#pragma unroll
    for (int r = 0; r < 4; ++r) { p[r] = __builtin_amdgcn_exp2f(fmaf(sc[t][r], C2, -mrow)); lsum += p[r]; }
    *(LAS v2u*)(L + L_P + (4 * t + (w >> 1)) * 1024 + lane * 16 + (w & 1) * 8) = v2u{pack_bf16(p[0], p[1]), pack_bf16(p[2], p[3])};
  }
  lsum = xg_sum(lsum);
  if (g == 0) *(LAS float*)(L + L_LS + (w * 16 + c) * 4) = lsum;
  wo_loads(16, 20); v_half(1, 0);

  const int ct = w & 3, kh = w >> 2;
  unsigned tra;
  {
    const int q4 = (lane & 15) >> 2, p4 = lane & 3;
    tra = (4 * g + q4) * 128 + (((2 * ct + (p4 >> 1)) ^ (((2 * g + (q4 >> 1)) & 3) << 1)) << 4) + (p4 & 1) * 8 + kh * 32768;
  }
  auto pv_pass = [&](int p) -> f32x4 {
    f32x4 a = {0.f, 0.f, 0.f, 0.f};
#pragma unroll
    for (int i = 0; i < 8; ++i) {
      const v4u pf = *(LAS v4u*)(L + L_P + (8 * kh + i) * 1024 + lane * 16);
      const int ub = L_RING + (p & 1) * 65536 + i * 4096;
      const s16x4 a0 = __builtin_amdgcn_ds_read_tr16_b64_v4i16((LAS s16x4*)(L + ub + tra));
      const s16x4 a1 = __builtin_amdgcn_ds_read_tr16_b64_v4i16((LAS s16x4*)(L + ub + 2048 + tra));
      const v2u x0 = __builtin_bit_cast(v2u, a0), x1 = __builtin_bit_cast(v2u, a1);
      a = mfma16(v4u{x0.x, x0.y, x1.x, x1.y}, pf, a);
    }
    return a;
  };
  const int bh = b * HKV + kvh;
  char* stb = uni(ws + W_ST + (size_t)bh * 1024);
  char* h1 = uni(ws + W_H1 + (size_t)bh * (16 * 4096));
  auto reduce_pass = [&](f32x4 a) -> f32x4 {
    if (kh == 1) *(LAS f32x4*)(L + L_HALF + ct * 1024 + lane * 16) = a;
    BARRIER();
    if (kh == 0) a += *(LAS f32x4*)(L + L_HALF + ct * 1024 + lane * 16);
    return a;
  };
  auto publish_pass = [&](int p, f32x4 a) {
    if (kh == 0) st128_dev(((((s + 1 + p) & 3) * 4 + s) * 4 + ct) * 1024 + lane * 16, enc4(a, T), h1);
  };
  STAMP(6);
  VMWAIT(8);
  BARRIER();
  STAMP(7);
  {
    const f32x4 a = reduce_pass(pv_pass(0));
    wo_loads(20, 23); v_half(1, 1);
    asm volatile("s_barrier" ::: "memory");
    wo_loads(23, 26); v_half(2, 0);
    publish_pass(0, a);
    if (w == 7) {
      float lt = *(LAS float*)(L + L_LS + c * 4);
#pragma unroll
      for (int ww = 1; ww < 8; ++ww) lt += *(LAS float*)(L + L_LS + (ww * 16 + c) * 4);
      if (g == 0) st128_dev((s * 16 + c) * 16, v4u{__float_as_uint(mrow), T, __float_as_uint(lt), T}, stb);
    }
    if (kh == 0 || w == 7) VMWAIT(8); else VMWAIT(7);
  }
  BARRIER();
  v4u stu;
  {
    const f32x4 a = reduce_pass(pv_pass(1));
    wo_loads(26, 32); v_half(2, 1);
    asm volatile("s_barrier" ::: "memory");
    v_half(3, 0);
    publish_pass(1, a);
  }
  ld128_dev(stu, lane * 16, stb);
  if (kh == 0) VMWAIT(6); else VMWAIT(5);
  asm volatile(""
               : "+v"(wv[0][0]), "+v"(wv[0][1]), "+v"(wv[0][2]), "+v"(wv[0][3]), "+v"(wv[0][4]), "+v"(wv[0][5]), "+v"(wv[0][6]), "+v"(wv[0][7]),
                 "+v"(wv[1][0]), "+v"(wv[1][1]), "+v"(wv[1][2]), "+v"(wv[1][3]), "+v"(wv[1][4]), "+v"(wv[1][5]), "+v"(wv[1][6]), "+v"(wv[1][7])
               :: "memory");
  asm volatile("" : "+v"(wv[2][0]), "+v"(wv[2][1]), "+v"(wv[2][2]), "+v"(wv[2][3]), "+v"(wv[2][4]), "+v"(wv[2][5]), "+v"(wv[2][6]), "+v"(wv[2][7]),
                    "+v"(wv[3][0]), "+v"(wv[3][1]), "+v"(wv[3][2]), "+v"(wv[3][3]), "+v"(wv[3][4]), "+v"(wv[3][5]), "+v"(wv[3][6]), "+v"(wv[3][7])
               :: "memory");
  BARRIER();
  publish_pass(2, reduce_pass(pv_pass(2)));
  v_half(3, 1);
  v4u hu[2];
  const int srcA = (s + (kh ? 3 : 1)) & 3, srcB = (s + (kh ? 3 : 2)) & 3;
  const unsigned hoA = ((s * 4 + srcA) * 4 + ct) * 1024 + lane * 16, hoB = ((s * 4 + srcB) * 4 + ct) * 1024 + lane * 16;
  ld128_dev(hu[0], hoA, h1);
  ld128_dev(hu[1], hoB, h1);
  v4u wf[4][4];
#pragma unroll
  for (int ks = 0; ks < 4; ++ks)
#pragma unroll
    for (int jj = 0; jj < 4; ++jj)
#pragma unroll
      for (int r = 0; r < 4; ++r) {
        const unsigned lo = wv[ks][2 * r][jj >> 1], hi = wv[ks][2 * r + 1][jj >> 1];
        wf[ks][jj][r] = (jj & 1) ? perm_hi(lo, hi) : perm_lo(lo, hi);
      }
  asm volatile("s_waitcnt vmcnt(6)" : "+v"(stu) :: "memory");
  for (unsigned spin = 0; !wave_all(tags_ok(stu, T)) && spin < SPIN_CAP; ++spin) reload1(stu, lane * 16, stb);
  float fS, fA, fB;
  {
    const float ms = __uint_as_float(stu.x), ls = __uint_as_float(stu.z);
    const float M = xg_max(ms);
    const float e = __builtin_amdgcn_exp2f(ms - M);
    const float fw = e / xg_sum(e * ls);
    fS = __shfl(fw, 16 * s + c); fA = __shfl(fw, 16 * srcA + c); fB = kh ? 0.f : __shfl(fw, 16 * srcB + c);
  }
  asm volatile("s_waitcnt vmcnt(0)" : "+v"(hu[0]), "+v"(hu[1]) :: "memory");
  STAMP(8);
  BARRIER();
  f32x4 own = pv_pass(3);
  STAMP(9);
  for (unsigned spin = 0; !wave_all(quad_ok(hu[0], T) && quad_ok(hu[1], T)) && spin < SPIN_CAP; ++spin) reload2(hu[0], hu[1], hoA, hoB, h1);
  STAMP(11);
  {
    own = own * fS + dec4(hu[0]) * fA + dec4(hu[1]) * fB;
    if (kh == 1) *(LAS f32x4*)(L + L_HALF + ct * 1024 + lane * 16) = own;
    BARRIER();
    if (kh == 0) {
      own += *(LAS f32x4*)(L + L_HALF + ct * 1024 + lane * 16);
      *(LAS v2u*)(L + L_OB + c * 144 + (16 * ct + 4 * g) * 2) = v2u{pack_bf16(own[0], own[1]), pack_bf16(own[2], own[3])};
    }
  }
  BARRIER();

  f32x4 pa[4];
#pragma unroll
  for (int jj = 0; jj < 4; ++jj) pa[jj] = f32x4{0.f, 0.f, 0.f, 0.f};
#pragma unroll
  for (int ks = 0; ks < 4; ++ks) {
    const v4u af = *(LAS v4u*)(L + L_OB + (8 * (ks >> 1) + (c & 7)) * 144 + (32 * (ks & 1) + 8 * g) * 2);
#pragma unroll
    for (int jj = 0; jj < 4; ++jj) pa[jj] = mfma16(af, wf[ks][jj], pa[jj]);
  }
  const int j32 = kvh * 4 + s;
  char* h2 = uni(ws + W_H2 + (size_t)b * (32 * 8 * 112 * 16));
  if (w < 7) {
#pragma unroll
    for (int rr = 0; rr < 2; ++rr) {
      const int q = 4 * (g & 1) + 2 * (g >> 1) + rr;
      const f32x4 v = (g >> 1) ? f32x4{pa[0][2 + rr], pa[1][2 + rr], pa[2][2 + rr], pa[3][2 + rr]} : f32x4{pa[0][rr], pa[1][rr], pa[2][rr], pa[3][rr]};
      st128_dev(((j32 * 8 + q) * 112 + w * 16 + c) * 16, enc4(v, T), h2);
    }
  }
  STAMP(12);
  {
    const int l31 = lane & 31, lu = l31 < 28 ? l31 : 27;
    v4u fu[2];
    unsigned fo[2];
#pragma unroll
    for (int k = 0; k < 2; ++k) {
      fo[k] = (((4 * w + 2 * k + (lane >> 5)) * 8 + (j32 >> 2)) * 112 + 28 * (j32 & 3) + lu) * 16;
      ld128_dev(fu[k], fo[k], h2);
    }
    asm volatile("s_waitcnt vmcnt(0)" : "+v"(fu[0]), "+v"(fu[1]) :: "memory");
    for (unsigned spin = 0; !wave_all(quad_ok(fu[0], T) && quad_ok(fu[1], T)) && spin < SPIN_CAP; ++spin) reload2(fu[0], fu[1], fo[0], fo[1], h2);
    STAMP(13);
    *(LAS f32x4*)(L + L_RED + w * 1024 + lane * 16) = dec4(fu[0]) + dec4(fu[1]);
  }
  BARRIER();
  if (w == 0) {
    const int l31 = lane & 31, hf = lane >> 5;
    f32x4 t = *(LAS f32x4*)(L + L_RED + (4 * hf) * 1024 + l31 * 16);
    t += *(LAS f32x4*)(L + L_RED + (4 * hf) * 1024 + 512 + l31 * 16);
#pragma unroll
    for (int ww = 1; ww < 4; ++ww) {
      t += *(LAS f32x4*)(L + L_RED + (4 * hf + ww) * 1024 + l31 * 16);
      t += *(LAS f32x4*)(L + L_RED + (4 * hf + ww) * 1024 + 512 + l31 * 16);
    }
#pragma unroll
    for (int e = 0; e < 4; ++e) {
      auto sw = __builtin_amdgcn_permlane32_swap(__float_as_uint(t[e]), __float_as_uint(t[e]), false, false);
      t[e] = __uint_as_float(sw[0]) + __uint_as_float(sw[1]);
    }
    if (lane < 28)
      *(v2u*)(out + (((b * SQ + (j32 >> 2)) * DM + 112 * (j32 & 3) + 4 * lane) >> 1)) = v2u{pack_bf16(t[0], t[1]), pack_bf16(t[2], t[3])};
    if ((b & 1) == 0 && j32 == 0 && lane == 0) st32_dev(0u, T, ws + W_EP + rb * 256);
  }
}

}

extern "C" void kernel_launch(void* const* d_in, const int* in_sizes, int n_in, void* d_out, int out_size, void* d_ws,
                              size_t ws_size, hipStream_t stream) {
  (void)in_sizes; (void)n_in; (void)out_size;
  if (ws_size < W_END) return;
  (void)hipFuncSetAttribute((const void*)attn_fused_kernel, hipFuncAttributeMaxDynamicSharedMemorySize, LDS_BYTES);
  attn_fused_kernel<<<dim3(256), dim3(512), LDS_BYTES, stream>>>((const char*)d_in[0], (const char*)d_in[1], (const char*)d_in[2],
                                                                 (const char*)d_in[3], (const char*)d_in[4], (unsigned*)d_out, (char*)d_ws);
}
```

```cpp
#define PROBE 1
#define PROBEWG (-1)
#include <hip/hip_runtime.h>
#include <hip/hip_bf16.h>

namespace {

typedef unsigned v4u __attribute__((ext_vector_type(4)));
typedef unsigned v2u __attribute__((ext_vector_type(2)));
typedef float f32x4 __attribute__((ext_vector_type(4)));
typedef float f32x2 __attribute__((ext_vector_type(2)));
typedef __bf16 bf16x8 __attribute__((ext_vector_type(8)));
typedef __bf16 bf16x2 __attribute__((ext_vector_type(2)));
typedef short s16x4 __attribute__((ext_vector_type(4)));
#define LAS __attribute__((address_space(3)))

constexpr int NB = 8, SQ = 8, DM = 448, HKV = 8, SK = 8192, WIN = 2048;
constexpr int NQ = 4096;
constexpr float C2 = 0.0625f * 1.4426950408889634f;
constexpr unsigned SPIN_CAP = 1u << 18;

constexpr int L_RING = 0;
constexpr int L_WQ = L_RING + 65536;
constexpr int L_P = 131072;
constexpr int L_X = L_P;
constexpr int L_QF = L_P + 16384;
constexpr int L_HALF = L_QF;
constexpr int L_MX = L_QF + 8192;
constexpr int L_LS = L_MX + 512;
constexpr int L_QR = L_LS + 512;
constexpr int L_FLAG = L_QR + 4096;
constexpr int L_OB = L_FLAG + 16;
constexpr int L_RED = L_P;
constexpr int LDS_BYTES = 163840;
static_assert(L_OB + 16 * 144 <= LDS_BYTES, "LDS map");

constexpr size_t W_EP = 0;
constexpr size_t W_QX = 4096;
constexpr size_t W_ST = W_QX + (size_t)8 * 4 * 8 * 4096;
constexpr size_t W_H1 = W_ST + (size_t)64 * 4 * 256;
constexpr size_t W_H2 = W_H1 + (size_t)64 * 16 * 4096;
constexpr size_t W_END = W_H2 + (size_t)8 * 32 * 8 * 112 * 16;

__device__ __forceinline__ f32x4 mfma16(v4u a, v4u b, f32x4 c) {
  return __builtin_amdgcn_mfma_f32_16x16x32_bf16(__builtin_bit_cast(bf16x8, a), __builtin_bit_cast(bf16x8, b), c, 0, 0, 0);
}
__device__ __forceinline__ unsigned pack_bf16(float lo, float hi) {
  bf16x2 r; r[0] = (__bf16)lo; r[1] = (__bf16)hi;
  return __builtin_bit_cast(unsigned, r);
}
__device__ __forceinline__ float xg_max(float x) {
  auto s = __builtin_amdgcn_permlane16_swap(__float_as_uint(x), __float_as_uint(x), false, false);
  x = fmaxf(__uint_as_float(s[0]), __uint_as_float(s[1]));
  auto t = __builtin_amdgcn_permlane32_swap(__float_as_uint(x), __float_as_uint(x), false, false);
  return fmaxf(__uint_as_float(t[0]), __uint_as_float(t[1]));
}
__device__ __forceinline__ float xg_sum(float x) {
  auto s = __builtin_amdgcn_permlane16_swap(__float_as_uint(x), __float_as_uint(x), false, false);
  x = __uint_as_float(s[0]) + __uint_as_float(s[1]);
  auto t = __builtin_amdgcn_permlane32_swap(__float_as_uint(x), __float_as_uint(x), false, false);
  return __uint_as_float(t[0]) + __uint_as_float(t[1]);
}
__device__ __forceinline__ unsigned perm_lo(unsigned a, unsigned b) { return __builtin_amdgcn_perm(b, a, 0x05040100u); }
__device__ __forceinline__ unsigned perm_hi(unsigned a, unsigned b) { return __builtin_amdgcn_perm(b, a, 0x07060302u); }

#define T2REG 1
#define KPOL " nt"
#define VPOL " nt"
#define WPOL ""
#define VMWAIT(N) asm volatile("s_waitcnt vmcnt(" #N ")" ::: "memory")
#define LGKM0() asm volatile("s_waitcnt lgkmcnt(0)" ::: "memory")
#define BARRIER() asm volatile("s_waitcnt lgkmcnt(0)\n\ts_barrier" ::: "memory")

__device__ __forceinline__ void dma_half(const char* gbase, unsigned lds_dst, unsigned v0, unsigned v1, unsigned v2, unsigned v3) {
  unsigned keep;
  asm volatile(
      "s_mov_b32 %[keep], m0\n\ts_mov_b32 m0, %[l0]\n\ts_nop 2\n\t"
      "global_load_lds_dwordx4 %[v0], %[sb]" KPOL "\n\t"
      "global_load_lds_dwordx4 %[v1], %[sb] offset:1024" KPOL "\n\t"
      "global_load_lds_dwordx4 %[v2], %[sb] offset:2048" KPOL "\n\t"
      "global_load_lds_dwordx4 %[v3], %[sb] offset:3072" KPOL "\n\t"
      "s_mov_b32 m0, %[keep]"
      : [keep] "=&s"(keep)
      : [v0] "v"(v0), [v1] "v"(v1), [v2] "v"(v2), [v3] "v"(v3), [sb] "s"(gbase), [l0] "s"(lds_dst)
      : "memory");
}
__device__ __forceinline__ void dma1w(const char* sb, unsigned lds_dst, unsigned voff) {
  unsigned keep;
  asm volatile("s_mov_b32 %[keep], m0\n\ts_mov_b32 m0, %[l0]\n\ts_nop 2\n\tglobal_load_lds_dwordx4 %[v0], %[sb]" WPOL "\n\ts_mov_b32 m0, %[keep]"
               : [keep] "=&s"(keep) : [v0] "v"(voff), [sb] "s"(sb), [l0] "s"(lds_dst) : "memory");
}
__device__ __forceinline__ void dma1(const char* sb, unsigned lds_dst, unsigned voff) {
  unsigned keep;
  asm volatile("s_mov_b32 %[keep], m0\n\ts_mov_b32 m0, %[l0]\n\ts_nop 2\n\tglobal_load_lds_dwordx4 %[v0], %[sb]" VPOL "\n\ts_mov_b32 m0, %[keep]"
               : [keep] "=&s"(keep) : [v0] "v"(voff), [sb] "s"(sb), [l0] "s"(lds_dst) : "memory");
}
__device__ __forceinline__ void ld128_nt(v4u& d, unsigned voff, const char* sb) {
  asm volatile("global_load_dwordx4 %0, %1, %2" KPOL : "=v"(d) : "v"(voff), "s"(sb) : "memory");
}
__device__ __forceinline__ void ld64(v2u& d, unsigned voff, const char* sb) {
  asm volatile("global_load_dwordx2 %0, %1, %2" WPOL : "=v"(d) : "v"(voff), "s"(sb) : "memory");
}
__device__ __forceinline__ void ld128_dev(v4u& d, unsigned voff, const char* sb) {
  asm volatile("global_load_dwordx4 %0, %1, %2 sc1" : "=v"(d) : "v"(voff), "s"(sb) : "memory");
}
__device__ __forceinline__ void ld32_dev(unsigned& d, unsigned voff, const char* sb) {
  asm volatile("global_load_dword %0, %1, %2 sc1" : "=v"(d) : "v"(voff), "s"(sb) : "memory");
}
__device__ __forceinline__ void st128_dev(unsigned voff, v4u v, char* sb) {
  asm volatile("global_store_dwordx4 %0, %1, %2 sc1\n\ts_nop 1" : : "v"(voff), "v"(v), "s"(sb) : "memory");
}
__device__ __forceinline__ unsigned tags_bad(v4u u, unsigned T) { return (u.y ^ T) | (u.w ^ T); }
constexpr unsigned TMASK = 0x3FFu;
__device__ __forceinline__ unsigned enc(float v, unsigned T) { return ((__float_as_uint(v) + 0x200u) & ~TMASK) | (T & TMASK); }
__device__ __forceinline__ float dec(unsigned u) { return __uint_as_float(u & ~TMASK); }
__device__ __forceinline__ v4u enc4(f32x4 a, unsigned T) { return v4u{enc(a[0], T), enc(a[1], T), enc(a[2], T), enc(a[3], T)}; }
__device__ __forceinline__ f32x4 dec4(v4u u) { return f32x4{dec(u.x), dec(u.y), dec(u.z), dec(u.w)}; }
__device__ __forceinline__ unsigned quad_bad(v4u u, unsigned T) { return ((u.x ^ T) | (u.y ^ T) | (u.z ^ T) | (u.w ^ T)) & TMASK; }
template <class P> __device__ __forceinline__ P* uni(P* p) {
  const unsigned long long a = (unsigned long long)p;
  const unsigned lo = __builtin_amdgcn_readfirstlane((unsigned)a), hi = __builtin_amdgcn_readfirstlane((unsigned)(a >> 32));
  return (P*)(((unsigned long long)hi << 32) | lo);
}
__device__ __forceinline__ bool any_bad(unsigned bad) { return __builtin_amdgcn_ballot_w64(bad != 0u) != 0ull; }
__device__ __forceinline__ void reload1(v4u& d0, unsigned o0, const char* sb) {
  asm volatile("global_load_dwordx4 %0, %1, %2 sc1\n\ts_waitcnt vmcnt(0)" : "=&v"(d0) : "v"(o0), "s"(sb) : "memory");
}
__device__ __forceinline__ void reload2(v4u& d0, v4u& d1, unsigned o0, unsigned o1, const char* sb) {
  asm volatile("global_load_dwordx4 %0, %2, %4 sc1\n\tglobal_load_dwordx4 %1, %3, %4 sc1\n\ts_waitcnt vmcnt(0)"
               : "=&v"(d0), "=&v"(d1) : "v"(o0), "v"(o1), "s"(sb) : "memory");
}

__device__ __forceinline__ void reload4(v4u& d0, v4u& d1, v4u& d2, v4u& d3, unsigned o0, unsigned o1, unsigned o2, unsigned o3, const char* sb) {
  asm volatile("global_load_dwordx4 %0, %4, %8 sc1\n\tglobal_load_dwordx4 %1, %5, %8 sc1\n\t"
               "global_load_dwordx4 %2, %6, %8 sc1\n\tglobal_load_dwordx4 %3, %7, %8 sc1\n\ts_waitcnt vmcnt(0)"
               : "=&v"(d0), "=&v"(d1), "=&v"(d2), "=&v"(d3) : "v"(o0), "v"(o1), "v"(o2), "v"(o3), "s"(sb) : "memory");
}

#define STAMP(i) do { if ((i) == PROBE) { unsigned long long t_; asm volatile("s_memrealtime %0\n\ts_waitcnt lgkmcnt(0)" : "=s"(t_) :: "memory"); tprobe = t_; } } while (0)

__global__ void __launch_bounds__(512, 2)
attn_fused_kernel(const char* __restrict__ x, const char* __restrict__ kc, const char* __restrict__ vc,
                  const char* __restrict__ wq, const char* __restrict__ wo, unsigned* __restrict__ out, char* __restrict__ ws) {
  extern __shared__ __attribute__((aligned(16))) unsigned char lds_raw[];
  LAS unsigned char* L = (LAS unsigned char*)lds_raw;
  const unsigned lbase = (unsigned)(size_t)L;
  const unsigned long long tbeg = __builtin_amdgcn_s_memrealtime();
  unsigned long long tprobe = tbeg;
  STAMP(0);
  const int tid = threadIdx.x, lane = tid & 63, w = __builtin_amdgcn_readfirstlane(tid >> 6);
  const int g = lane >> 4, c = lane & 15;
  const int bid = blockIdx.x, kvh = bid & 7, s = (bid >> 3) & 3, b = bid >> 5;
  const int rot = 8 * s + b;
  const int rb = b >> 1, cb = (b & 1) * 4 + s;
  asm volatile("" :: "s"(x), "s"(kc), "s"(vc), "s"(wq), "s"(wo), "s"(out), "s"(ws));
  if (tid == 0) *(LAS unsigned*)(L + L_FLAG) = 0u;
  __builtin_amdgcn_s_setprio(3);

  unsigned ep;
  ld32_dev(ep, 0u, ws + W_EP + rb * 256);
  {
    const unsigned r8 = lane >> 3, f = ((r8 >> 1) & 1) | ((w & 1) << 1);
    const unsigned vo = r8 * (NQ * 2) + (((lane & 7) ^ (f << 1)) << 4);
    const char* wqb = wq + (size_t)(8 * w) * (NQ * 2) + (kvh * 512 + cb * 64) * 2;
#pragma unroll
    for (int i = 0; i < 7; ++i) dma1w(wqb + (size_t)(64 * i) * (NQ * 2), lbase + L_WQ + (8 * i + w) * 1024, vo);
    const char* xb = x + (size_t)(16 * rb) * (DM * 2);
#pragma unroll
    for (int i = 0; i < 2; ++i) {
      const int m = 8 * i + w;
      if (m < 14) {
        const unsigned p = 64 * m + lane, row = (p * 1171u) >> 16, ch = p - 56 * row;
        dma1w(xb, lbase + L_X + m * 1024, row * (DM * 2) + (((ch & ~7u) | ((ch & 7u) ^ ((row >> 1) & 7u))) << 4));
      }
    }
  }
  asm volatile("s_barrier" ::: "memory");
  const size_t headrow = (size_t)(b * HKV + kvh) * SK + (SK - WIN) + s * 512;
  const char* kwin = kc + headrow * 512;
  const char* vwin = vc + headrow * 512;
  unsigned ko[8];
  {
    const unsigned hi = lane >> 5, l31 = lane & 31;
    const unsigned laneK = (hi << 9) | ((l31 ^ hi) << 4);
#pragma unroll
    for (int q = 0; q < 8; ++q) ko[q] = (laneK ^ (unsigned)(q << 5)) + ((q >> 2) << 12);
  }
  auto tile_off = [&](int unit) -> size_t { return (size_t)((unit + rot) & 31) * 8192; };
  auto k_half = [&](int t, int h) {
    dma_half(kwin + tile_off(8 * t + w), lbase + L_RING + ((t & 1) * 8 + w) * 8192 + h * 4096, ko[4 * h], ko[4 * h + 1], ko[4 * h + 2], ko[4 * h + 3]);
  };
  k_half(0, 0);
  asm volatile("s_waitcnt vmcnt(4)" : "+v"(ep) :: "memory");
  const unsigned T = __builtin_amdgcn_readfirstlane(ep) + 1u;
  STAMP(1);
  BARRIER();
  char* qx = uni(ws + W_QX + (size_t)(kvh * 4 + rb) * (8 * 4096));
  {
    const int nt = w & 3, kh = w >> 2;
    const int q4 = (lane & 15) >> 2, p4 = lane & 3;
    const unsigned fa = (unsigned)(q4 >> 1) | ((unsigned)(g & 1) << 1);
    const unsigned ta = L_WQ + (224 * kh + 8 * g + q4) * 128 + (((2 * nt + (p4 >> 1)) ^ (fa << 1)) << 4) + (p4 & 1) * 8;
    f32x4 qa = {0.f, 0.f, 0.f, 0.f};
#pragma unroll
    for (int i = 0; i < 7; ++i) {
      const s16x4 a0 = __builtin_amdgcn_ds_read_tr16_b64_v4i16((LAS s16x4*)(L + ta + i * 4096));
      const s16x4 a1 = __builtin_amdgcn_ds_read_tr16_b64_v4i16((LAS s16x4*)(L + ta + i * 4096 + 512));
      const unsigned ch = 4 * (7 * kh + i) + g;
      const v4u xb = *(LAS v4u*)(L + L_X + c * 896 + (((ch & ~7u) | ((ch & 7u) ^ ((c >> 1) & 7u))) << 4));
      const v2u x0 = __builtin_bit_cast(v2u, a0), x1 = __builtin_bit_cast(v2u, a1);
      qa = mfma16(v4u{x0.x, x0.y, x1.x, x1.y}, xb, qa);
    }
    if (kh == 1) *(LAS f32x4*)(L + L_QR + nt * 1024 + lane * 16) = qa;
    STAMP(17);
    BARRIER();
    if (kh == 0) {
      qa += *(LAS f32x4*)(L + L_QR + nt * 1024 + lane * 16);
      st128_dev(cb * 4096 + nt * 1024 + lane * 16, v4u{pack_bf16(qa[0], qa[1]), T, pack_bf16(qa[2], qa[3]), T}, qx);
    }
  }
  STAMP(18);
  k_half(0, 1);
  k_half(1, 0);
  if (w < 4) {
    v4u qu[4];
    const unsigned qgo = w * 4096 + (lane >> 5) * 1024 + (16 * ((lane >> 3) & 3) + 8 * (b & 1) + (lane & 7)) * 16;
    STAMP(19);
    reload4(qu[0], qu[1], qu[2], qu[3], qgo, qgo + 2048, qgo + 16384, qgo + 18432, qx);
    STAMP(3);
    for (unsigned spin = 0; any_bad(tags_bad(qu[0], T) | tags_bad(qu[1], T)) && spin < SPIN_CAP; ++spin) reload2(qu[0], qu[1], qgo, qgo + 2048, qx);
    for (unsigned spin = 0; any_bad(tags_bad(qu[2], T) | tags_bad(qu[3], T)) && spin < SPIN_CAP; ++spin) reload2(qu[2], qu[3], qgo + 16384, qgo + 18432, qx);
    const int gq = (lane >> 3) & 3, q = lane & 7;
#pragma unroll
    for (int u = 0; u < 4; ++u) {
      const int nt = 2 * (u & 1) + (lane >> 5);
      const int st = w * 2 + (nt >> 1), gk = 2 * (nt & 1) + (gq >> 1);
      *(LAS v2u*)(L + L_QF + st * 1024 + (16 * gk + 8 * (u >> 1) + q) * 16 + (gq & 1) * 8) = v2u{qu[u].x, qu[u].z};
    }
    LGKM0();
    if (lane == 0) __hip_atomic_fetch_add((LAS unsigned*)(L + L_FLAG), 1u, __ATOMIC_RELAXED, __HIP_MEMORY_SCOPE_WORKGROUP);
  }
  STAMP(4);
  k_half(1, 1);
  v4u kreg[8];
  {
    const char* t2b = kwin + tile_off(16 + w);
#pragma unroll
    for (int q = 0; q < 8; ++q) ld128_nt(kreg[q], q * 1024 + lane * 16, t2b);
  }
  STAMP(2);
  for (unsigned spin = 0; *(volatile LAS unsigned*)(L + L_FLAG) < 4u && spin < (SPIN_CAP << 6); ++spin) __builtin_amdgcn_s_sleep(1);
  asm volatile("" ::: "memory");
  VMWAIT(16);
  v4u qf[8];
#pragma unroll
  for (int st = 0; st < 8; ++st) qf[st] = *(LAS v4u*)(L + L_QF + st * 1024 + lane * 16);

  f32x4 sc[4];
  auto qk_tile = [&](int slot) -> f32x4 {
    v4u kf[8];
#pragma unroll
    for (int st = 0; st < 8; ++st) kf[st] = *(LAS v4u*)(L + L_RING + slot * 8192 + c * 512 + (((4 * st + g) ^ c) << 4));
    f32x4 a = {0.f, 0.f, 0.f, 0.f};
#pragma unroll
    for (int st = 0; st < 8; ++st) a = mfma16(kf[st], qf[st], a);
    return a;
  };
  const unsigned vlane = (lane >> 3) * 512 + (((lane & 7) ^ (((lane >> 4) & 3) << 1)) << 4);
  auto v_piece = [&](int p, int q) {
    dma1(vwin + tile_off(4 * w + (q >> 1)) + (q & 1) * 4096 + ((s + 1 + p) & 3) * 128,
         lbase + L_RING + ((p & 1) * 8 + w) * 8192 + q * 1024, vlane);
  };
  const int wc = w < 7 ? w : 6;
  v2u wv[4][8];
  const char* wob = wo + (size_t)(2 * kvh * 256 + 64 * s) * (DM * 2) + wc * 128;
  const unsigned wvo = (unsigned)(8 * g) * (DM * 2) + c * 8;
  auto wo_load = [&](int ks, int i) { ld64(wv[ks][i], wvo + (unsigned)(256 * (ks >> 1) + 32 * (ks & 1) + i) * (DM * 2), wob); };

  auto wo_loads = [&](int n0, int n1) {
#pragma unroll
    for (int n = n0; n < n1; ++n) wo_load(n >> 3, n & 7);
  };
  auto v_half = [&](int p, int h) {
#pragma unroll
    for (int q = 4 * h; q < 4 * h + 4; ++q) v_piece(p, q);
  };
#define PRIO(n) __builtin_amdgcn_s_setprio(n)
  sc[0] = qk_tile(w);
  VMWAIT(8);
  sc[1] = qk_tile(8 + w);
  LGKM0();
  PRIO(2);
  wo_loads(0, 4); k_half(3, 0); wo_loads(4, 7);
  asm volatile("s_waitcnt vmcnt(11)"
               : "+v"(kreg[0]), "+v"(kreg[1]), "+v"(kreg[2]), "+v"(kreg[3]), "+v"(kreg[4]), "+v"(kreg[5]), "+v"(kreg[6]), "+v"(kreg[7]) :: "memory");
#pragma unroll
  for (int q = 0; q < 8; ++q) *(LAS v4u*)(L + L_RING + w * 8192 + ko[q] + (q & 3) * 1024) = kreg[q];
  sc[2] = qk_tile(w);
  LGKM0();
  PRIO(1);
  wo_loads(7, 10); k_half(3, 1); wo_loads(10, 13); v_half(0, 0);
  VMWAIT(7);
  sc[3] = qk_tile(8 + w);
  LGKM0();
  PRIO(0);
  wo_loads(13, 16); v_half(0, 1);
  STAMP(5);
  float mx = fmaxf(fmaxf(fmaxf(sc[0][0], sc[0][1]), fmaxf(sc[0][2], sc[0][3])), fmaxf(fmaxf(sc[1][0], sc[1][1]), fmaxf(sc[1][2], sc[1][3])));
  mx = fmaxf(mx, fmaxf(fmaxf(fmaxf(sc[2][0], sc[2][1]), fmaxf(sc[2][2], sc[2][3])), fmaxf(fmaxf(sc[3][0], sc[3][1]), fmaxf(sc[3][2], sc[3][3]))));
  mx = xg_max(mx);
  if (g == 0) *(LAS float*)(L + L_MX + (w * 16 + c) * 4) = mx;
  BARRIER();
  float mrow = *(LAS float*)(L + L_MX + c * 4);
#pragma unroll
  for (int ww = 1; ww < 8; ++ww) mrow = fmaxf(mrow, *(LAS float*)(L + L_MX + (ww * 16 + c) * 4));
  mrow *= C2;
  float lsum = 0.f;
#pragma unroll
  for (int t = 0; t < 4; ++t) {
    float p[4];
#pragma unroll
    for (int r = 0; r < 4; ++r) { p[r] = __builtin_amdgcn_exp2f(fmaf(sc[t][r], C2, -mrow)); lsum += p[r]; }
    *(LAS v2u*)(L + L_P + (4 * t + (w >> 1)) * 1024 + lane * 16 + (w & 1) * 8) = v2u{pack_bf16(p[0], p[1]), pack_bf16(p[2], p[3])};
  }
  lsum = xg_sum(lsum);
  if (g == 0) *(LAS float*)(L + L_LS + (w * 16 + c) * 4) = lsum;
  wo_loads(16, 20); v_half(1, 0);

  const int ct = w & 3, kh = w >> 2;
  unsigned tra;
  {
    const int q4 = (lane & 15) >> 2, p4 = lane & 3;
    tra = (4 * g + q4) * 128 + (((2 * ct + (p4 >> 1)) ^ (((2 * g + (q4 >> 1)) & 3) << 1)) << 4) + (p4 & 1) * 8 + kh * 32768;
  }
  auto pv_pass = [&](int p) -> f32x4 {
    f32x4 a = {0.f, 0.f, 0.f, 0.f};
#pragma unroll
    for (int i = 0; i < 8; ++i) {
      const v4u pf = *(LAS v4u*)(L + L_P + (8 * kh + i) * 1024 + lane * 16);
      const int ub = L_RING + (p & 1) * 65536 + i * 4096;
      const s16x4 a0 = __builtin_amdgcn_ds_read_tr16_b64_v4i16((LAS s16x4*)(L + ub + tra));
      const s16x4 a1 = __builtin_amdgcn_ds_read_tr16_b64_v4i16((LAS s16x4*)(L + ub + 2048 + tra));
      const v2u x0 = __builtin_bit_cast(v2u, a0), x1 = __builtin_bit_cast(v2u, a1);
      a = mfma16(v4u{x0.x, x0.y, x1.x, x1.y}, pf, a);
    }
    return a;
  };
  const int bh = b * HKV + kvh;
  char* stb = uni(ws + W_ST + (size_t)bh * 1024);
  char* h1 = uni(ws + W_H1 + (size_t)bh * (16 * 4096));
  auto reduce_pass = [&](f32x4 a) -> f32x4 {
    if (kh == 1) *(LAS f32x4*)(L + L_HALF + ct * 1024 + lane * 16) = a;
    BARRIER();
    if (kh == 0) a += *(LAS f32x4*)(L + L_HALF + ct * 1024 + lane * 16);
    return a;
  };
  auto publish_pass = [&](int p, f32x4 a) {
    if (kh == 0) st128_dev(((((s + 1 + p) & 3) * 4 + s) * 4 + ct) * 1024 + lane * 16, enc4(a, T), h1);
  };
  STAMP(6);
  VMWAIT(8);
  BARRIER();
  STAMP(7);
  {
    const f32x4 a = reduce_pass(pv_pass(0));
    wo_loads(20, 23); v_half(1, 1);
    asm volatile("s_barrier" ::: "memory");
    wo_loads(23, 26); v_half(2, 0);
    publish_pass(0, a);
    if (w == 7) {
      float lt = *(LAS float*)(L + L_LS + c * 4);
#pragma unroll
      for (int ww = 1; ww < 8; ++ww) lt += *(LAS float*)(L + L_LS + (ww * 16 + c) * 4);
      if (g == 0) st128_dev((s * 16 + c) * 16, v4u{__float_as_uint(mrow), T, __float_as_uint(lt), T}, stb);
    }
    if (kh == 0 || w == 7) VMWAIT(8); else VMWAIT(7);
  }
  BARRIER();
  v4u stu;
  {
    const f32x4 a = reduce_pass(pv_pass(1));
    wo_loads(26, 32); v_half(2, 1);
    asm volatile("s_barrier" ::: "memory");
    v_half(3, 0);
    publish_pass(1, a);
  }
  ld128_dev(stu, lane * 16, stb);
  if (kh == 0) VMWAIT(6); else VMWAIT(5);
  asm volatile(""
               : "+v"(wv[0][0]), "+v"(wv[0][1]), "+v"(wv[0][2]), "+v"(wv[0][3]), "+v"(wv[0][4]), "+v"(wv[0][5]), "+v"(wv[0][6]), "+v"(wv[0][7]),
                 "+v"(wv[1][0]), "+v"(wv[1][1]), "+v"(wv[1][2]), "+v"(wv[1][3]), "+v"(wv[1][4]), "+v"(wv[1][5]), "+v"(wv[1][6]), "+v"(wv[1][7])
               :: "memory");
  asm volatile("" : "+v"(wv[2][0]), "+v"(wv[2][1]), "+v"(wv[2][2]), "+v"(wv[2][3]), "+v"(wv[2][4]), "+v"(wv[2][5]), "+v"(wv[2][6]), "+v"(wv[2][7]),
                    "+v"(wv[3][0]), "+v"(wv[3][1]), "+v"(wv[3][2]), "+v"(wv[3][3]), "+v"(wv[3][4]), "+v"(wv[3][5]), "+v"(wv[3][6]), "+v"(wv[3][7])
               :: "memory");
  BARRIER();
  publish_pass(2, reduce_pass(pv_pass(2)));
  v_half(3, 1);
  v4u hu[2];
  const int srcA = (s + (kh ? 3 : 1)) & 3, srcB = (s + (kh ? 3 : 2)) & 3;
  const unsigned hoA = ((s * 4 + srcA) * 4 + ct) * 1024 + lane * 16, hoB = ((s * 4 + srcB) * 4 + ct) * 1024 + lane * 16;
  ld128_dev(hu[0], hoA, h1);
  ld128_dev(hu[1], hoB, h1);
  v4u wf[4][4];
#pragma unroll
  for (int ks = 0; ks < 4; ++ks)
#pragma unroll
    for (int jj = 0; jj < 4; ++jj)
#pragma unroll
      for (int r = 0; r < 4; ++r) {
        const unsigned lo = wv[ks][2 * r][jj >> 1], hi = wv[ks][2 * r + 1][jj >> 1];
        wf[ks][jj][r] = (jj & 1) ? perm_hi(lo, hi) : perm_lo(lo, hi);
      }
  const int j32 = kvh * 4 + s;
  char* h2 = uni(ws + W_H2 + (size_t)b * (32 * 8 * 112 * 16));
  unsigned h2st = ((j32 * 8 + 4 * (g & 1) + 2 * (g >> 1)) * 112 + wc * 16 + c) * 16;
  const int l31 = lane & 31;
  unsigned h2ld = (((4 * w + (lane >> 5)) * 8 + (j32 >> 2)) * 112 + 28 * (j32 & 3) + (l31 < 28 ? l31 : 27)) * 16;
  unsigned obw = L_OB + c * 144 + (16 * ct + 4 * g) * 2, obr = L_OB + (c & 7) * 144 + g * 16, hlf = L_HALF + ct * 1024 + lane * 16;
  asm volatile("" : "+v"(wf[0][0]), "+v"(wf[0][1]), "+v"(wf[0][2]), "+v"(wf[0][3]), "+v"(wf[1][0]), "+v"(wf[1][1]), "+v"(wf[1][2]), "+v"(wf[1][3]),
                    "+v"(wf[2][0]), "+v"(wf[2][1]), "+v"(wf[2][2]), "+v"(wf[2][3]), "+v"(wf[3][0]), "+v"(wf[3][1]), "+v"(wf[3][2]), "+v"(wf[3][3]),
                    "+v"(h2st), "+v"(h2ld), "+v"(obw), "+v"(obr), "+v"(hlf));
  asm volatile("s_waitcnt vmcnt(6)" : "+v"(stu) :: "memory");
  for (unsigned spin = 0; any_bad(tags_bad(stu, T)) && spin < SPIN_CAP; ++spin) reload1(stu, lane * 16, stb);
  float fS, fA, fB;
  {
    const float ms = __uint_as_float(stu.x), ls = __uint_as_float(stu.z);
    const float M = xg_max(ms);
    const float e = __builtin_amdgcn_exp2f(ms - M);
    const float fw = e / xg_sum(e * ls);
    fS = __shfl(fw, 16 * s + c); fA = __shfl(fw, 16 * srcA + c); fB = kh ? 0.f : __shfl(fw, 16 * srcB + c);
  }
  asm volatile("s_waitcnt vmcnt(0)" : "+v"(hu[0]), "+v"(hu[1]) :: "memory");
  STAMP(8);
  BARRIER();
  f32x4 own = pv_pass(3);
  STAMP(9);
  for (unsigned spin = 0; any_bad(quad_bad(hu[0], T) | quad_bad(hu[1], T)) && spin < SPIN_CAP; ++spin) reload2(hu[0], hu[1], hoA, hoB, h1);
  STAMP(11);
  {
    own = own * fS + dec4(hu[0]) * fA + dec4(hu[1]) * fB;
    if (kh == 1) *(LAS f32x4*)(L + hlf) = own;
    BARRIER();
    if (kh == 0) {
      own += *(LAS f32x4*)(L + hlf);
      *(LAS v2u*)(L + obw) = v2u{pack_bf16(own[0], own[1]), pack_bf16(own[2], own[3])};
    }
  }
  BARRIER();

  f32x4 pa[4];
#pragma unroll
  for (int jj = 0; jj < 4; ++jj) pa[jj] = f32x4{0.f, 0.f, 0.f, 0.f};
#pragma unroll
  for (int ks = 0; ks < 4; ++ks) {
    const v4u af = *(LAS v4u*)(L + obr + 8 * (ks >> 1) * 144 + 64 * (ks & 1));
#pragma unroll
    for (int jj = 0; jj < 4; ++jj) pa[jj] = mfma16(af, wf[ks][jj], pa[jj]);
  }
  if (w < 7) {
#pragma unroll
    for (int rr = 0; rr < 2; ++rr) {
      const f32x4 v = (g >> 1) ? f32x4{pa[0][2 + rr], pa[1][2 + rr], pa[2][2 + rr], pa[3][2 + rr]} : f32x4{pa[0][rr], pa[1][rr], pa[2][rr], pa[3][rr]};
      st128_dev(h2st + rr * (112 * 16), enc4(v, T), h2);
    }
  }
  STAMP(12);
  {
    v4u fu[2];
    const unsigned fo[2] = {h2ld, h2ld + 2 * 8 * 112 * 16};
    ld128_dev(fu[0], fo[0], h2);
    ld128_dev(fu[1], fo[1], h2);
    asm volatile("s_waitcnt vmcnt(0)" : "+v"(fu[0]), "+v"(fu[1]) :: "memory");
    for (unsigned spin = 0; any_bad(quad_bad(fu[0], T) | quad_bad(fu[1], T)) && spin < SPIN_CAP; ++spin) reload2(fu[0], fu[1], fo[0], fo[1], h2);
    STAMP(13);
    *(LAS f32x4*)(L + L_RED + w * 1024 + lane * 16) = dec4(fu[0]) + dec4(fu[1]);
  }
  BARRIER();
  if (w == 0) {
    const int hf = lane >> 5;
    f32x4 t = *(LAS f32x4*)(L + L_RED + (4 * hf) * 1024 + l31 * 16);
    t += *(LAS f32x4*)(L + L_RED + (4 * hf) * 1024 + 512 + l31 * 16);
#pragma unroll
    for (int ww = 1; ww < 4; ++ww) {
      t += *(LAS f32x4*)(L + L_RED + (4 * hf + ww) * 1024 + l31 * 16);
      t += *(LAS f32x4*)(L + L_RED + (4 * hf + ww) * 1024 + 512 + l31 * 16);
    }
#pragma unroll
    for (int e = 0; e < 4; ++e) {
      auto sw = __builtin_amdgcn_permlane32_swap(__float_as_uint(t[e]), __float_as_uint(t[e]), false, false);
      t[e] = __uint_as_float(sw[0]) + __uint_as_float(sw[1]);
    }
    if (lane < 28)
      *(v2u*)(out + (((b * SQ + (j32 >> 2)) * DM + 112 * (j32 & 3) + 4 * lane) >> 1)) = v2u{pack_bf16(t[0], t[1]), pack_bf16(t[2], t[3])};
  }
  if (w == 1 && (b & 1) == 0 && j32 == 0 && lane == 0) *(unsigned*)(ws + W_EP + rb * 256) = T;
  STAMP(14);
  if (PROBEWG < 0 || bid == PROBEWG) {
    const unsigned long long until = tbeg + 6000ull + 4ull * (tprobe - tbeg);
    while (__builtin_amdgcn_s_memrealtime() < until) __builtin_amdgcn_s_sleep(4);
  }
}

}

extern "C" void kernel_launch(void* const* d_in, const int* in_sizes, int n_in, void* d_out, int out_size, void* d_ws,
                              size_t ws_size, hipStream_t stream) {
  (void)in_sizes; (void)n_in; (void)out_size;
  if (ws_size < W_END) return;
  (void)hipFuncSetAttribute((const void*)attn_fused_kernel, hipFuncAttributeMaxDynamicSharedMemorySize, LDS_BYTES);
  attn_fused_kernel<<<dim3(256), dim3(512), LDS_BYTES, stream>>>((const char*)d_in[0], (const char*)d_in[1], (const char*)d_in[2],
                                                                 (const char*)d_in[3], (const char*)d_in[4], (unsigned*)d_out, (char*)d_ws);
}
```

```cpp
#include <hip/hip_runtime.h>
#include <hip/hip_bf16.h>

namespace {

typedef unsigned v4u __attribute__((ext_vector_type(4)));
typedef unsigned v2u __attribute__((ext_vector_type(2)));
typedef float f32x4 __attribute__((ext_vector_type(4)));
typedef float f32x2 __attribute__((ext_vector_type(2)));
typedef __bf16 bf16x8 __attribute__((ext_vector_type(8)));
typedef __bf16 bf16x2 __attribute__((ext_vector_type(2)));
typedef short s16x4 __attribute__((ext_vector_type(4)));
#define LAS __attribute__((address_space(3)))

constexpr int NB = 8, SQ = 8, DM = 448, HKV = 8, SK = 8192, WIN = 2048;
constexpr int NQ = 4096;
constexpr float C2 = 0.0625f * 1.4426950408889634f;
constexpr unsigned SPIN_CAP = 1u << 18;

constexpr int L_RING = 0;
constexpr int L_WQ = L_RING + 65536;
constexpr int L_P = 131072;
constexpr int L_X = L_P;
constexpr int L_QF = L_P + 16384;
constexpr int L_HALF = L_QF;
constexpr int L_MX = L_QF + 8192;
constexpr int L_LS = L_MX + 512;
constexpr int L_QR = L_LS + 512;
constexpr int L_FLAG = L_QR + 4096;
constexpr int L_OB = L_FLAG + 16;
constexpr int L_RED = L_P;
constexpr int LDS_BYTES = 163840;
static_assert(L_OB + 16 * 144 <= LDS_BYTES, "LDS map");

constexpr size_t W_EP = 0;
constexpr size_t W_QX = 4096;
constexpr size_t W_ST = W_QX + (size_t)8 * 4 * 8 * 4096;
constexpr size_t W_H1 = W_ST + (size_t)64 * 4 * 256;
constexpr size_t W_H2 = W_H1 + (size_t)64 * 16 * 4096;
constexpr size_t W_END = W_H2 + (size_t)8 * 32 * 8 * 112 * 16;

__device__ __forceinline__ f32x4 mfma16(v4u a, v4u b, f32x4 c) {
  return __builtin_amdgcn_mfma_f32_16x16x32_bf16(__builtin_bit_cast(bf16x8, a), __builtin_bit_cast(bf16x8, b), c, 0, 0, 0);
}
__device__ __forceinline__ unsigned pack_bf16(float lo, float hi) {
  bf16x2 r; r[0] = (__bf16)lo; r[1] = (__bf16)hi;
  return __builtin_bit_cast(unsigned, r);
}
__device__ __forceinline__ float xg_max(float x) {
  auto s = __builtin_amdgcn_permlane16_swap(__float_as_uint(x), __float_as_uint(x), false, false);
  x = fmaxf(__uint_as_float(s[0]), __uint_as_float(s[1]));
  auto t = __builtin_amdgcn_permlane32_swap(__float_as_uint(x), __float_as_uint(x), false, false);
  return fmaxf(__uint_as_float(t[0]), __uint_as_float(t[1]));
}
__device__ __forceinline__ float xg_sum(float x) {
  auto s = __builtin_amdgcn_permlane16_swap(__float_as_uint(x), __float_as_uint(x), false, false);
  x = __uint_as_float(s[0]) + __uint_as_float(s[1]);
  auto t = __builtin_amdgcn_permlane32_swap(__float_as_uint(x), __float_as_uint(x), false, false);
  return __uint_as_float(t[0]) + __uint_as_float(t[1]);
}
__device__ __forceinline__ unsigned perm_lo(unsigned a, unsigned b) { return __builtin_amdgcn_perm(b, a, 0x05040100u); }
__device__ __forceinline__ unsigned perm_hi(unsigned a, unsigned b) { return __builtin_amdgcn_perm(b, a, 0x07060302u); }

#define T2REG 1
#define KPOL " nt"
#define VPOL " nt"
#define WPOL ""
#define VMWAIT(N) asm volatile("s_waitcnt vmcnt(" #N ")" ::: "memory")
#define LGKM0() asm volatile("s_waitcnt lgkmcnt(0)" ::: "memory")
#define BARRIER() asm volatile("s_waitcnt lgkmcnt(0)\n\ts_barrier" ::: "memory")

__device__ __forceinline__ void dma_half(const char* gbase, unsigned lds_dst, unsigned v0, unsigned v1, unsigned v2, unsigned v3) {
  unsigned keep;
  asm volatile(
      "s_mov_b32 %[keep], m0\n\ts_mov_b32 m0, %[l0]\n\ts_nop 2\n\t"
      "global_load_lds_dwordx4 %[v0], %[sb]" KPOL "\n\t"
      "global_load_lds_dwordx4 %[v1], %[sb] offset:1024" KPOL "\n\t"
      "global_load_lds_dwordx4 %[v2], %[sb] offset:2048" KPOL "\n\t"
      "global_load_lds_dwordx4 %[v3], %[sb] offset:3072" KPOL "\n\t"
      "s_mov_b32 m0, %[keep]"
      : [keep] "=&s"(keep)
      : [v0] "v"(v0), [v1] "v"(v1), [v2] "v"(v2), [v3] "v"(v3), [sb] "s"(gbase), [l0] "s"(lds_dst)
      : "memory");
}
__device__ __forceinline__ void dma1w(const char* sb, unsigned lds_dst, unsigned voff) {
  unsigned keep;
  asm volatile("s_mov_b32 %[keep], m0\n\ts_mov_b32 m0, %[l0]\n\ts_nop 2\n\tglobal_load_lds_dwordx4 %[v0], %[sb]" WPOL "\n\ts_mov_b32 m0, %[keep]"
               : [keep] "=&s"(keep) : [v0] "v"(voff), [sb] "s"(sb), [l0] "s"(lds_dst) : "memory");
}
__device__ __forceinline__ void dma1(const char* sb, unsigned lds_dst, unsigned voff) {
  unsigned keep;
  asm volatile("s_mov_b32 %[keep], m0\n\ts_mov_b32 m0, %[l0]\n\ts_nop 2\n\tglobal_load_lds_dwordx4 %[v0], %[sb]" VPOL "\n\ts_mov_b32 m0, %[keep]"
               : [keep] "=&s"(keep) : [v0] "v"(voff), [sb] "s"(sb), [l0] "s"(lds_dst) : "memory");
}
__device__ __forceinline__ void ld128_nt(v4u& d, unsigned voff, const char* sb) {
  asm volatile("global_load_dwordx4 %0, %1, %2" KPOL : "=v"(d) : "v"(voff), "s"(sb) : "memory");
}
__device__ __forceinline__ void ld64(v2u& d, unsigned voff, const char* sb) {
  asm volatile("global_load_dwordx2 %0, %1, %2" WPOL : "=v"(d) : "v"(voff), "s"(sb) : "memory");
}
__device__ __forceinline__ void ld128_dev(v4u& d, unsigned voff, const char* sb) {
  asm volatile("global_load_dwordx4 %0, %1, %2 sc1" : "=v"(d) : "v"(voff), "s"(sb) : "memory");
}
__device__ __forceinline__ void ld32_dev(unsigned& d, unsigned voff, const char* sb) {
  asm volatile("global_load_dword %0, %1, %2 sc1" : "=v"(d) : "v"(voff), "s"(sb) : "memory");
}
__device__ __forceinline__ void st128_dev(unsigned voff, v4u v, char* sb) {
  asm volatile("global_store_dwordx4 %0, %1, %2 sc1\n\ts_nop 1" : : "v"(voff), "v"(v), "s"(sb) : "memory");
}
__device__ __forceinline__ unsigned tags_bad(v4u u, unsigned T) { return (u.y ^ T) | (u.w ^ T); }
constexpr unsigned TMASK = 0x3FFu;
__device__ __forceinline__ unsigned enc(float v, unsigned T) { return ((__float_as_uint(v) + 0x200u) & ~TMASK) | (T & TMASK); }
__device__ __forceinline__ float dec(unsigned u) { return __uint_as_float(u & ~TMASK); }
__device__ __forceinline__ v4u enc4(f32x4 a, unsigned T) { return v4u{enc(a[0], T), enc(a[1], T), enc(a[2], T), enc(a[3], T)}; }
__device__ __forceinline__ f32x4 dec4(v4u u) { return f32x4{dec(u.x), dec(u.y), dec(u.z), dec(u.w)}; }
__device__ __forceinline__ unsigned quad_bad(v4u u, unsigned T) { return ((u.x ^ T) | (u.y ^ T) | (u.z ^ T) | (u.w ^ T)) & TMASK; }
template <class P> __device__ __forceinline__ P* uni(P* p) {
  const unsigned long long a = (unsigned long long)p;
  const unsigned lo = __builtin_amdgcn_readfirstlane((unsigned)a), hi = __builtin_amdgcn_readfirstlane((unsigned)(a >> 32));
  return (P*)(((unsigned long long)hi << 32) | lo);
}
__device__ __forceinline__ bool any_bad(unsigned bad) { return __builtin_amdgcn_ballot_w64(bad != 0u) != 0ull; }
__device__ __forceinline__ void reload1(v4u& d0, unsigned o0, const char* sb) {
  asm volatile("global_load_dwordx4 %0, %1, %2 sc1\n\ts_waitcnt vmcnt(0)" : "=&v"(d0) : "v"(o0), "s"(sb) : "memory");
}
__device__ __forceinline__ void reload2(v4u& d0, v4u& d1, unsigned o0, unsigned o1, const char* sb) {
  asm volatile("global_load_dwordx4 %0, %2, %4 sc1\n\tglobal_load_dwordx4 %1, %3, %4 sc1\n\ts_waitcnt vmcnt(0)"
               : "=&v"(d0), "=&v"(d1) : "v"(o0), "v"(o1), "s"(sb) : "memory");
}

__device__ __forceinline__ void reload4(v4u& d0, v4u& d1, v4u& d2, v4u& d3, unsigned o0, unsigned o1, unsigned o2, unsigned o3, const char* sb) {
  asm volatile("global_load_dwordx4 %0, %4, %8 sc1\n\tglobal_load_dwordx4 %1, %5, %8 sc1\n\t"
               "global_load_dwordx4 %2, %6, %8 sc1\n\tglobal_load_dwordx4 %3, %7, %8 sc1\n\ts_waitcnt vmcnt(0)"
               : "=&v"(d0), "=&v"(d1), "=&v"(d2), "=&v"(d3) : "v"(o0), "v"(o1), "v"(o2), "v"(o3), "s"(sb) : "memory");
}

#define STAMP(i) do { } while (0)

__global__ void __launch_bounds__(512, 2)
attn_fused_kernel(const char* __restrict__ x, const char* __restrict__ kc, const char* __restrict__ vc,
                  const char* __restrict__ wq, const char* __restrict__ wo, unsigned* __restrict__ out, char* __restrict__ ws) {
  extern __shared__ __attribute__((aligned(16))) unsigned char lds_raw[];
  LAS unsigned char* L = (LAS unsigned char*)lds_raw;
  const unsigned lbase = (unsigned)(size_t)L;
  STAMP(0);
  const int tid = threadIdx.x, lane = tid & 63, w = __builtin_amdgcn_readfirstlane(tid >> 6);
  const int g = lane >> 4, c = lane & 15;
  const int bid = blockIdx.x, kvh = bid & 7, s = (bid >> 3) & 3, b = bid >> 5;
  const int rot = 8 * s + b;
  const int rb = b >> 1, cb = (b & 1) * 4 + s;
  asm volatile("" :: "s"(x), "s"(kc), "s"(vc), "s"(wq), "s"(wo), "s"(out), "s"(ws));
  if (tid == 0) *(LAS unsigned*)(L + L_FLAG) = 0u;
  __builtin_amdgcn_s_setprio(3);

  unsigned ep;
  ld32_dev(ep, 0u, ws + W_EP + rb * 256);
  {
    const unsigned r8 = lane >> 3, f = ((r8 >> 1) & 1) | ((w & 1) << 1);
    const unsigned vo = r8 * (NQ * 2) + (((lane & 7) ^ (f << 1)) << 4);
    const char* wqb = wq + (size_t)(8 * w) * (NQ * 2) + (kvh * 512 + cb * 64) * 2;
#pragma unroll
    for (int i = 0; i < 7; ++i) dma1w(wqb + (size_t)(64 * i) * (NQ * 2), lbase + L_WQ + (8 * i + w) * 1024, vo);
    const char* xb = x + (size_t)(16 * rb) * (DM * 2);
#pragma unroll
    for (int i = 0; i < 2; ++i) {
      const int m = 8 * i + w;
      if (m < 14) {
        const unsigned p = 64 * m + lane, row = (p * 1171u) >> 16, ch = p - 56 * row;
        dma1w(xb, lbase + L_X + m * 1024, row * (DM * 2) + (((ch & ~7u) | ((ch & 7u) ^ ((row >> 1) & 7u))) << 4));
      }
    }
  }
  asm volatile("s_barrier" ::: "memory");
  const size_t headrow = (size_t)(b * HKV + kvh) * SK + (SK - WIN) + s * 512;
  const char* kwin = kc + headrow * 512;
  const char* vwin = vc + headrow * 512;
  unsigned ko[8];
  {
    const unsigned hi = lane >> 5, l31 = lane & 31;
    const unsigned laneK = (hi << 9) | ((l31 ^ hi) << 4);
#pragma unroll
    for (int q = 0; q < 8; ++q) ko[q] = (laneK ^ (unsigned)(q << 5)) + ((q >> 2) << 12);
  }
  auto tile_off = [&](int unit) -> size_t { return (size_t)((unit + rot) & 31) * 8192; };
  auto k_half = [&](int t, int h) {
    dma_half(kwin + tile_off(8 * t + w), lbase + L_RING + ((t & 1) * 8 + w) * 8192 + h * 4096, ko[4 * h], ko[4 * h + 1], ko[4 * h + 2], ko[4 * h + 3]);
  };
  k_half(0, 0);
  asm volatile("s_waitcnt vmcnt(4)" : "+v"(ep) :: "memory");
  const unsigned T = __builtin_amdgcn_readfirstlane(ep) + 1u;
  STAMP(1);
  BARRIER();
  char* qx = uni(ws + W_QX + (size_t)(kvh * 4 + rb) * (8 * 4096));
  {
    const int nt = w & 3, kh = w >> 2;
    const int q4 = (lane & 15) >> 2, p4 = lane & 3;
    const unsigned fa = (unsigned)(q4 >> 1) | ((unsigned)(g & 1) << 1);
    const unsigned ta = L_WQ + (224 * kh + 8 * g + q4) * 128 + (((2 * nt + (p4 >> 1)) ^ (fa << 1)) << 4) + (p4 & 1) * 8;
    f32x4 qa = {0.f, 0.f, 0.f, 0.f};
#pragma unroll
    for (int i = 0; i < 7; ++i) {
      const s16x4 a0 = __builtin_amdgcn_ds_read_tr16_b64_v4i16((LAS s16x4*)(L + ta + i * 4096));
      const s16x4 a1 = __builtin_amdgcn_ds_read_tr16_b64_v4i16((LAS s16x4*)(L + ta + i * 4096 + 512));
      const unsigned ch = 4 * (7 * kh + i) + g;
      const v4u xb = *(LAS v4u*)(L + L_X + c * 896 + (((ch & ~7u) | ((ch & 7u) ^ ((c >> 1) & 7u))) << 4));
      const v2u x0 = __builtin_bit_cast(v2u, a0), x1 = __builtin_bit_cast(v2u, a1);
      qa = mfma16(v4u{x0.x, x0.y, x1.x, x1.y}, xb, qa);
    }
    if (kh == 1) *(LAS f32x4*)(L + L_QR + nt * 1024 + lane * 16) = qa;
    STAMP(17);
    BARRIER();
    if (kh == 0) {
      qa += *(LAS f32x4*)(L + L_QR + nt * 1024 + lane * 16);
      st128_dev(cb * 4096 + nt * 1024 + lane * 16, v4u{pack_bf16(qa[0], qa[1]), T, pack_bf16(qa[2], qa[3]), T}, qx);
    }
  }
  STAMP(18);
  k_half(0, 1);
  k_half(1, 0);
  if (w < 4) {
    v4u qu[4];
    const unsigned qgo = w * 4096 + (lane >> 5) * 1024 + (16 * ((lane >> 3) & 3) + 8 * (b & 1) + (lane & 7)) * 16;
    STAMP(19);
    reload4(qu[0], qu[1], qu[2], qu[3], qgo, qgo + 2048, qgo + 16384, qgo + 18432, qx);
    STAMP(3);
    for (unsigned spin = 0; any_bad(tags_bad(qu[0], T) | tags_bad(qu[1], T)) && spin < SPIN_CAP; ++spin) reload2(qu[0], qu[1], qgo, qgo + 2048, qx);
    for (unsigned spin = 0; any_bad(tags_bad(qu[2], T) | tags_bad(qu[3], T)) && spin < SPIN_CAP; ++spin) reload2(qu[2], qu[3], qgo + 16384, qgo + 18432, qx);
    const int gq = (lane >> 3) & 3, q = lane & 7;
#pragma unroll
    for (int u = 0; u < 4; ++u) {
      const int nt = 2 * (u & 1) + (lane >> 5);
      const int st = w * 2 + (nt >> 1), gk = 2 * (nt & 1) + (gq >> 1);
      *(LAS v2u*)(L + L_QF + st * 1024 + (16 * gk + 8 * (u >> 1) + q) * 16 + (gq & 1) * 8) = v2u{qu[u].x, qu[u].z};
    }
    LGKM0();
    if (lane == 0) __hip_atomic_fetch_add((LAS unsigned*)(L + L_FLAG), 1u, __ATOMIC_RELAXED, __HIP_MEMORY_SCOPE_WORKGROUP);
  }
  STAMP(4);
  k_half(1, 1);
  v4u kreg[8];
  {
    const char* t2b = kwin + tile_off(16 + w);
#pragma unroll
    for (int q = 0; q < 8; ++q) ld128_nt(kreg[q], q * 1024 + lane * 16, t2b);
  }
  STAMP(2);
  for (unsigned spin = 0; *(volatile LAS unsigned*)(L + L_FLAG) < 4u && spin < (SPIN_CAP << 6); ++spin) __builtin_amdgcn_s_sleep(1);
  asm volatile("" ::: "memory");
  VMWAIT(16);
  v4u qf[8];
#pragma unroll
  for (int st = 0; st < 8; ++st) qf[st] = *(LAS v4u*)(L + L_QF + st * 1024 + lane * 16);

  f32x4 sc[4];
  auto qk_tile = [&](int slot) -> f32x4 {
    v4u kf[8];
#pragma unroll
    for (int st = 0; st < 8; ++st) kf[st] = *(LAS v4u*)(L + L_RING + slot * 8192 + c * 512 + (((4 * st + g) ^ c) << 4));
    f32x4 a = {0.f, 0.f, 0.f, 0.f};
#pragma unroll
    for (int st = 0; st < 8; ++st) a = mfma16(kf[st], qf[st], a);
    return a;
  };
  const unsigned vlane = (lane >> 3) * 512 + (((lane & 7) ^ (((lane >> 4) & 3) << 1)) << 4);
  auto v_piece = [&](int p, int q) {
    dma1(vwin + tile_off(4 * w + (q >> 1)) + (q & 1) * 4096 + ((s + 1 + p) & 3) * 128,
         lbase + L_RING + ((p & 1) * 8 + w) * 8192 + q * 1024, vlane);
  };
  const int wc = w < 7 ? w : 6;
  v2u wv[4][8];
  const char* wob = wo + (size_t)(2 * kvh * 256 + 64 * s) * (DM * 2) + wc * 128;
  const unsigned wvo = (unsigned)(8 * g) * (DM * 2) + c * 8;
  auto wo_load = [&](int ks, int i) { ld64(wv[ks][i], wvo + (unsigned)(256 * (ks >> 1) + 32 * (ks & 1) + i) * (DM * 2), wob); };

  auto wo_loads = [&](int n0, int n1) {
#pragma unroll
    for (int n = n0; n < n1; ++n) wo_load(n >> 3, n & 7);
  };
  auto v_half = [&](int p, int h) {
#pragma unroll
    for (int q = 4 * h; q < 4 * h + 4; ++q) v_piece(p, q);
  };
#define PRIO(n) __builtin_amdgcn_s_setprio(n)
  sc[0] = qk_tile(w);
  VMWAIT(8);
  sc[1] = qk_tile(8 + w);
  LGKM0();
  PRIO(2);
  wo_loads(0, 4); k_half(3, 0); wo_loads(4, 7);
  asm volatile("s_waitcnt vmcnt(11)"
               : "+v"(kreg[0]), "+v"(kreg[1]), "+v"(kreg[2]), "+v"(kreg[3]), "+v"(kreg[4]), "+v"(kreg[5]), "+v"(kreg[6]), "+v"(kreg[7]) :: "memory");
#pragma unroll
  for (int q = 0; q < 8; ++q) *(LAS v4u*)(L + L_RING + w * 8192 + ko[q] + (q & 3) * 1024) = kreg[q];
  sc[2] = qk_tile(w);
  LGKM0();
  PRIO(1);
  wo_loads(7, 10); k_half(3, 1); wo_loads(10, 13); v_half(0, 0);
  VMWAIT(7);
  sc[3] = qk_tile(8 + w);
  LGKM0();
  PRIO(0);
  wo_loads(13, 16); v_half(0, 1);
  STAMP(5);
  float mx = fmaxf(fmaxf(fmaxf(sc[0][0], sc[0][1]), fmaxf(sc[0][2], sc[0][3])), fmaxf(fmaxf(sc[1][0], sc[1][1]), fmaxf(sc[1][2], sc[1][3])));
  mx = fmaxf(mx, fmaxf(fmaxf(fmaxf(sc[2][0], sc[2][1]), fmaxf(sc[2][2], sc[2][3])), fmaxf(fmaxf(sc[3][0], sc[3][1]), fmaxf(sc[3][2], sc[3][3]))));
  mx = xg_max(mx);
  if (g == 0) *(LAS float*)(L + L_MX + (w * 16 + c) * 4) = mx;
  BARRIER();
  float mrow = *(LAS float*)(L + L_MX + c * 4);
#pragma unroll
  for (int ww = 1; ww < 8; ++ww) mrow = fmaxf(mrow, *(LAS float*)(L + L_MX + (ww * 16 + c) * 4));
  mrow *= C2;
  float lsum = 0.f;
#pragma unroll
  for (int t = 0; t < 4; ++t) {
    float p[4];
#pragma unroll
    for (int r = 0; r < 4; ++r) { p[r] = __builtin_amdgcn_exp2f(fmaf(sc[t][r], C2, -mrow)); lsum += p[r]; }
    *(LAS v2u*)(L + L_P + (4 * t + (w >> 1)) * 1024 + lane * 16 + (w & 1) * 8) = v2u{pack_bf16(p[0], p[1]), pack_bf16(p[2], p[3])};
  }
  lsum = xg_sum(lsum);
  if (g == 0) *(LAS float*)(L + L_LS + (w * 16 + c) * 4) = lsum;
  wo_loads(16, 20); v_half(1, 0);

  const int ct = w & 3, kh = w >> 2;
  unsigned tra;
  {
    const int q4 = (lane & 15) >> 2, p4 = lane & 3;
    tra = (4 * g + q4) * 128 + (((2 * ct + (p4 >> 1)) ^ (((2 * g + (q4 >> 1)) & 3) << 1)) << 4) + (p4 & 1) * 8 + kh * 32768;
  }
  auto pv_pass = [&](int p) -> f32x4 {
    f32x4 a = {0.f, 0.f, 0.f, 0.f};
#pragma unroll
    for (int i = 0; i < 8; ++i) {
      const v4u pf = *(LAS v4u*)(L + L_P + (8 * kh + i) * 1024 + lane * 16);
      const int ub = L_RING + (p & 1) * 65536 + i * 4096;
      const s16x4 a0 = __builtin_amdgcn_ds_read_tr16_b64_v4i16((LAS s16x4*)(L + ub + tra));
      const s16x4 a1 = __builtin_amdgcn_ds_read_tr16_b64_v4i16((LAS s16x4*)(L + ub + 2048 + tra));
      const v2u x0 = __builtin_bit_cast(v2u, a0), x1 = __builtin_bit_cast(v2u, a1);
      a = mfma16(v4u{x0.x, x0.y, x1.x, x1.y}, pf, a);
    }
    return a;
  };
  const int bh = b * HKV + kvh;
  char* stb = uni(ws + W_ST + (size_t)bh * 1024);
  char* h1 = uni(ws + W_H1 + (size_t)bh * (16 * 4096));
  auto reduce_pass = [&](f32x4 a) -> f32x4 {
    if (kh == 1) *(LAS f32x4*)(L + L_HALF + ct * 1024 + lane * 16) = a;
    BARRIER();
    if (kh == 0) a += *(LAS f32x4*)(L + L_HALF + ct * 1024 + lane * 16);
    return a;
  };
  auto publish_pass = [&](int p, f32x4 a) {
    if (kh == 0) st128_dev(((((s + 1 + p) & 3) * 4 + s) * 4 + ct) * 1024 + lane * 16, enc4(a, T), h1);
  };
  STAMP(6);
  VMWAIT(8);
  BARRIER();
  STAMP(7);
  {
    const f32x4 a = reduce_pass(pv_pass(0));
    wo_loads(20, 23); v_half(1, 1);
    asm volatile("s_barrier" ::: "memory");
    wo_loads(23, 26); v_half(2, 0);
    publish_pass(0, a);
    if (w == 7) {
      float lt = *(LAS float*)(L + L_LS + c * 4);
#pragma unroll
      for (int ww = 1; ww < 8; ++ww) lt += *(LAS float*)(L + L_LS + (ww * 16 + c) * 4);
      if (g == 0) st128_dev((s * 16 + c) * 16, v4u{__float_as_uint(mrow), T, __float_as_uint(lt), T}, stb);
    }
    if (kh == 0 || w == 7) VMWAIT(8); else VMWAIT(7);
  }
  BARRIER();
  v4u stu;
  {
    const f32x4 a = reduce_pass(pv_pass(1));
    wo_loads(26, 32); v_half(2, 1);
    asm volatile("s_barrier" ::: "memory");
    v_half(3, 0);
    publish_pass(1, a);
  }
  ld128_dev(stu, lane * 16, stb);
  if (kh == 0) VMWAIT(6); else VMWAIT(5);
  asm volatile(""
               : "+v"(wv[0][0]), "+v"(wv[0][1]), "+v"(wv[0][2]), "+v"(wv[0][3]), "+v"(wv[0][4]), "+v"(wv[0][5]), "+v"(wv[0][6]), "+v"(wv[0][7]),
                 "+v"(wv[1][0]), "+v"(wv[1][1]), "+v"(wv[1][2]), "+v"(wv[1][3]), "+v"(wv[1][4]), "+v"(wv[1][5]), "+v"(wv[1][6]), "+v"(wv[1][7])
               :: "memory");
  asm volatile("" : "+v"(wv[2][0]), "+v"(wv[2][1]), "+v"(wv[2][2]), "+v"(wv[2][3]), "+v"(wv[2][4]), "+v"(wv[2][5]), "+v"(wv[2][6]), "+v"(wv[2][7]),
                    "+v"(wv[3][0]), "+v"(wv[3][1]), "+v"(wv[3][2]), "+v"(wv[3][3]), "+v"(wv[3][4]), "+v"(wv[3][5]), "+v"(wv[3][6]), "+v"(wv[3][7])
               :: "memory");
  BARRIER();
  publish_pass(2, reduce_pass(pv_pass(2)));
  v_half(3, 1);
  v4u hu[2];
  const int srcA = (s + (kh ? 3 : 1)) & 3, srcB = (s + (kh ? 3 : 2)) & 3;
  const unsigned hoA = ((s * 4 + srcA) * 4 + ct) * 1024 + lane * 16, hoB = ((s * 4 + srcB) * 4 + ct) * 1024 + lane * 16;
  ld128_dev(hu[0], hoA, h1);
  ld128_dev(hu[1], hoB, h1);
  v4u wf[4][4];
#pragma unroll
  for (int ks = 0; ks < 4; ++ks)
#pragma unroll
    for (int jj = 0; jj < 4; ++jj)
#pragma unroll
      for (int r = 0; r < 4; ++r) {
        const unsigned lo = wv[ks][2 * r][jj >> 1], hi = wv[ks][2 * r + 1][jj >> 1];
        wf[ks][jj][r] = (jj & 1) ? perm_hi(lo, hi) : perm_lo(lo, hi);
      }
  const int j32 = kvh * 4 + s;
  char* h2 = uni(ws + W_H2 + (size_t)b * (32 * 8 * 112 * 16));
  unsigned h2st = ((j32 * 8 + 4 * (g & 1) + 2 * (g >> 1)) * 112 + wc * 16 + c) * 16;
  const int l31 = lane & 31;
  unsigned h2ld = (((4 * w + (lane >> 5)) * 8 + (j32 >> 2)) * 112 + 28 * (j32 & 3) + (l31 < 28 ? l31 : 27)) * 16;
  unsigned obw = L_OB + c * 144 + (16 * ct + 4 * g) * 2, obr = L_OB + (c & 7) * 144 + g * 16, hlf = L_HALF + ct * 1024 + lane * 16;
  asm volatile("" : "+v"(wf[0][0]), "+v"(wf[0][1]), "+v"(wf[0][2]), "+v"(wf[0][3]), "+v"(wf[1][0]), "+v"(wf[1][1]), "+v"(wf[1][2]), "+v"(wf[1][3]),
                    "+v"(wf[2][0]), "+v"(wf[2][1]), "+v"(wf[2][2]), "+v"(wf[2][3]), "+v"(wf[3][0]), "+v"(wf[3][1]), "+v"(wf[3][2]), "+v"(wf[3][3]),
                    "+v"(h2st), "+v"(h2ld), "+v"(obw), "+v"(obr), "+v"(hlf));
  asm volatile("s_waitcnt vmcnt(6)" : "+v"(stu) :: "memory");
  for (unsigned spin = 0; any_bad(tags_bad(stu, T)) && spin < SPIN_CAP; ++spin) reload1(stu, lane * 16, stb);
  float fS, fA, fB;
  {
    const float ms = __uint_as_float(stu.x), ls = __uint_as_float(stu.z);
    const float M = xg_max(ms);
    const float e = __builtin_amdgcn_exp2f(ms - M);
    const float fw = e / xg_sum(e * ls);
    fS = __shfl(fw, 16 * s + c); fA = __shfl(fw, 16 * srcA + c); fB = kh ? 0.f : __shfl(fw, 16 * srcB + c);
  }
  asm volatile("s_waitcnt vmcnt(0)" : "+v"(hu[0]), "+v"(hu[1]) :: "memory");
  STAMP(8);
  BARRIER();
  f32x4 own = pv_pass(3);
  STAMP(9);
  for (unsigned spin = 0; any_bad(quad_bad(hu[0], T) | quad_bad(hu[1], T)) && spin < SPIN_CAP; ++spin) reload2(hu[0], hu[1], hoA, hoB, h1);
  STAMP(11);
  {
    own = own * fS + dec4(hu[0]) * fA + dec4(hu[1]) * fB;
    if (kh == 1) *(LAS f32x4*)(L + hlf) = own;
    BARRIER();
    if (kh == 0) {
      own += *(LAS f32x4*)(L + hlf);
      *(LAS v2u*)(L + obw) = v2u{pack_bf16(own[0], own[1]), pack_bf16(own[2], own[3])};
    }
  }
  BARRIER();

  f32x4 pa[4];
#pragma unroll
  for (int jj = 0; jj < 4; ++jj) pa[jj] = f32x4{0.f, 0.f, 0.f, 0.f};
#pragma unroll
  for (int ks = 0; ks < 4; ++ks) {
    const v4u af = *(LAS v4u*)(L + obr + 8 * (ks >> 1) * 144 + 64 * (ks & 1));
#pragma unroll
    for (int jj = 0; jj < 4; ++jj) pa[jj] = mfma16(af, wf[ks][jj], pa[jj]);
  }
  if (w < 7) {
#pragma unroll
    for (int rr = 0; rr < 2; ++rr) {
      const f32x4 v = (g >> 1) ? f32x4{pa[0][2 + rr], pa[1][2 + rr], pa[2][2 + rr], pa[3][2 + rr]} : f32x4{pa[0][rr], pa[1][rr], pa[2][rr], pa[3][rr]};
      st128_dev(h2st + rr * (112 * 16), enc4(v, T), h2);
    }
  }
  STAMP(12);
  {
    v4u fu[2];
    const unsigned fo[2] = {h2ld, h2ld + 2 * 8 * 112 * 16};
    ld128_dev(fu[0], fo[0], h2);
    ld128_dev(fu[1], fo[1], h2);
    asm volatile("s_waitcnt vmcnt(0)" : "+v"(fu[0]), "+v"(fu[1]) :: "memory");
    for (unsigned spin = 0; any_bad(quad_bad(fu[0], T) | quad_bad(fu[1], T)) && spin < SPIN_CAP; ++spin) reload2(fu[0], fu[1], fo[0], fo[1], h2);
    STAMP(13);
    *(LAS f32x4*)(L + L_RED + w * 1024 + lane * 16) = dec4(fu[0]) + dec4(fu[1]);
  }
  BARRIER();
  if (w == 0) {
    const int hf = lane >> 5;
    f32x4 t = *(LAS f32x4*)(L + L_RED + (4 * hf) * 1024 + l31 * 16);
    t += *(LAS f32x4*)(L + L_RED + (4 * hf) * 1024 + 512 + l31 * 16);
#pragma unroll
    for (int ww = 1; ww < 4; ++ww) {
      t += *(LAS f32x4*)(L + L_RED + (4 * hf + ww) * 1024 + l31 * 16);
      t += *(LAS f32x4*)(L + L_RED + (4 * hf + ww) * 1024 + 512 + l31 * 16);
    }
#pragma unroll
    for (int e = 0; e < 4; ++e) {
      auto sw = __builtin_amdgcn_permlane32_swap(__float_as_uint(t[e]), __float_as_uint(t[e]), false, false);
      t[e] = __uint_as_float(sw[0]) + __uint_as_float(sw[1]);
    }
    if (lane < 28)
      *(v2u*)(out + (((b * SQ + (j32 >> 2)) * DM + 112 * (j32 & 3) + 4 * lane) >> 1)) = v2u{pack_bf16(t[0], t[1]), pack_bf16(t[2], t[3])};
  }
  if (w == 1 && (b & 1) == 0 && j32 == 0 && lane == 0) *(unsigned*)(ws + W_EP + rb * 256) = T;
}

}

extern "C" void kernel_launch(void* const* d_in, const int* in_sizes, int n_in, void* d_out, int out_size, void* d_ws,
                              size_t ws_size, hipStream_t stream) {
  (void)in_sizes; (void)n_in; (void)out_size;
  if (ws_size < W_END) return;
  (void)hipFuncSetAttribute((const void*)attn_fused_kernel, hipFuncAttributeMaxDynamicSharedMemorySize, LDS_BYTES);
  attn_fused_kernel<<<dim3(256), dim3(512), LDS_BYTES, stream>>>((const char*)d_in[0], (const char*)d_in[1], (const char*)d_in[2],
                                                                 (const char*)d_in[3], (const char*)d_in[4], (unsigned*)d_out, (char*)d_ws);
}
```

```cpp
#include <hip/hip_runtime.h>
#include <hip/hip_bf16.h>

namespace {

typedef unsigned v4u __attribute__((ext_vector_type(4)));
typedef unsigned v2u __attribute__((ext_vector_type(2)));
typedef float f32x4 __attribute__((ext_vector_type(4)));
typedef float f32x2 __attribute__((ext_vector_type(2)));
typedef __bf16 bf16x8 __attribute__((ext_vector_type(8)));
typedef __bf16 bf16x2 __attribute__((ext_vector_type(2)));
typedef short s16x4 __attribute__((ext_vector_type(4)));
#define LAS __attribute__((address_space(3)))

constexpr int NB = 8, SQ = 8, DM = 448, HKV = 8, SK = 8192, WIN = 2048;
constexpr int NQ = 4096;
constexpr float C2 = 0.0625f * 1.4426950408889634f;
constexpr unsigned SPIN_CAP = 1u << 18;

constexpr int L_RING = 0;
constexpr int L_WQ = L_RING + 65536;
constexpr int L_P = 131072;
constexpr int L_X = L_P;
constexpr int L_QF = L_P + 16384;
constexpr int L_HALF = L_QF;
constexpr int L_MX = L_QF + 8192;
constexpr int L_LS = L_MX + 512;
constexpr int L_QR = L_LS + 512;
constexpr int L_FLAG = L_QR + 4096;
constexpr int L_OB = L_FLAG + 16;
constexpr int L_RED = L_P;
constexpr int LDS_BYTES = 163840;
static_assert(L_OB + 16 * 144 <= LDS_BYTES, "LDS map");

constexpr size_t W_EP = 0;
constexpr size_t W_QX = 4096;
constexpr size_t W_ST = W_QX + (size_t)8 * 4 * 8 * 4096;
constexpr size_t W_H1 = W_ST + (size_t)64 * 4 * 256;
constexpr size_t W_H2 = W_H1 + (size_t)64 * 16 * 2048;
constexpr size_t W_END = W_H2 + (size_t)8 * 32 * 4 * 112 * 16;

__device__ __forceinline__ f32x4 mfma16(v4u a, v4u b, f32x4 c) {
  return __builtin_amdgcn_mfma_f32_16x16x32_bf16(__builtin_bit_cast(bf16x8, a), __builtin_bit_cast(bf16x8, b), c, 0, 0, 0);
}
__device__ __forceinline__ unsigned pack_bf16(float lo, float hi) {
  bf16x2 r; r[0] = (__bf16)lo; r[1] = (__bf16)hi;
  return __builtin_bit_cast(unsigned, r);
}
__device__ __forceinline__ float xg_max(float x) {
  auto s = __builtin_amdgcn_permlane16_swap(__float_as_uint(x), __float_as_uint(x), false, false);
  x = fmaxf(__uint_as_float(s[0]), __uint_as_float(s[1]));
  auto t = __builtin_amdgcn_permlane32_swap(__float_as_uint(x), __float_as_uint(x), false, false);
  return fmaxf(__uint_as_float(t[0]), __uint_as_float(t[1]));
}
__device__ __forceinline__ float xg_sum(float x) {
  auto s = __builtin_amdgcn_permlane16_swap(__float_as_uint(x), __float_as_uint(x), false, false);
  x = __uint_as_float(s[0]) + __uint_as_float(s[1]);
  auto t = __builtin_amdgcn_permlane32_swap(__float_as_uint(x), __float_as_uint(x), false, false);
  return __uint_as_float(t[0]) + __uint_as_float(t[1]);
}
__device__ __forceinline__ unsigned perm_lo(unsigned a, unsigned b) { return __builtin_amdgcn_perm(b, a, 0x05040100u); }
__device__ __forceinline__ unsigned perm_hi(unsigned a, unsigned b) { return __builtin_amdgcn_perm(b, a, 0x07060302u); }

#define T2REG 1
#define KPOL " nt"
#define VPOL " nt"
#define WPOL ""
#define VMWAIT(N) asm volatile("s_waitcnt vmcnt(" #N ")" ::: "memory")
#define LGKM0() asm volatile("s_waitcnt lgkmcnt(0)" ::: "memory")
#define BARRIER() asm volatile("s_waitcnt lgkmcnt(0)\n\ts_barrier" ::: "memory")

__device__ __forceinline__ void dma_half(const char* gbase, unsigned lds_dst, unsigned v0, unsigned v1, unsigned v2, unsigned v3) {
  unsigned keep;
  asm volatile(
      "s_mov_b32 %[keep], m0\n\ts_mov_b32 m0, %[l0]\n\ts_nop 2\n\t"
      "global_load_lds_dwordx4 %[v0], %[sb]" KPOL "\n\t"
      "global_load_lds_dwordx4 %[v1], %[sb] offset:1024" KPOL "\n\t"
      "global_load_lds_dwordx4 %[v2], %[sb] offset:2048" KPOL "\n\t"
      "global_load_lds_dwordx4 %[v3], %[sb] offset:3072" KPOL "\n\t"
      "s_mov_b32 m0, %[keep]"
      : [keep] "=&s"(keep)
      : [v0] "v"(v0), [v1] "v"(v1), [v2] "v"(v2), [v3] "v"(v3), [sb] "s"(gbase), [l0] "s"(lds_dst)
      : "memory");
}
__device__ __forceinline__ void dma1w(const char* sb, unsigned lds_dst, unsigned voff) {
  unsigned keep;
  asm volatile("s_mov_b32 %[keep], m0\n\ts_mov_b32 m0, %[l0]\n\ts_nop 2\n\tglobal_load_lds_dwordx4 %[v0], %[sb]" WPOL "\n\ts_mov_b32 m0, %[keep]"
               : [keep] "=&s"(keep) : [v0] "v"(voff), [sb] "s"(sb), [l0] "s"(lds_dst) : "memory");
}
__device__ __forceinline__ void dma1(const char* sb, unsigned lds_dst, unsigned voff) {
  unsigned keep;
  asm volatile("s_mov_b32 %[keep], m0\n\ts_mov_b32 m0, %[l0]\n\ts_nop 2\n\tglobal_load_lds_dwordx4 %[v0], %[sb]" VPOL "\n\ts_mov_b32 m0, %[keep]"
               : [keep] "=&s"(keep) : [v0] "v"(voff), [sb] "s"(sb), [l0] "s"(lds_dst) : "memory");
}
__device__ __forceinline__ void ld128_nt(v4u& d, unsigned voff, const char* sb) {
  asm volatile("global_load_dwordx4 %0, %1, %2" KPOL : "=v"(d) : "v"(voff), "s"(sb) : "memory");
}
__device__ __forceinline__ void ld64(v2u& d, unsigned voff, const char* sb) {
  asm volatile("global_load_dwordx2 %0, %1, %2" WPOL : "=v"(d) : "v"(voff), "s"(sb) : "memory");
}
__device__ __forceinline__ void ld128_dev(v4u& d, unsigned voff, const char* sb) {
  asm volatile("global_load_dwordx4 %0, %1, %2 sc1" : "=v"(d) : "v"(voff), "s"(sb) : "memory");
}
__device__ __forceinline__ void ld32_dev(unsigned& d, unsigned voff, const char* sb) {
  asm volatile("global_load_dword %0, %1, %2 sc1" : "=v"(d) : "v"(voff), "s"(sb) : "memory");
}
__device__ __forceinline__ void st128_dev(unsigned voff, v4u v, char* sb) {
  asm volatile("global_store_dwordx4 %0, %1, %2 sc1\n\ts_nop 1" : : "v"(voff), "v"(v), "s"(sb) : "memory");
}
__device__ __forceinline__ unsigned tags_bad(v4u u, unsigned T) { return (u.y ^ T) | (u.w ^ T); }
struct PairTag { unsigned a, b; };
__device__ __forceinline__ PairTag pair_tag(unsigned T, unsigned pos) {
  const unsigned t = T >> (8u * (pos & 1u));
  return PairTag{t << 20, (t << 8) & 0xFFF00000u};
}
__device__ __forceinline__ v2u bfp_enc(f32x4 a, unsigned tag) {
  const unsigned m = max(max(__float_as_uint(a[0]) & 0x7FFFFFFFu, __float_as_uint(a[1]) & 0x7FFFFFFFu),
                         max(__float_as_uint(a[2]) & 0x7FFFFFFFu, __float_as_uint(a[3]) & 0x7FFFFFFFu));
  const unsigned e = max(m >> 23, 10u);
  const float sc = __uint_as_float((263u - e) << 23);
  int q[4];
#pragma unroll
  for (int i = 0; i < 4; ++i) q[i] = min(max((int)__builtin_rintf(a[i] * sc), -1023), 1023);
  return v2u{((unsigned)q[0] & 0x7FFu) | (((unsigned)q[1] & 0x7FFu) << 11) | ((unsigned)q[2] << 22),
             (((unsigned)q[2] >> 10) & 1u) | (((unsigned)q[3] & 0x7FFu) << 1) | (e << 12) | tag};
}
__device__ __forceinline__ f32x4 bfp_dec(unsigned d0, unsigned d1) {
  const unsigned e = (d1 >> 12) & 0xFFu;
  const float sc = __uint_as_float(e == 255u ? 0x7F800000u : (e - 9u) << 23);
  const int q0 = __builtin_amdgcn_sbfe((int)d0, 0, 11), q1 = __builtin_amdgcn_sbfe((int)d0, 11, 11);
  const int q2 = __builtin_amdgcn_sbfe((int)__builtin_amdgcn_alignbit(d1, d0, 22), 0, 11), q3 = __builtin_amdgcn_sbfe((int)d1, 1, 11);
  return f32x4{(float)q0 * sc, (float)q1 * sc, (float)q2 * sc, (float)q3 * sc};
}
__device__ __forceinline__ v4u bfp_pair(f32x4 a, f32x4 b, PairTag t) {
  const v2u x = bfp_enc(a, t.a), y = bfp_enc(b, t.b);
  return v4u{x.x, x.y, y.x, y.y};
}
__device__ __forceinline__ unsigned pair_bad(v4u u, PairTag t) { return ((u.y ^ t.a) | (u.w ^ t.b)) & 0xFFF00000u; }
template <class P> __device__ __forceinline__ P* uni(P* p) {
  const unsigned long long a = (unsigned long long)p;
  const unsigned lo = __builtin_amdgcn_readfirstlane((unsigned)a), hi = __builtin_amdgcn_readfirstlane((unsigned)(a >> 32));
  return (P*)(((unsigned long long)hi << 32) | lo);
}
__device__ __forceinline__ bool any_bad(unsigned bad) { return __builtin_amdgcn_ballot_w64(bad != 0u) != 0ull; }
__device__ __forceinline__ void reload1(v4u& d0, unsigned o0, const char* sb) {
  asm volatile("global_load_dwordx4 %0, %1, %2 sc1\n\ts_waitcnt vmcnt(0)" : "=&v"(d0) : "v"(o0), "s"(sb) : "memory");
}
__device__ __forceinline__ void reload2(v4u& d0, v4u& d1, unsigned o0, unsigned o1, const char* sb) {
  asm volatile("global_load_dwordx4 %0, %2, %4 sc1\n\tglobal_load_dwordx4 %1, %3, %4 sc1\n\ts_waitcnt vmcnt(0)"
               : "=&v"(d0), "=&v"(d1) : "v"(o0), "v"(o1), "s"(sb) : "memory");
}

__device__ __forceinline__ void reload4(v4u& d0, v4u& d1, v4u& d2, v4u& d3, unsigned o0, unsigned o1, unsigned o2, unsigned o3, const char* sb) {
  asm volatile("global_load_dwordx4 %0, %4, %8 sc1\n\tglobal_load_dwordx4 %1, %5, %8 sc1\n\t"
               "global_load_dwordx4 %2, %6, %8 sc1\n\tglobal_load_dwordx4 %3, %7, %8 sc1\n\ts_waitcnt vmcnt(0)"
               : "=&v"(d0), "=&v"(d1), "=&v"(d2), "=&v"(d3) : "v"(o0), "v"(o1), "v"(o2), "v"(o3), "s"(sb) : "memory");
}

#define STAMP(i) do { } while (0)

__global__ void __launch_bounds__(512, 2)
attn_fused_kernel(const char* __restrict__ x, const char* __restrict__ kc, const char* __restrict__ vc,
                  const char* __restrict__ wq, const char* __restrict__ wo, unsigned* __restrict__ out, char* __restrict__ ws) {
  extern __shared__ __attribute__((aligned(16))) unsigned char lds_raw[];
  LAS unsigned char* L = (LAS unsigned char*)lds_raw;
  const unsigned lbase = (unsigned)(size_t)L;
  STAMP(0);
  const int tid = threadIdx.x, lane = tid & 63, w = __builtin_amdgcn_readfirstlane(tid >> 6);
  const int g = lane >> 4, c = lane & 15;
  const int bid = blockIdx.x, kvh = bid & 7, s = (bid >> 3) & 3, b = bid >> 5;
  const int rot = 8 * s + b;
  const int rb = b >> 1, cb = (b & 1) * 4 + s;
  asm volatile("" :: "s"(x), "s"(kc), "s"(vc), "s"(wq), "s"(wo), "s"(out), "s"(ws));
  if (tid == 0) *(LAS unsigned*)(L + L_FLAG) = 0u;
  __builtin_amdgcn_s_setprio(3);

  const size_t headrow = (size_t)(b * HKV + kvh) * SK + (SK - WIN) + s * 512;
  const char* kwin = kc + headrow * 512;
  const char* vwin = vc + headrow * 512;
  {
    const unsigned r8 = lane >> 3, f = ((r8 >> 1) & 1) | ((w & 1) << 1);
    const unsigned vo = r8 * (NQ * 2) + (((lane & 7) ^ (f << 1)) << 4);
    const char* wqb = wq + (size_t)(8 * w) * (NQ * 2) + (kvh * 512 + cb * 64) * 2;
#pragma unroll
    for (int i = 0; i < 7; ++i) dma1w(wqb + (size_t)(64 * i) * (NQ * 2), lbase + L_WQ + (8 * i + w) * 1024, vo);
    const char* xb = x + (size_t)(16 * rb) * (DM * 2);
#pragma unroll
    for (int i = 0; i < 2; ++i) {
      const int m = 8 * i + w;
      if (m < 14) {
        const unsigned p = 64 * m + lane, row = (p * 1171u) >> 16, ch = p - 56 * row;
        dma1w(xb, lbase + L_X + m * 1024, row * (DM * 2) + (((ch & ~7u) | ((ch & 7u) ^ ((row >> 1) & 7u))) << 4));
      }
    }
  }
  unsigned ep = 0;
  if (w == 7) ld32_dev(ep, 0u, ws + W_EP + rb * 256);
  asm volatile("s_barrier" ::: "memory");
  unsigned ko[8];
  {
    const unsigned hi = lane >> 5, l31 = lane & 31;
    const unsigned laneK = (hi << 9) | ((l31 ^ hi) << 4);
#pragma unroll
    for (int q = 0; q < 8; ++q) ko[q] = (laneK ^ (unsigned)(q << 5)) + ((q >> 2) << 12);
  }
  auto tile_off = [&](int unit) -> size_t { return (size_t)((unit + rot) & 31) * 8192; };
  auto k_half = [&](int t, int h) {
    dma_half(kwin + tile_off(8 * t + w), lbase + L_RING + ((t & 1) * 8 + w) * 8192 + h * 4096, ko[4 * h], ko[4 * h + 1], ko[4 * h + 2], ko[4 * h + 3]);
  };
  k_half(0, 0);
  asm volatile("s_waitcnt vmcnt(4)" : "+v"(ep) :: "memory");
  if (w == 7 && lane == 0) *(LAS unsigned*)(L + L_FLAG + 4) = ep + 0x10000001u;
  STAMP(1);
  BARRIER();
  const unsigned T = __builtin_amdgcn_readfirstlane(*(LAS unsigned*)(L + L_FLAG + 4));
  char* qx = uni(ws + W_QX + (size_t)(kvh * 4 + rb) * (8 * 4096));
  {
    const int nt = w & 3, kh = w >> 2;
    const int q4 = (lane & 15) >> 2, p4 = lane & 3;
    const unsigned fa = (unsigned)(q4 >> 1) | ((unsigned)(g & 1) << 1);
    const unsigned ta = L_WQ + (224 * kh + 8 * g + q4) * 128 + (((2 * nt + (p4 >> 1)) ^ (fa << 1)) << 4) + (p4 & 1) * 8;
    f32x4 qa = {0.f, 0.f, 0.f, 0.f};
#pragma unroll
    for (int i = 0; i < 7; ++i) {
      const s16x4 a0 = __builtin_amdgcn_ds_read_tr16_b64_v4i16((LAS s16x4*)(L + ta + i * 4096));
      const s16x4 a1 = __builtin_amdgcn_ds_read_tr16_b64_v4i16((LAS s16x4*)(L + ta + i * 4096 + 512));
      const unsigned ch = 4 * (7 * kh + i) + g;
      const v4u xb = *(LAS v4u*)(L + L_X + c * 896 + (((ch & ~7u) | ((ch & 7u) ^ ((c >> 1) & 7u))) << 4));
      const v2u x0 = __builtin_bit_cast(v2u, a0), x1 = __builtin_bit_cast(v2u, a1);
      qa = mfma16(v4u{x0.x, x0.y, x1.x, x1.y}, xb, qa);
    }
    if (kh == 1) *(LAS f32x4*)(L + L_QR + nt * 1024 + lane * 16) = qa;
    STAMP(17);
    BARRIER();
    if (kh == 0) {
      qa += *(LAS f32x4*)(L + L_QR + nt * 1024 + lane * 16);
      st128_dev(cb * 4096 + nt * 1024 + lane * 16, v4u{pack_bf16(qa[0], qa[1]), T, pack_bf16(qa[2], qa[3]), T}, qx);
    }
  }
  STAMP(18);
  k_half(0, 1);
  k_half(1, 0);
  if (w < 4) {
    v4u qu[4];
    const unsigned qgo = w * 4096 + (lane >> 5) * 1024 + (16 * ((lane >> 3) & 3) + 8 * (b & 1) + (lane & 7)) * 16;
    STAMP(19);
    reload4(qu[0], qu[1], qu[2], qu[3], qgo, qgo + 2048, qgo + 16384, qgo + 18432, qx);
    STAMP(3);
    for (unsigned spin = 0; any_bad(tags_bad(qu[0], T) | tags_bad(qu[1], T)) && spin < SPIN_CAP; ++spin) reload2(qu[0], qu[1], qgo, qgo + 2048, qx);
    for (unsigned spin = 0; any_bad(tags_bad(qu[2], T) | tags_bad(qu[3], T)) && spin < SPIN_CAP; ++spin) reload2(qu[2], qu[3], qgo + 16384, qgo + 18432, qx);
    const int gq = (lane >> 3) & 3, q = lane & 7;
#pragma unroll
    for (int u = 0; u < 4; ++u) {
      const int nt = 2 * (u & 1) + (lane >> 5);
      const int st = w * 2 + (nt >> 1), gk = 2 * (nt & 1) + (gq >> 1);
      *(LAS v2u*)(L + L_QF + st * 1024 + (16 * gk + 8 * (u >> 1) + q) * 16 + (gq & 1) * 8) = v2u{qu[u].x, qu[u].z};
    }
    LGKM0();
    if (lane == 0) __hip_atomic_fetch_add((LAS unsigned*)(L + L_FLAG), 1u, __ATOMIC_RELAXED, __HIP_MEMORY_SCOPE_WORKGROUP);
  }
  STAMP(4);
  k_half(1, 1);
  v4u kreg[8];
  {
    const char* t2b = kwin + tile_off(16 + w);
#pragma unroll
    for (int q = 0; q < 8; ++q) ld128_nt(kreg[q], q * 1024 + lane * 16, t2b);
  }
  STAMP(2);
  for (unsigned spin = 0; *(volatile LAS unsigned*)(L + L_FLAG) < 4u && spin < (SPIN_CAP << 6); ++spin) __builtin_amdgcn_s_sleep(1);
  asm volatile("" ::: "memory");
  VMWAIT(16);
  v4u qf[8];
#pragma unroll
  for (int st = 0; st < 8; ++st) qf[st] = *(LAS v4u*)(L + L_QF + st * 1024 + lane * 16);

  f32x4 sc[4];
  auto qk_tile = [&](int slot) -> f32x4 {
    v4u kf[8];
#pragma unroll
    for (int st = 0; st < 8; ++st) kf[st] = *(LAS v4u*)(L + L_RING + slot * 8192 + c * 512 + (((4 * st + g) ^ c) << 4));
    f32x4 a = {0.f, 0.f, 0.f, 0.f};
#pragma unroll
    for (int st = 0; st < 8; ++st) a = mfma16(kf[st], qf[st], a);
    return a;
  };
  const unsigned vlane = (lane >> 3) * 512 + (((lane & 7) ^ (((lane >> 4) & 3) << 1)) << 4);
  auto v_piece = [&](int p, int q) {
    dma1(vwin + tile_off(4 * w + (q >> 1)) + (q & 1) * 4096 + ((s + 1 + p) & 3) * 128,
         lbase + L_RING + ((p & 1) * 8 + w) * 8192 + q * 1024, vlane);
  };
  const int wc = w < 7 ? w : 6;
  v2u wv[4][8];
  const char* wob = wo + (size_t)(2 * kvh * 256 + 64 * s) * (DM * 2) + wc * 128;
  const unsigned wvo = (unsigned)(8 * g) * (DM * 2) + c * 8;
  auto wo_load = [&](int ks, int i) { ld64(wv[ks][i], wvo + (unsigned)(256 * (ks >> 1) + 32 * (ks & 1) + i) * (DM * 2), wob); };

  auto wo_loads = [&](int n0, int n1) {
#pragma unroll
    for (int n = n0; n < n1; ++n) wo_load(n >> 3, n & 7);
  };
  auto v_half = [&](int p, int h) {
#pragma unroll
    for (int q = 4 * h; q < 4 * h + 4; ++q) v_piece(p, q);
  };
#define PRIO(n) __builtin_amdgcn_s_setprio(n)
  sc[0] = qk_tile(w);
  VMWAIT(8);
  sc[1] = qk_tile(8 + w);
  LGKM0();
  PRIO(2);
  wo_loads(0, 4); k_half(3, 0); wo_loads(4, 7);
  asm volatile("s_waitcnt vmcnt(11)"
               : "+v"(kreg[0]), "+v"(kreg[1]), "+v"(kreg[2]), "+v"(kreg[3]), "+v"(kreg[4]), "+v"(kreg[5]), "+v"(kreg[6]), "+v"(kreg[7]) :: "memory");
#pragma unroll
  for (int q = 0; q < 8; ++q) *(LAS v4u*)(L + L_RING + w * 8192 + ko[q] + (q & 3) * 1024) = kreg[q];
  sc[2] = qk_tile(w);
  LGKM0();
  PRIO(1);
  wo_loads(7, 10); k_half(3, 1); wo_loads(10, 13); v_half(0, 0);
  VMWAIT(7);
  sc[3] = qk_tile(8 + w);
  LGKM0();
  PRIO(0);
  wo_loads(13, 16); v_half(0, 1);
  STAMP(5);
  float mx = fmaxf(fmaxf(fmaxf(sc[0][0], sc[0][1]), fmaxf(sc[0][2], sc[0][3])), fmaxf(fmaxf(sc[1][0], sc[1][1]), fmaxf(sc[1][2], sc[1][3])));
  mx = fmaxf(mx, fmaxf(fmaxf(fmaxf(sc[2][0], sc[2][1]), fmaxf(sc[2][2], sc[2][3])), fmaxf(fmaxf(sc[3][0], sc[3][1]), fmaxf(sc[3][2], sc[3][3]))));
  mx = xg_max(mx);
  if (g == 0) *(LAS float*)(L + L_MX + (w * 16 + c) * 4) = mx;
  BARRIER();
  float mrow = *(LAS float*)(L + L_MX + c * 4);
#pragma unroll
  for (int ww = 1; ww < 8; ++ww) mrow = fmaxf(mrow, *(LAS float*)(L + L_MX + (ww * 16 + c) * 4));
  mrow *= C2;
  float lsum = 0.f;
#pragma unroll
  for (int t = 0; t < 4; ++t) {
    float p[4];
#pragma unroll
    for (int r = 0; r < 4; ++r) { p[r] = __builtin_amdgcn_exp2f(fmaf(sc[t][r], C2, -mrow)); lsum += p[r]; }
    *(LAS v2u*)(L + L_P + (4 * t + (w >> 1)) * 1024 + lane * 16 + (w & 1) * 8) = v2u{pack_bf16(p[0], p[1]), pack_bf16(p[2], p[3])};
  }
  lsum = xg_sum(lsum);
  if (g == 0) *(LAS float*)(L + L_LS + (w * 16 + c) * 4) = lsum;
  wo_loads(16, 20); v_half(1, 0);

  const int ct = w & 3, kh = w >> 2;
  unsigned tra;
  {
    const int q4 = (lane & 15) >> 2, p4 = lane & 3;
    tra = (4 * g + q4) * 128 + (((2 * ct + (p4 >> 1)) ^ (((2 * g + (q4 >> 1)) & 3) << 1)) << 4) + (p4 & 1) * 8 + kh * 32768;
  }
  auto pv_pass = [&](int p) -> f32x4 {
    f32x4 a = {0.f, 0.f, 0.f, 0.f};
#pragma unroll
    for (int i = 0; i < 8; ++i) {
      const v4u pf = *(LAS v4u*)(L + L_P + (8 * kh + i) * 1024 + lane * 16);
      const int ub = L_RING + (p & 1) * 65536 + i * 4096;
      const s16x4 a0 = __builtin_amdgcn_ds_read_tr16_b64_v4i16((LAS s16x4*)(L + ub + tra));
      const s16x4 a1 = __builtin_amdgcn_ds_read_tr16_b64_v4i16((LAS s16x4*)(L + ub + 2048 + tra));
      const v2u x0 = __builtin_bit_cast(v2u, a0), x1 = __builtin_bit_cast(v2u, a1);
      a = mfma16(v4u{x0.x, x0.y, x1.x, x1.y}, pf, a);
    }
    return a;
  };
  const int bh = b * HKV + kvh;
  char* stb = uni(ws + W_ST + (size_t)bh * 1024);
  char* h1 = uni(ws + W_H1 + (size_t)bh * (16 * 2048));
  const PairTag tg1 = pair_tag(T, lane);
  auto reduce_pass = [&](f32x4 a) -> v4u {
    *(LAS f32x4*)(L + L_HALF + (kh * 4 + ct) * 1024 + lane * 16) = a;
    BARRIER();
    v4u u = {0u, 0u, 0u, 0u};
    if (w < 2) {
      const LAS unsigned char* hp = L + L_HALF + 2 * w * 1024 + lane * 16;
      u = bfp_pair(*(const LAS f32x4*)hp + *(const LAS f32x4*)(hp + 4096), *(const LAS f32x4*)(hp + 1024) + *(const LAS f32x4*)(hp + 5120), tg1);
    }
    return u;
  };
  auto publish_pass = [&](int p, v4u u) {
    if (w < 2) st128_dev(((((s + 1 + p) & 3) * 4 + s) * 2 + w) * 1024 + lane * 16, u, h1);
  };
  STAMP(6);
  VMWAIT(8);
  BARRIER();
  STAMP(7);
  {
    const v4u a = reduce_pass(pv_pass(0));
    wo_loads(20, 23); v_half(1, 1);
    asm volatile("s_barrier" ::: "memory");
    wo_loads(23, 26); v_half(2, 0);
    publish_pass(0, a);
    if (w == 7) {
      float lt = *(LAS float*)(L + L_LS + c * 4);
#pragma unroll
      for (int ww = 1; ww < 8; ++ww) lt += *(LAS float*)(L + L_LS + (ww * 16 + c) * 4);
      if (g == 0) st128_dev((s * 16 + c) * 16, v4u{__float_as_uint(mrow), T, __float_as_uint(lt), T}, stb);
    }
    if (w < 2 || w == 7) VMWAIT(8); else VMWAIT(7);
  }
  BARRIER();
  v4u stu;
  {
    const v4u a = reduce_pass(pv_pass(1));
    wo_loads(26, 32); v_half(2, 1);
    asm volatile("s_barrier" ::: "memory");
    v_half(3, 0);
    publish_pass(1, a);
  }
  ld128_dev(stu, lane * 16, stb);
  if (w < 2) VMWAIT(6); else VMWAIT(5);
  asm volatile(""
               : "+v"(wv[0][0]), "+v"(wv[0][1]), "+v"(wv[0][2]), "+v"(wv[0][3]), "+v"(wv[0][4]), "+v"(wv[0][5]), "+v"(wv[0][6]), "+v"(wv[0][7]),
                 "+v"(wv[1][0]), "+v"(wv[1][1]), "+v"(wv[1][2]), "+v"(wv[1][3]), "+v"(wv[1][4]), "+v"(wv[1][5]), "+v"(wv[1][6]), "+v"(wv[1][7])
               :: "memory");
  asm volatile("" : "+v"(wv[2][0]), "+v"(wv[2][1]), "+v"(wv[2][2]), "+v"(wv[2][3]), "+v"(wv[2][4]), "+v"(wv[2][5]), "+v"(wv[2][6]), "+v"(wv[2][7]),
                    "+v"(wv[3][0]), "+v"(wv[3][1]), "+v"(wv[3][2]), "+v"(wv[3][3]), "+v"(wv[3][4]), "+v"(wv[3][5]), "+v"(wv[3][6]), "+v"(wv[3][7])
               :: "memory");
  BARRIER();
  publish_pass(2, reduce_pass(pv_pass(2)));
  v_half(3, 1);
  v4u hu[2];
  const int srcA = (s + (kh ? 3 : 1)) & 3, srcB = (s + (kh ? 3 : 2)) & 3;
  const unsigned hoA = ((s * 4 + srcA) * 2 + (ct >> 1)) * 1024 + lane * 16, hoB = ((s * 4 + srcB) * 2 + (ct >> 1)) * 1024 + lane * 16;
  v4u wf[4][4];
#pragma unroll
  for (int ks = 0; ks < 4; ++ks)
#pragma unroll
    for (int jj = 0; jj < 4; ++jj)
#pragma unroll
      for (int r = 0; r < 4; ++r) {
        const unsigned lo = wv[ks][2 * r][jj >> 1], hi = wv[ks][2 * r + 1][jj >> 1];
        wf[ks][jj][r] = (jj & 1) ? perm_hi(lo, hi) : perm_lo(lo, hi);
      }
  const int j32 = kvh * 4 + s;
  char* h2 = uni(ws + W_H2 + (size_t)b * (32 * 4 * 112 * 16));
  unsigned h2st = ((j32 * 4 + 2 * (g & 1) + (g >> 1)) * 112 + wc * 16 + c) * 16;
  const unsigned pos2 = 14 * (j32 & 7) + (c < 14 ? c : 13);
  unsigned h2ld = (((4 * w + g) * 4 + (j32 >> 3)) * 112 + pos2) * 16;
  const PairTag tg2s = pair_tag(T, c), tg2l = pair_tag(T, pos2);
  unsigned obw = L_OB + c * 144 + (16 * ct + 4 * g) * 2, obr = L_OB + (c & 7) * 144 + g * 16, hlf = L_HALF + ct * 1024 + lane * 16;
  asm volatile("" : "+v"(wf[0][0]), "+v"(wf[0][1]), "+v"(wf[0][2]), "+v"(wf[0][3]), "+v"(wf[1][0]), "+v"(wf[1][1]), "+v"(wf[1][2]), "+v"(wf[1][3]),
                    "+v"(wf[2][0]), "+v"(wf[2][1]), "+v"(wf[2][2]), "+v"(wf[2][3]), "+v"(wf[3][0]), "+v"(wf[3][1]), "+v"(wf[3][2]), "+v"(wf[3][3]),
                    "+v"(h2st), "+v"(h2ld), "+v"(obw), "+v"(obr), "+v"(hlf));
  asm volatile("s_waitcnt vmcnt(4)" : "+v"(stu) :: "memory");
  for (unsigned spin = 0; any_bad(tags_bad(stu, T)) && spin < SPIN_CAP; ++spin) reload1(stu, lane * 16, stb);
  float fS, fA, fB;
  {
    const float ms = __uint_as_float(stu.x), ls = __uint_as_float(stu.z);
    const float M = xg_max(ms);
    const float e = __builtin_amdgcn_exp2f(ms - M);
    const float fw = e / xg_sum(e * ls);
    fS = __shfl(fw, 16 * s + c); fA = __shfl(fw, 16 * srcA + c); fB = kh ? 0.f : __shfl(fw, 16 * srcB + c);
  }
#define H1LATE 2
  asm volatile("s_waitcnt vmcnt(%0)" :: "n"(H1LATE) : "memory");
  ld128_dev(hu[0], hoA, h1);
  ld128_dev(hu[1], hoB, h1);
  asm volatile("s_waitcnt vmcnt(0)" : "+v"(hu[0]), "+v"(hu[1]) :: "memory");
  STAMP(8);
  BARRIER();
  f32x4 own = pv_pass(3);
  STAMP(9);
  for (unsigned spin = 0; any_bad(pair_bad(hu[0], tg1) | pair_bad(hu[1], tg1)) && spin < SPIN_CAP; ++spin) reload2(hu[0], hu[1], hoA, hoB, h1);
  STAMP(11);
  {
    own = own * fS + ((ct & 1) ? bfp_dec(hu[0].z, hu[0].w) : bfp_dec(hu[0].x, hu[0].y)) * fA
                   + ((ct & 1) ? bfp_dec(hu[1].z, hu[1].w) : bfp_dec(hu[1].x, hu[1].y)) * fB;
    if (kh == 1) *(LAS f32x4*)(L + hlf) = own;
    BARRIER();
    if (kh == 0) {
      own += *(LAS f32x4*)(L + hlf);
      *(LAS v2u*)(L + obw) = v2u{pack_bf16(own[0], own[1]), pack_bf16(own[2], own[3])};
    }
  }
  BARRIER();

  f32x4 pa[4];
#pragma unroll
  for (int jj = 0; jj < 4; ++jj) pa[jj] = f32x4{0.f, 0.f, 0.f, 0.f};
#pragma unroll
  for (int ks = 0; ks < 4; ++ks) {
    const v4u af = *(LAS v4u*)(L + obr + 8 * (ks >> 1) * 144 + 64 * (ks & 1));
#pragma unroll
    for (int jj = 0; jj < 4; ++jj) pa[jj] = mfma16(af, wf[ks][jj], pa[jj]);
  }
  if (w < 7) {
    const f32x4 v0 = (g >> 1) ? f32x4{pa[0][2], pa[1][2], pa[2][2], pa[3][2]} : f32x4{pa[0][0], pa[1][0], pa[2][0], pa[3][0]};
    const f32x4 v1 = (g >> 1) ? f32x4{pa[0][3], pa[1][3], pa[2][3], pa[3][3]} : f32x4{pa[0][1], pa[1][1], pa[2][1], pa[3][1]};
    st128_dev(h2st, bfp_pair(v0, v1, tg2s), h2);
  }
  STAMP(12);
  {
    v4u fu;
    ld128_dev(fu, h2ld, h2);
    asm volatile("s_waitcnt vmcnt(0)" : "+v"(fu) :: "memory");
    for (unsigned spin = 0; any_bad(pair_bad(fu, tg2l)) && spin < SPIN_CAP; ++spin) reload1(fu, h2ld, h2);
    STAMP(13);
    const f32x4 r0 = bfp_dec(fu.x, fu.y), r1 = bfp_dec(fu.z, fu.w);
    f32x4 t;
#pragma unroll
    for (int e = 0; e < 4; ++e) {
      const auto u = __builtin_amdgcn_permlane32_swap(__float_as_uint(r0[e]), __float_as_uint(r1[e]), false, false);
      const float h = __uint_as_float(u[0]) + __uint_as_float(u[1]);
      const auto v = __builtin_amdgcn_permlane16_swap(__float_as_uint(h), __float_as_uint(h), false, false);
      t[e] = __uint_as_float(v[0]) + __uint_as_float(v[1]);
    }
    *(LAS f32x4*)(L + L_RED + w * 1024 + lane * 16) = t;
  }
  BARRIER();
  if (w == 0) {
    f32x4 t = *(LAS f32x4*)(L + L_RED + lane * 16);
#pragma unroll
    for (int ww = 1; ww < 8; ++ww) t += *(LAS f32x4*)(L + L_RED + ww * 1024 + lane * 16);
    if ((lane & 31) < 14)
      *(v2u*)(out + (((b * SQ + 2 * (j32 >> 3) + (lane >> 5)) * DM + 56 * (j32 & 7) + 4 * (lane & 31)) >> 1)) =
          v2u{pack_bf16(t[0], t[1]), pack_bf16(t[2], t[3])};
  }
  if (w == 1 && (b & 1) == 0 && j32 == 0 && lane == 0) *(unsigned*)(ws + W_EP + rb * 256) = T;
}

}

extern "C" void kernel_launch(void* const* d_in, const int* in_sizes, int n_in, void* d_out, int out_size, void* d_ws,
                              size_t ws_size, hipStream_t stream) {
  (void)in_sizes; (void)n_in; (void)out_size;
  if (ws_size < W_END) return;
  (void)hipFuncSetAttribute((const void*)attn_fused_kernel, hipFuncAttributeMaxDynamicSharedMemorySize, LDS_BYTES);
  attn_fused_kernel<<<dim3(256), dim3(512), LDS_BYTES, stream>>>((const char*)d_in[0], (const char*)d_in[1], (const char*)d_in[2],
                                                                 (const char*)d_in[3], (const char*)d_in[4], (unsigned*)d_out, (char*)d_ws);
}
```

```cpp
#include <hip/hip_runtime.h>
#include <hip/hip_bf16.h>

namespace {

typedef unsigned v4u __attribute__((ext_vector_type(4)));
typedef unsigned v2u __attribute__((ext_vector_type(2)));
typedef float f32x4 __attribute__((ext_vector_type(4)));
typedef float f32x2 __attribute__((ext_vector_type(2)));
typedef __bf16 bf16x8 __attribute__((ext_vector_type(8)));
typedef __bf16 bf16x2 __attribute__((ext_vector_type(2)));
typedef short s16x4 __attribute__((ext_vector_type(4)));
#define LAS __attribute__((address_space(3)))

constexpr int NB = 8, SQ = 8, DM = 448, HKV = 8, SK = 8192, WIN = 2048;
constexpr int NQ = 4096;
constexpr float C2 = 0.0625f * 1.4426950408889634f;
constexpr unsigned SPIN_CAP = 1u << 18;

constexpr int L_RING = 0;
constexpr int L_WQ = L_RING + 65536;
constexpr int L_P = 131072;
constexpr int L_X = L_P;
constexpr int L_QF = L_P + 16384;
constexpr int L_HALF = L_QF;
constexpr int L_MX = L_QF + 8192;
constexpr int L_LS = L_MX + 512;
constexpr int L_QR = L_LS + 512;
constexpr int L_FLAG = L_QR + 4096;
constexpr int L_OB = L_FLAG + 16;
constexpr int L_RED = L_P;
constexpr int LDS_BYTES = 163840;
static_assert(L_OB + 16 * 144 <= LDS_BYTES, "LDS map");

constexpr size_t W_EP = 0;
constexpr size_t W_QX = 4096;
constexpr size_t W_ST = W_QX + (size_t)8 * 4 * 8 * 2048;
constexpr size_t W_H1 = W_ST + (size_t)64 * 4 * 256;
constexpr size_t W_H2 = W_H1 + (size_t)64 * 16 * 2048;
constexpr size_t W_END = W_H2 + (size_t)8 * 32 * 4 * 112 * 16;

__device__ __forceinline__ f32x4 mfma16(v4u a, v4u b, f32x4 c) {
  return __builtin_amdgcn_mfma_f32_16x16x32_bf16(__builtin_bit_cast(bf16x8, a), __builtin_bit_cast(bf16x8, b), c, 0, 0, 0);
}
__device__ __forceinline__ unsigned pack_bf16(float lo, float hi) {
  bf16x2 r; r[0] = (__bf16)lo; r[1] = (__bf16)hi;
  return __builtin_bit_cast(unsigned, r);
}
__device__ __forceinline__ float xg_max(float x) {
  auto s = __builtin_amdgcn_permlane16_swap(__float_as_uint(x), __float_as_uint(x), false, false);
  x = fmaxf(__uint_as_float(s[0]), __uint_as_float(s[1]));
  auto t = __builtin_amdgcn_permlane32_swap(__float_as_uint(x), __float_as_uint(x), false, false);
  return fmaxf(__uint_as_float(t[0]), __uint_as_float(t[1]));
}
__device__ __forceinline__ float xg_sum(float x) {
  auto s = __builtin_amdgcn_permlane16_swap(__float_as_uint(x), __float_as_uint(x), false, false);
  x = __uint_as_float(s[0]) + __uint_as_float(s[1]);
  auto t = __builtin_amdgcn_permlane32_swap(__float_as_uint(x), __float_as_uint(x), false, false);
  return __uint_as_float(t[0]) + __uint_as_float(t[1]);
}
__device__ __forceinline__ unsigned perm_lo(unsigned a, unsigned b) { return __builtin_amdgcn_perm(b, a, 0x05040100u); }
__device__ __forceinline__ unsigned perm_hi(unsigned a, unsigned b) { return __builtin_amdgcn_perm(b, a, 0x07060302u); }

#define KPOL " nt"
#define VPOL " nt"
#define WPOL ""
#define VMWAIT(N) asm volatile("s_waitcnt vmcnt(" #N ")" ::: "memory")
#define LGKM0() asm volatile("s_waitcnt lgkmcnt(0)" ::: "memory")
#define BARRIER() asm volatile("s_waitcnt lgkmcnt(0)\n\ts_barrier" ::: "memory")

__device__ __forceinline__ void dma_half(const char* gbase, unsigned lds_dst, unsigned v0, unsigned v1, unsigned v2, unsigned v3) {
  unsigned keep;
  asm volatile(
      "s_mov_b32 %[keep], m0\n\ts_mov_b32 m0, %[l0]\n\ts_nop 2\n\t"
      "global_load_lds_dwordx4 %[v0], %[sb]" KPOL "\n\t"
      "global_load_lds_dwordx4 %[v1], %[sb] offset:1024" KPOL "\n\t"
      "global_load_lds_dwordx4 %[v2], %[sb] offset:2048" KPOL "\n\t"
      "global_load_lds_dwordx4 %[v3], %[sb] offset:3072" KPOL "\n\t"
      "s_mov_b32 m0, %[keep]"
      : [keep] "=&s"(keep)
      : [v0] "v"(v0), [v1] "v"(v1), [v2] "v"(v2), [v3] "v"(v3), [sb] "s"(gbase), [l0] "s"(lds_dst)
      : "memory");
}
__device__ __forceinline__ void dma1w(const char* sb, unsigned lds_dst, unsigned voff) {
  unsigned keep;
  asm volatile("s_mov_b32 %[keep], m0\n\ts_mov_b32 m0, %[l0]\n\ts_nop 2\n\tglobal_load_lds_dwordx4 %[v0], %[sb]" WPOL "\n\ts_mov_b32 m0, %[keep]"
               : [keep] "=&s"(keep) : [v0] "v"(voff), [sb] "s"(sb), [l0] "s"(lds_dst) : "memory");
}
__device__ __forceinline__ void dma1(const char* sb, unsigned lds_dst, unsigned voff) {
  unsigned keep;
  asm volatile("s_mov_b32 %[keep], m0\n\ts_mov_b32 m0, %[l0]\n\ts_nop 2\n\tglobal_load_lds_dwordx4 %[v0], %[sb]" VPOL "\n\ts_mov_b32 m0, %[keep]"
               : [keep] "=&s"(keep) : [v0] "v"(voff), [sb] "s"(sb), [l0] "s"(lds_dst) : "memory");
}
__device__ __forceinline__ void ld128_nt(v4u& d, unsigned voff, const char* sb) {
  asm volatile("global_load_dwordx4 %0, %1, %2" KPOL : "=v"(d) : "v"(voff), "s"(sb) : "memory");
}
__device__ __forceinline__ void ld64(v2u& d, unsigned voff, const char* sb) {
  asm volatile("global_load_dwordx2 %0, %1, %2" WPOL : "=v"(d) : "v"(voff), "s"(sb) : "memory");
}
__device__ __forceinline__ void ld128_dev(v4u& d, unsigned voff, const char* sb) {
  asm volatile("global_load_dwordx4 %0, %1, %2 sc1" : "=v"(d) : "v"(voff), "s"(sb) : "memory");
}
__device__ __forceinline__ void ld32_dev(unsigned& d, unsigned voff, const char* sb) {
  asm volatile("global_load_dword %0, %1, %2 sc1" : "=v"(d) : "v"(voff), "s"(sb) : "memory");
}
__device__ __forceinline__ void st128_dev(unsigned voff, v4u v, char* sb) {
  asm volatile("global_store_dwordx4 %0, %1, %2 sc1\n\ts_nop 1" : : "v"(voff), "v"(v), "s"(sb) : "memory");
}
__device__ __forceinline__ unsigned tags_bad(v4u u, unsigned T) { return (u.y ^ T) | (u.w ^ T); }
struct PairTag { unsigned a, b; };
__device__ __forceinline__ PairTag pair_tag(unsigned T, unsigned pos) {
  const unsigned t = T >> (8u * (pos & 1u));
  return PairTag{t << 20, (t << 8) & 0xFFF00000u};
}
__device__ __forceinline__ v2u bfp_enc(f32x4 a, unsigned tag) {
  const unsigned m = max(max(max(__float_as_uint(a[0]) & 0x7FFFFFFFu, __float_as_uint(a[1]) & 0x7FFFFFFFu), __float_as_uint(a[2]) & 0x7FFFFFFFu),
                         __float_as_uint(a[3]) & 0x7FFFFFFFu);
  const unsigned e = max(m >> 23, 10u);
  const float sc = __uint_as_float((263u - e) << 23);
  unsigned q[4];
#pragma unroll
  for (int i = 0; i < 4; ++i) q[i] = __float_as_uint(__builtin_amdgcn_fmed3f(a[i] * sc, -1023.f, 1023.f) + 12582912.f);
  return v2u{(q[0] & 0x7FFu) | ((q[1] & 0x7FFu) << 11) | (q[2] << 22), ((q[2] >> 10) & 1u) | ((q[3] & 0x7FFu) << 1) | (e << 12) | tag};
}
__device__ __forceinline__ f32x4 bfp_dec(unsigned d0, unsigned d1) {
  const unsigned e = (d1 >> 12) & 0xFFu;
  const float sc = __uint_as_float(e == 255u ? 0x7F800000u : (e - 9u) << 23);
  const int q0 = __builtin_amdgcn_sbfe((int)d0, 0, 11), q1 = __builtin_amdgcn_sbfe((int)d0, 11, 11);
  const int q2 = __builtin_amdgcn_sbfe((int)__builtin_amdgcn_alignbit(d1, d0, 22), 0, 11), q3 = __builtin_amdgcn_sbfe((int)d1, 1, 11);
  return f32x4{(float)q0 * sc, (float)q1 * sc, (float)q2 * sc, (float)q3 * sc};
}
__device__ __forceinline__ v4u bfp_pair(f32x4 a, f32x4 b, PairTag t) {
  const v2u x = bfp_enc(a, t.a), y = bfp_enc(b, t.b);
  return v4u{x.x, x.y, y.x, y.y};
}
__device__ __forceinline__ unsigned pair_bad(v4u u, PairTag t) { return ((u.y ^ t.a) | (u.w ^ t.b)) & 0xFFF00000u; }
template <class P> __device__ __forceinline__ P* uni(P* p) {
  const unsigned long long a = (unsigned long long)p;
  const unsigned lo = __builtin_amdgcn_readfirstlane((unsigned)a), hi = __builtin_amdgcn_readfirstlane((unsigned)(a >> 32));
  return (P*)(((unsigned long long)hi << 32) | lo);
}
__device__ __forceinline__ bool any_bad(unsigned bad) { return __builtin_amdgcn_ballot_w64(bad != 0u) != 0ull; }
__device__ __forceinline__ void reload1(v4u& d0, unsigned o0, const char* sb) {
  asm volatile("global_load_dwordx4 %0, %1, %2 sc1\n\ts_waitcnt vmcnt(0)" : "=&v"(d0) : "v"(o0), "s"(sb) : "memory");
}
__device__ __forceinline__ void reload2(v4u& d0, v4u& d1, unsigned o0, unsigned o1, const char* sb) {
  asm volatile("global_load_dwordx4 %0, %2, %4 sc1\n\tglobal_load_dwordx4 %1, %3, %4 sc1\n\ts_waitcnt vmcnt(0)"
               : "=&v"(d0), "=&v"(d1) : "v"(o0), "v"(o1), "s"(sb) : "memory");
}

__device__ __forceinline__ void reload12(v4u& d0, v4u& d1, unsigned o0, unsigned o1, unsigned two, const char* sb) {
  asm volatile("global_load_dwordx4 %0, %2, %5 sc1\n\ts_cmp_eq_u32 %4, 0\n\ts_cbranch_scc1 .Lr12_%=\n\t"
               "global_load_dwordx4 %1, %3, %5 sc1\n.Lr12_%=:\n\ts_waitcnt vmcnt(0)"
               : "=&v"(d0), "+v"(d1) : "v"(o0), "v"(o1), "s"(two), "s"(sb) : "memory", "scc");
}

#define STAMP(i) do { } while (0)

__global__ void __launch_bounds__(512, 2)
attn_fused_kernel(const char* __restrict__ x, const char* __restrict__ kc, const char* __restrict__ vc,
                  const char* __restrict__ wq, const char* __restrict__ wo, unsigned* __restrict__ out, char* __restrict__ ws) {
  extern __shared__ __attribute__((aligned(16))) unsigned char lds_raw[];
  LAS unsigned char* L = (LAS unsigned char*)lds_raw;
  const unsigned lbase = (unsigned)(size_t)L;
  const unsigned long long tbeg = __builtin_amdgcn_s_memrealtime();
  STAMP(0);
  const int tid = threadIdx.x, lane = tid & 63, w = __builtin_amdgcn_readfirstlane(tid >> 6);
  const int g = lane >> 4, c = lane & 15;
  const int bid = blockIdx.x, kvh = bid & 7, s = (bid >> 3) & 3, b = bid >> 5;
#define ROT (8 * s + b)
  const int rot = ROT;
  const int rb = b >> 1, cb = (b & 1) * 4 + s;
  asm volatile("" :: "s"(x), "s"(kc), "s"(vc), "s"(wq), "s"(wo), "s"(out), "s"(ws));
  if (tid == 0) *(LAS unsigned*)(L + L_FLAG) = 0u;
  __builtin_amdgcn_s_setprio(3);

  const size_t headrow = (size_t)(b * HKV + kvh) * SK + (SK - WIN) + s * 512;
  const char* kwin = kc + headrow * 512;
  const char* vwin = vc + headrow * 512;
  {
    const unsigned r8 = lane >> 3, f = ((r8 >> 1) & 1) | ((w & 1) << 1);
    const unsigned vo = r8 * (NQ * 2) + (((lane & 7) ^ (f << 1)) << 4);
    const char* wqb = wq + (size_t)(8 * w) * (NQ * 2) + (kvh * 512 + cb * 64) * 2;
#pragma unroll
    for (int i = 0; i < 7; ++i) dma1w(wqb + (size_t)(64 * i) * (NQ * 2), lbase + L_WQ + (8 * i + w) * 1024, vo);
    const char* xb = x + (size_t)(16 * rb) * (DM * 2);
#pragma unroll
    for (int i = 0; i < 2; ++i) {
      const int m = 8 * i + w;
      if (m < 14) {
        const unsigned p = 64 * m + lane, row = (p * 1171u) >> 16, ch = p - 56 * row;
        dma1w(xb, lbase + L_X + m * 1024, row * (DM * 2) + (((ch & ~7u) | ((ch & 7u) ^ ((row >> 1) & 7u))) << 4));
      }
    }
  }
  unsigned ep = 0;
  if (w == 7) ld32_dev(ep, 0u, ws + W_EP + rb * 256);
  asm volatile("s_barrier" ::: "memory");
  unsigned ko[8];
  {
    const unsigned hi = lane >> 5, l31 = lane & 31;
    const unsigned laneK = (hi << 9) | ((l31 ^ hi) << 4);
#pragma unroll
    for (int q = 0; q < 8; ++q) ko[q] = (laneK ^ (unsigned)(q << 5)) + ((q >> 2) << 12);
  }
  auto tile_off = [&](int unit) -> size_t { return (size_t)((unit + rot) & 31) * 8192; };
  auto k_half = [&](int t, int h) {
    dma_half(kwin + tile_off(8 * t + w), lbase + L_RING + ((t & 1) * 8 + w) * 8192 + h * 4096, ko[4 * h], ko[4 * h + 1], ko[4 * h + 2], ko[4 * h + 3]);
  };
  k_half(0, 0);
  asm volatile("s_waitcnt vmcnt(4)" : "+v"(ep) :: "memory");
  if (w == 7 && lane == 0) *(LAS unsigned*)(L + L_FLAG + 4) = ep + 0x10000001u;
  STAMP(1);
  BARRIER();
  const unsigned T = __builtin_amdgcn_readfirstlane(*(LAS unsigned*)(L + L_FLAG + 4));
  char* qx = uni(ws + W_QX + (size_t)(kvh * 4 + rb) * (8 * 2048));
  {
    const int nt = w & 3, kh = w >> 2;
    const int q4 = (lane & 15) >> 2, p4 = lane & 3;
    const unsigned fa = (unsigned)(q4 >> 1) | ((unsigned)(g & 1) << 1);
    const unsigned ta = L_WQ + (224 * kh + 8 * g + q4) * 128 + (((2 * nt + (p4 >> 1)) ^ (fa << 1)) << 4) + (p4 & 1) * 8;
    f32x4 qa = {0.f, 0.f, 0.f, 0.f};
#pragma unroll
    for (int i = 0; i < 7; ++i) {
      const s16x4 a0 = __builtin_amdgcn_ds_read_tr16_b64_v4i16((LAS s16x4*)(L + ta + i * 4096));
      const s16x4 a1 = __builtin_amdgcn_ds_read_tr16_b64_v4i16((LAS s16x4*)(L + ta + i * 4096 + 512));
      const unsigned ch = 4 * (7 * kh + i) + g;
      const v4u xb = *(LAS v4u*)(L + L_X + c * 896 + (((ch & ~7u) | ((ch & 7u) ^ ((c >> 1) & 7u))) << 4));
      const v2u x0 = __builtin_bit_cast(v2u, a0), x1 = __builtin_bit_cast(v2u, a1);
      qa = mfma16(v4u{x0.x, x0.y, x1.x, x1.y}, xb, qa);
    }
    if (kh == 1) *(LAS f32x4*)(L + L_QR + nt * 1024 + lane * 16) = qa;
    STAMP(17);
    BARRIER();
    if (kh == 0) {
      qa += *(LAS f32x4*)(L + L_QR + nt * 1024 + lane * 16);
      f32x4 qb;
#pragma unroll
      for (int i = 0; i < 4; ++i) qb[i] = __int_as_float(__builtin_amdgcn_mov_dpp(__float_as_int(qa[i]), 0xB1, 0xF, 0xF, true));
      const v4u u = bfp_pair(qa, qb, pair_tag(T, c >> 1));
      if ((c & 1) == 0) st128_dev(cb * 2048 + nt * 512 + (c >> 3) * 256 + g * 64 + ((c >> 1) & 3) * 16, u, qx);
    }
  }
  STAMP(18);
#define QSHIM 1
  v4u kreg[8];
  const char* t2b = kwin + tile_off(16 + w);
  k_half(0, 1);
  k_half(1, 0);
  k_half(1, 1);
#pragma unroll
  for (int q = 0; q < QSHIM - 1; ++q) ld128_nt(kreg[q], q * 1024 + lane * 16, t2b);
  if (w < 4) {
    v4u qu[2];
    const unsigned qgo = w * 2048 + (lane >> 4) * 512 + (b & 1) * 256 + (lane & 15) * 16;
    const PairTag tgq = pair_tag(T, lane & 3);
    STAMP(19);
    reload2(qu[0], qu[1], qgo, qgo + 8192, qx);
    STAMP(3);
    for (unsigned spin = 0; any_bad(pair_bad(qu[0], tgq) | pair_bad(qu[1], tgq)) && spin < SPIN_CAP; ++spin) reload2(qu[0], qu[1], qgo, qgo + 8192, qx);
    const int nt = lane >> 4, gq = (lane >> 2) & 3;
    const unsigned qfa = L_QF + (w * 2 + (nt >> 1)) * 1024 + (16 * (2 * (nt & 1) + (gq >> 1)) + 2 * (lane & 3)) * 16 + (gq & 1) * 8;
#pragma unroll
    for (int u = 0; u < 2; ++u) {
      const f32x4 r0 = bfp_dec(qu[u].x, qu[u].y), r1 = bfp_dec(qu[u].z, qu[u].w);
      *(LAS v2u*)(L + qfa + u * 128) = v2u{pack_bf16(r0[0], r0[1]), pack_bf16(r0[2], r0[3])};
      *(LAS v2u*)(L + qfa + u * 128 + 16) = v2u{pack_bf16(r1[0], r1[1]), pack_bf16(r1[2], r1[3])};
    }
    LGKM0();
    if (lane == 0) __hip_atomic_fetch_add((LAS unsigned*)(L + L_FLAG), 1u, __ATOMIC_RELAXED, __HIP_MEMORY_SCOPE_WORKGROUP);
  }
  STAMP(4);
#pragma unroll
  for (int q = QSHIM - 1; q < 8; ++q) ld128_nt(kreg[q], q * 1024 + lane * 16, t2b);
  STAMP(2);
  for (unsigned spin = 0; *(volatile LAS unsigned*)(L + L_FLAG) < 4u && spin < (SPIN_CAP << 6); ++spin) __builtin_amdgcn_s_sleep(1);
  asm volatile("" ::: "memory");
  VMWAIT(16);
  v4u qf[8];
#pragma unroll
  for (int st = 0; st < 8; ++st) qf[st] = *(LAS v4u*)(L + L_QF + st * 1024 + lane * 16);

  f32x4 sc[4];
  auto qk_tile = [&](int slot) -> f32x4 {
    v4u kf[8];
#pragma unroll
    for (int st = 0; st < 8; ++st) kf[st] = *(LAS v4u*)(L + L_RING + slot * 8192 + c * 512 + (((4 * st + g) ^ c) << 4));
    f32x4 a = {0.f, 0.f, 0.f, 0.f};
#pragma unroll
    for (int st = 0; st < 8; ++st) a = mfma16(kf[st], qf[st], a);
    return a;
  };
  const unsigned vlane = (lane >> 3) * 512 + (((lane & 7) ^ (((lane >> 4) & 3) << 1)) << 4);
  auto v_piece = [&](int p, int q) {
    dma1(vwin + tile_off(4 * w + (q >> 1)) + (q & 1) * 4096 + ((s + 1 + p) & 3) * 128,
         lbase + L_RING + ((p & 1) * 8 + w) * 8192 + q * 1024, vlane);
  };
  const int wc = w < 7 ? w : 6;
  v2u wv[4][8];
  const char* wob = wo + (size_t)(2 * kvh * 256 + 64 * s) * (DM * 2) + wc * 128;
  const unsigned wvo = (unsigned)(8 * g) * (DM * 2) + c * 8;
  auto wo_load = [&](int ks, int i) { ld64(wv[ks][i], wvo + (unsigned)(256 * (ks >> 1) + 32 * (ks & 1) + i) * (DM * 2), wob); };

  auto wo_loads = [&](int n0, int n1) {
#pragma unroll
    for (int n = n0; n < n1; ++n) wo_load(n >> 3, n & 7);
  };
  auto v_half = [&](int p, int h) {
#pragma unroll
    for (int q = 4 * h; q < 4 * h + 4; ++q) v_piece(p, q);
  };
#define PRIO(n) __builtin_amdgcn_s_setprio(n)
  sc[0] = qk_tile(w);
  VMWAIT(8);
  sc[1] = qk_tile(8 + w);
  LGKM0();
  PRIO(2);
  wo_loads(0, 4); k_half(3, 0); wo_loads(4, 7);
  asm volatile("s_waitcnt vmcnt(11)"
               : "+v"(kreg[0]), "+v"(kreg[1]), "+v"(kreg[2]), "+v"(kreg[3]), "+v"(kreg[4]), "+v"(kreg[5]), "+v"(kreg[6]), "+v"(kreg[7]) :: "memory");
#pragma unroll
  for (int q = 0; q < 8; ++q) *(LAS v4u*)(L + L_RING + w * 8192 + ko[q] + (q & 3) * 1024) = kreg[q];
  sc[2] = qk_tile(w);
  LGKM0();
  PRIO(1);
  wo_loads(7, 10); k_half(3, 1); wo_loads(10, 13); v_half(0, 0);
  VMWAIT(7);
  sc[3] = qk_tile(8 + w);
  LGKM0();
  PRIO(0);
  wo_loads(13, 16); v_half(0, 1);
  STAMP(5);
  float mx = fmaxf(fmaxf(fmaxf(sc[0][0], sc[0][1]), fmaxf(sc[0][2], sc[0][3])), fmaxf(fmaxf(sc[1][0], sc[1][1]), fmaxf(sc[1][2], sc[1][3])));
  mx = fmaxf(mx, fmaxf(fmaxf(fmaxf(sc[2][0], sc[2][1]), fmaxf(sc[2][2], sc[2][3])), fmaxf(fmaxf(sc[3][0], sc[3][1]), fmaxf(sc[3][2], sc[3][3]))));
  mx = xg_max(mx);
  if (g == 0) *(LAS float*)(L + L_MX + (w * 16 + c) * 4) = mx;
  BARRIER();
  float mrow = *(LAS float*)(L + L_MX + c * 4);
#pragma unroll
  for (int ww = 1; ww < 8; ++ww) mrow = fmaxf(mrow, *(LAS float*)(L + L_MX + (ww * 16 + c) * 4));
  mrow *= C2;
  float lsum = 0.f;
#pragma unroll
  for (int t = 0; t < 4; ++t) {
    float p[4];
#pragma unroll
    for (int r = 0; r < 4; ++r) { p[r] = __builtin_amdgcn_exp2f(fmaf(sc[t][r], C2, -mrow)); lsum += p[r]; }
    *(LAS v2u*)(L + L_P + (4 * t + (w >> 1)) * 1024 + lane * 16 + (w & 1) * 8) = v2u{pack_bf16(p[0], p[1]), pack_bf16(p[2], p[3])};
  }
  lsum = xg_sum(lsum);
  if (g == 0) *(LAS float*)(L + L_LS + (w * 16 + c) * 4) = lsum;
  wo_loads(16, 20); v_half(1, 0);

  const int ct = w & 3, kh = w >> 2;
  unsigned tra;
  {
    const int q4 = (lane & 15) >> 2, p4 = lane & 3;
    tra = (4 * g + q4) * 128 + (((2 * ct + (p4 >> 1)) ^ (((2 * g + (q4 >> 1)) & 3) << 1)) << 4) + (p4 & 1) * 8 + kh * 32768;
  }
  auto pv_pass = [&](int p) -> f32x4 {
    f32x4 a = {0.f, 0.f, 0.f, 0.f};
#pragma unroll
    for (int i = 0; i < 8; ++i) {
      const v4u pf = *(LAS v4u*)(L + L_P + (8 * kh + i) * 1024 + lane * 16);
      const int ub = L_RING + (p & 1) * 65536 + i * 4096;
      const s16x4 a0 = __builtin_amdgcn_ds_read_tr16_b64_v4i16((LAS s16x4*)(L + ub + tra));
      const s16x4 a1 = __builtin_amdgcn_ds_read_tr16_b64_v4i16((LAS s16x4*)(L + ub + 2048 + tra));
      const v2u x0 = __builtin_bit_cast(v2u, a0), x1 = __builtin_bit_cast(v2u, a1);
      a = mfma16(v4u{x0.x, x0.y, x1.x, x1.y}, pf, a);
    }
    return a;
  };
  const int bh = b * HKV + kvh;
  char* stb = uni(ws + W_ST + (size_t)bh * 1024);
  char* h1 = uni(ws + W_H1 + (size_t)bh * (16 * 2048));
  const PairTag tg1 = pair_tag(T, lane);
  auto reduce_pass = [&](f32x4 a) -> v4u {
    *(LAS f32x4*)(L + L_HALF + (kh * 4 + ct) * 1024 + lane * 16) = a;
    BARRIER();
    v4u u = {0u, 0u, 0u, 0u};
    if (w < 2) {
      const LAS unsigned char* hp = L + L_HALF + 2 * w * 1024 + lane * 16;
      u = bfp_pair(*(const LAS f32x4*)hp + *(const LAS f32x4*)(hp + 4096), *(const LAS f32x4*)(hp + 1024) + *(const LAS f32x4*)(hp + 5120), tg1);
    }
    return u;
  };
  auto publish_pass = [&](int p, v4u u) {
    if (w < 2) st128_dev(((((s + 1 + p) & 3) * 4 + s) * 2 + w) * 1024 + lane * 16, u, h1);
  };
  STAMP(6);
  VMWAIT(8);
  BARRIER();
  STAMP(7);
  {
    const v4u a = reduce_pass(pv_pass(0));
    wo_loads(20, 23); v_half(1, 1);
    asm volatile("s_barrier" ::: "memory");
    wo_loads(23, 26); v_half(2, 0);
    publish_pass(0, a);
    if (w == 7) {
      float lt = *(LAS float*)(L + L_LS + c * 4);
#pragma unroll
      for (int ww = 1; ww < 8; ++ww) lt += *(LAS float*)(L + L_LS + (ww * 16 + c) * 4);
      if (g == 0) st128_dev((s * 16 + c) * 16, v4u{__float_as_uint(mrow), T, __float_as_uint(lt), T}, stb);
    }
    if (w < 2 || w == 7) VMWAIT(8); else VMWAIT(7);
  }
  BARRIER();
  v4u stu;
  {
    const v4u a = reduce_pass(pv_pass(1));
    wo_loads(26, 32); v_half(2, 1);
    asm volatile("s_barrier" ::: "memory");
    v_half(3, 0);
    publish_pass(1, a);
  }
  ld128_dev(stu, lane * 16, stb);
  if (w < 2) VMWAIT(6); else VMWAIT(5);
  asm volatile(""
               : "+v"(wv[0][0]), "+v"(wv[0][1]), "+v"(wv[0][2]), "+v"(wv[0][3]), "+v"(wv[0][4]), "+v"(wv[0][5]), "+v"(wv[0][6]), "+v"(wv[0][7]),
                 "+v"(wv[1][0]), "+v"(wv[1][1]), "+v"(wv[1][2]), "+v"(wv[1][3]), "+v"(wv[1][4]), "+v"(wv[1][5]), "+v"(wv[1][6]), "+v"(wv[1][7])
               :: "memory");
  asm volatile("" : "+v"(wv[2][0]), "+v"(wv[2][1]), "+v"(wv[2][2]), "+v"(wv[2][3]), "+v"(wv[2][4]), "+v"(wv[2][5]), "+v"(wv[2][6]), "+v"(wv[2][7]),
                    "+v"(wv[3][0]), "+v"(wv[3][1]), "+v"(wv[3][2]), "+v"(wv[3][3]), "+v"(wv[3][4]), "+v"(wv[3][5]), "+v"(wv[3][6]), "+v"(wv[3][7])
               :: "memory");
  BARRIER();
  publish_pass(2, reduce_pass(pv_pass(2)));
  v_half(3, 1);
  v4u hu[2];
  const int srcA = (s + (kh ? 3 : 1)) & 3, srcB = (s + (kh ? 3 : 2)) & 3;
  const unsigned hoA = ((s * 4 + srcA) * 2 + (ct >> 1)) * 1024 + lane * 16, hoB = ((s * 4 + srcB) * 2 + (ct >> 1)) * 1024 + lane * 16;
  v4u wf[4][4];
#pragma unroll
  for (int ks = 0; ks < 4; ++ks)
#pragma unroll
    for (int jj = 0; jj < 4; ++jj)
#pragma unroll
      for (int r = 0; r < 4; ++r) {
        const unsigned lo = wv[ks][2 * r][jj >> 1], hi = wv[ks][2 * r + 1][jj >> 1];
        wf[ks][jj][r] = (jj & 1) ? perm_hi(lo, hi) : perm_lo(lo, hi);
      }
  const int j32 = kvh * 4 + s;
  char* h2 = uni(ws + W_H2 + (size_t)b * (32 * 4 * 112 * 16));
  unsigned h2st = ((j32 * 4 + 2 * (g & 1) + (g >> 1)) * 112 + wc * 16 + c) * 16;
  const unsigned pos2 = 14 * (j32 & 7) + (c < 14 ? c : 13);
  unsigned h2ld = (((4 * w + g) * 4 + (j32 >> 3)) * 112 + pos2) * 16;
  PairTag tg2s = pair_tag(T, c), tg2l = pair_tag(T, pos2);
  unsigned obw = L_OB + c * 144 + (16 * ct + 4 * g) * 2, obr = L_OB + (c & 7) * 144 + g * 16, hlf = L_HALF + ct * 1024 + lane * 16;
  asm volatile("" : "+v"(wf[0][0]), "+v"(wf[0][1]), "+v"(wf[0][2]), "+v"(wf[0][3]), "+v"(wf[1][0]), "+v"(wf[1][1]), "+v"(wf[1][2]), "+v"(wf[1][3]),
                    "+v"(wf[2][0]), "+v"(wf[2][1]), "+v"(wf[2][2]), "+v"(wf[2][3]), "+v"(wf[3][0]), "+v"(wf[3][1]), "+v"(wf[3][2]), "+v"(wf[3][3]),
                    "+v"(h2st), "+v"(h2ld), "+v"(obw), "+v"(obr), "+v"(hlf), "+v"(tg2s.a), "+v"(tg2s.b), "+v"(tg2l.a), "+v"(tg2l.b));
  asm volatile("s_waitcnt vmcnt(4)" : "+v"(stu) :: "memory");
  for (unsigned spin = 0; any_bad(tags_bad(stu, T)) && spin < SPIN_CAP; ++spin) reload1(stu, lane * 16, stb);
  float fS, fA, fB;
  {
    const float ms = __uint_as_float(stu.x), ls = __uint_as_float(stu.z);
    const float M = xg_max(ms);
    const float e = __builtin_amdgcn_exp2f(ms - M);
    const float fw = e / xg_sum(e * ls);
    fS = __shfl(fw, 16 * s + c); fA = __shfl(fw, 16 * srcA + c); fB = kh ? 0.f : __shfl(fw, 16 * srcB + c);
  }
#define H1LATE 2
  asm volatile("s_waitcnt vmcnt(%0)" :: "n"(H1LATE) : "memory");
  const unsigned two = __builtin_amdgcn_readfirstlane(kh == 0);
  const PairTag tgB = two ? tg1 : PairTag{0u, 0u};
  hu[1] = v4u{0u, 0u, 0u, 0u};
  reload12(hu[0], hu[1], hoA, hoB, two, h1);
  STAMP(8);
  BARRIER();
  f32x4 own = pv_pass(3);
  STAMP(9);
  for (unsigned spin = 0; any_bad(pair_bad(hu[0], tg1) | pair_bad(hu[1], tgB)) && spin < SPIN_CAP; ++spin) reload12(hu[0], hu[1], hoA, hoB, two, h1);
  STAMP(11);
  {
    const unsigned odd = 0u - (unsigned)(ct & 1);
    own = own * fS + bfp_dec((hu[0].x & ~odd) | (hu[0].z & odd), (hu[0].y & ~odd) | (hu[0].w & odd)) * fA
                   + bfp_dec((hu[1].x & ~odd) | (hu[1].z & odd), (hu[1].y & ~odd) | (hu[1].w & odd)) * fB;
    if (kh == 1) *(LAS f32x4*)(L + hlf) = own;
    BARRIER();
    if (kh == 0) {
      own += *(LAS f32x4*)(L + hlf);
      *(LAS v2u*)(L + obw) = v2u{pack_bf16(own[0], own[1]), pack_bf16(own[2], own[3])};
    }
  }
  BARRIER();

  f32x4 pa[4];
#pragma unroll
  for (int jj = 0; jj < 4; ++jj) pa[jj] = f32x4{0.f, 0.f, 0.f, 0.f};
#pragma unroll
  for (int ks = 0; ks < 4; ++ks) {
    const v4u af = *(LAS v4u*)(L + obr + 8 * (ks >> 1) * 144 + 64 * (ks & 1));
#pragma unroll
    for (int jj = 0; jj < 4; ++jj) pa[jj] = mfma16(af, wf[ks][jj], pa[jj]);
  }
  if (w < 7) {
    const f32x4 v0 = (g >> 1) ? f32x4{pa[0][2], pa[1][2], pa[2][2], pa[3][2]} : f32x4{pa[0][0], pa[1][0], pa[2][0], pa[3][0]};
    const f32x4 v1 = (g >> 1) ? f32x4{pa[0][3], pa[1][3], pa[2][3], pa[3][3]} : f32x4{pa[0][1], pa[1][1], pa[2][1], pa[3][1]};
    st128_dev(h2st, bfp_pair(v0, v1, tg2s), h2);
  }
  STAMP(12);
  {
    v4u fu;
    ld128_dev(fu, h2ld, h2);
    asm volatile("s_waitcnt vmcnt(0)" : "+v"(fu) :: "memory");
    for (unsigned spin = 0; any_bad(pair_bad(fu, tg2l)) && spin < SPIN_CAP; ++spin) reload1(fu, h2ld, h2);
    STAMP(13);
    const f32x4 r0 = bfp_dec(fu.x, fu.y), r1 = bfp_dec(fu.z, fu.w);
    f32x4 t;
#pragma unroll
    for (int e = 0; e < 4; ++e) {
      const auto u = __builtin_amdgcn_permlane32_swap(__float_as_uint(r0[e]), __float_as_uint(r1[e]), false, false);
      const float h = __uint_as_float(u[0]) + __uint_as_float(u[1]);
      const auto v = __builtin_amdgcn_permlane16_swap(__float_as_uint(h), __float_as_uint(h), false, false);
      t[e] = __uint_as_float(v[0]) + __uint_as_float(v[1]);
    }
    *(LAS f32x4*)(L + L_RED + w * 1024 + lane * 16) = t;
  }
  BARRIER();
  if (w == 0) {
    f32x4 t = *(LAS f32x4*)(L + L_RED + lane * 16);
#pragma unroll
    for (int ww = 1; ww < 8; ++ww) t += *(LAS f32x4*)(L + L_RED + ww * 1024 + lane * 16);
    if ((lane & 31) < 14)
      *(v2u*)(out + (((b * SQ + 2 * (j32 >> 3) + (lane >> 5)) * DM + 56 * (j32 & 7) + 4 * (lane & 31)) >> 1)) =
          v2u{pack_bf16(t[0], t[1]), pack_bf16(t[2], t[3])};
  }
  if (w == 1 && (b & 1) == 0 && j32 == 0 && lane == 0)
    __hip_atomic_store((unsigned*)(ws + W_EP + rb * 256), T, __ATOMIC_RELAXED, __HIP_MEMORY_SCOPE_AGENT);
  while (__builtin_amdgcn_s_memrealtime() - tbeg < 6000ull) __builtin_amdgcn_s_sleep(4);
}

}

extern "C" void kernel_launch(void* const* d_in, const int* in_sizes, int n_in, void* d_out, int out_size, void* d_ws,
                              size_t ws_size, hipStream_t stream) {
  (void)in_sizes; (void)n_in; (void)out_size;
  if (ws_size < W_END) return;
  (void)hipFuncSetAttribute((const void*)attn_fused_kernel, hipFuncAttributeMaxDynamicSharedMemorySize, LDS_BYTES);
  attn_fused_kernel<<<dim3(256), dim3(512), LDS_BYTES, stream>>>((const char*)d_in[0], (const char*)d_in[1], (const char*)d_in[2],
                                                                 (const char*)d_in[3], (const char*)d_in[4], (unsigned*)d_out, (char*)d_ws);
}
```

```cpp
#include <hip/hip_runtime.h>
#include <hip/hip_bf16.h>

namespace {

typedef unsigned v4u __attribute__((ext_vector_type(4)));
typedef unsigned v2u __attribute__((ext_vector_type(2)));
typedef float f32x4 __attribute__((ext_vector_type(4)));
typedef float f32x2 __attribute__((ext_vector_type(2)));
typedef __bf16 bf16x8 __attribute__((ext_vector_type(8)));
typedef __bf16 bf16x2 __attribute__((ext_vector_type(2)));
typedef short s16x4 __attribute__((ext_vector_type(4)));
#define LAS __attribute__((address_space(3)))

constexpr int NB = 8, SQ = 8, DM = 448, HKV = 8, SK = 8192, WIN = 2048;
constexpr int NQ = 4096;
constexpr float C2 = 0.0625f * 1.4426950408889634f;
constexpr unsigned SPIN_CAP = 1u << 18;

constexpr int L_RING = 0;
constexpr int L_WQ = L_RING + 65536;
constexpr int L_P = 131072;
constexpr int L_X = L_P;
constexpr int L_QF = L_P + 16384;
constexpr int L_HALF = L_QF;
constexpr int L_MX = L_QF + 8192;
constexpr int L_LS = L_MX + 512;
constexpr int L_QR = L_LS + 512;
constexpr int L_FLAG = L_QR + 4096;
constexpr int L_OB = L_FLAG + 16;
constexpr int L_RED = L_P;
constexpr int LDS_BYTES = 163840;
static_assert(L_OB + 16 * 144 <= LDS_BYTES, "LDS map");

constexpr size_t W_EP = 0;
constexpr size_t W_QX = 4096;
constexpr size_t W_ST = W_QX + (size_t)8 * 4 * 8 * 2048;
constexpr size_t W_H1 = W_ST + (size_t)64 * 4 * 256;
constexpr size_t W_H2 = W_H1 + (size_t)64 * 16 * 2048;
constexpr size_t W_END = W_H2 + (size_t)8 * 32 * 4 * 112 * 16;

__device__ __forceinline__ f32x4 mfma16(v4u a, v4u b, f32x4 c) {
  return __builtin_amdgcn_mfma_f32_16x16x32_bf16(__builtin_bit_cast(bf16x8, a), __builtin_bit_cast(bf16x8, b), c, 0, 0, 0);
}
__device__ __forceinline__ unsigned pack_bf16(float lo, float hi) {
  bf16x2 r; r[0] = (__bf16)lo; r[1] = (__bf16)hi;
  return __builtin_bit_cast(unsigned, r);
}
__device__ __forceinline__ float xg_max(float x) {
  auto s = __builtin_amdgcn_permlane16_swap(__float_as_uint(x), __float_as_uint(x), false, false);
  x = fmaxf(__uint_as_float(s[0]), __uint_as_float(s[1]));
  auto t = __builtin_amdgcn_permlane32_swap(__float_as_uint(x), __float_as_uint(x), false, false);
  return fmaxf(__uint_as_float(t[0]), __uint_as_float(t[1]));
}
__device__ __forceinline__ float xg_sum(float x) {
  auto s = __builtin_amdgcn_permlane16_swap(__float_as_uint(x), __float_as_uint(x), false, false);
  x = __uint_as_float(s[0]) + __uint_as_float(s[1]);
  auto t = __builtin_amdgcn_permlane32_swap(__float_as_uint(x), __float_as_uint(x), false, false);
  return __uint_as_float(t[0]) + __uint_as_float(t[1]);
}
__device__ __forceinline__ unsigned perm_lo(unsigned a, unsigned b) { return __builtin_amdgcn_perm(b, a, 0x05040100u); }
__device__ __forceinline__ unsigned perm_hi(unsigned a, unsigned b) { return __builtin_amdgcn_perm(b, a, 0x07060302u); }

#define T2REG 1
#define KPOL " nt"
#define VPOL " nt"
#define WPOL ""
#define VMWAIT(N) asm volatile("s_waitcnt vmcnt(" #N ")" ::: "memory")
#define LGKM0() asm volatile("s_waitcnt lgkmcnt(0)" ::: "memory")
#define BARRIER() asm volatile("s_waitcnt lgkmcnt(0)\n\ts_barrier" ::: "memory")

__device__ __forceinline__ void dma_half(const char* gbase, unsigned lds_dst, unsigned v0, unsigned v1, unsigned v2, unsigned v3) {
  unsigned keep;
  asm volatile(
      "s_mov_b32 %[keep], m0\n\ts_mov_b32 m0, %[l0]\n\ts_nop 2\n\t"
      "global_load_lds_dwordx4 %[v0], %[sb]" KPOL "\n\t"
      "global_load_lds_dwordx4 %[v1], %[sb] offset:1024" KPOL "\n\t"
      "global_load_lds_dwordx4 %[v2], %[sb] offset:2048" KPOL "\n\t"
      "global_load_lds_dwordx4 %[v3], %[sb] offset:3072" KPOL "\n\t"
      "s_mov_b32 m0, %[keep]"
      : [keep] "=&s"(keep)
      : [v0] "v"(v0), [v1] "v"(v1), [v2] "v"(v2), [v3] "v"(v3), [sb] "s"(gbase), [l0] "s"(lds_dst)
      : "memory");
}
__device__ __forceinline__ void dma1w(const char* sb, unsigned lds_dst, unsigned voff) {
  unsigned keep;
  asm volatile("s_mov_b32 %[keep], m0\n\ts_mov_b32 m0, %[l0]\n\ts_nop 2\n\tglobal_load_lds_dwordx4 %[v0], %[sb]" WPOL "\n\ts_mov_b32 m0, %[keep]"
               : [keep] "=&s"(keep) : [v0] "v"(voff), [sb] "s"(sb), [l0] "s"(lds_dst) : "memory");
}
__device__ __forceinline__ void dma1(const char* sb, unsigned lds_dst, unsigned voff) {
  unsigned keep;
  asm volatile("s_mov_b32 %[keep], m0\n\ts_mov_b32 m0, %[l0]\n\ts_nop 2\n\tglobal_load_lds_dwordx4 %[v0], %[sb]" VPOL "\n\ts_mov_b32 m0, %[keep]"
               : [keep] "=&s"(keep) : [v0] "v"(voff), [sb] "s"(sb), [l0] "s"(lds_dst) : "memory");
}
__device__ __forceinline__ void ld128_nt(v4u& d, unsigned voff, const char* sb) {
  asm volatile("global_load_dwordx4 %0, %1, %2" KPOL : "=v"(d) : "v"(voff), "s"(sb) : "memory");
}
__device__ __forceinline__ void ld64(v2u& d, unsigned voff, const char* sb) {
  asm volatile("global_load_dwordx2 %0, %1, %2" WPOL : "=v"(d) : "v"(voff), "s"(sb) : "memory");
}
__device__ __forceinline__ void ld128_dev(v4u& d, unsigned voff, const char* sb) {
  asm volatile("global_load_dwordx4 %0, %1, %2 sc1" : "=v"(d) : "v"(voff), "s"(sb) : "memory");
}
__device__ __forceinline__ void ld32_dev(unsigned& d, unsigned voff, const char* sb) {
  asm volatile("global_load_dword %0, %1, %2 sc1" : "=v"(d) : "v"(voff), "s"(sb) : "memory");
}
__device__ __forceinline__ void st128_dev(unsigned voff, v4u v, char* sb) {
  asm volatile("global_store_dwordx4 %0, %1, %2 sc1\n\ts_nop 1" : : "v"(voff), "v"(v), "s"(sb) : "memory");
}
__device__ __forceinline__ unsigned tags_bad(v4u u, unsigned T) { return (u.y ^ T) | (u.w ^ T); }
struct PairTag { unsigned a, b; };
__device__ __forceinline__ PairTag pair_tag(unsigned T, unsigned pos) {
  const unsigned t = T >> (8u * (pos & 1u));
  return PairTag{t << 20, (t << 8) & 0xFFF00000u};
}
__device__ __forceinline__ v2u bfp_enc(f32x4 a, unsigned tag) {
  const unsigned m = max(max(max(__float_as_uint(a[0]) & 0x7FFFFFFFu, __float_as_uint(a[1]) & 0x7FFFFFFFu), __float_as_uint(a[2]) & 0x7FFFFFFFu),
                         __float_as_uint(a[3]) & 0x7FFFFFFFu);
  const unsigned e = max(m >> 23, 10u);
  const float sc = __uint_as_float((263u - e) << 23);
  unsigned q[4];
#pragma unroll
  for (int i = 0; i < 4; ++i) q[i] = __float_as_uint(__builtin_amdgcn_fmed3f(a[i] * sc, -1023.f, 1023.f) + 12582912.f);
  return v2u{(q[0] & 0x7FFu) | ((q[1] & 0x7FFu) << 11) | (q[2] << 22), ((q[2] >> 10) & 1u) | ((q[3] & 0x7FFu) << 1) | (e << 12) | tag};
}
__device__ __forceinline__ f32x4 bfp_dec(unsigned d0, unsigned d1) {
  const unsigned e = (d1 >> 12) & 0xFFu;
  const float sc = __uint_as_float(e == 255u ? 0x7F800000u : (e - 9u) << 23);
  const int q0 = __builtin_amdgcn_sbfe((int)d0, 0, 11), q1 = __builtin_amdgcn_sbfe((int)d0, 11, 11);
  const int q2 = __builtin_amdgcn_sbfe((int)__builtin_amdgcn_alignbit(d1, d0, 22), 0, 11), q3 = __builtin_amdgcn_sbfe((int)d1, 1, 11);
  return f32x4{(float)q0 * sc, (float)q1 * sc, (float)q2 * sc, (float)q3 * sc};
}
__device__ __forceinline__ v4u bfp_pair(f32x4 a, f32x4 b, PairTag t) {
  const v2u x = bfp_enc(a, t.a), y = bfp_enc(b, t.b);
  return v4u{x.x, x.y, y.x, y.y};
}
__device__ __forceinline__ unsigned pair_bad(v4u u, PairTag t) { return ((u.y ^ t.a) | (u.w ^ t.b)) & 0xFFF00000u; }
template <class P> __device__ __forceinline__ P* uni(P* p) {
  const unsigned long long a = (unsigned long long)p;
  const unsigned lo = __builtin_amdgcn_readfirstlane((unsigned)a), hi = __builtin_amdgcn_readfirstlane((unsigned)(a >> 32));
  return (P*)(((unsigned long long)hi << 32) | lo);
}
__device__ __forceinline__ bool any_bad(unsigned bad) { return __builtin_amdgcn_ballot_w64(bad != 0u) != 0ull; }
__device__ __forceinline__ void reload1(v4u& d0, unsigned o0, const char* sb) {
  asm volatile("global_load_dwordx4 %0, %1, %2 sc1\n\ts_waitcnt vmcnt(0)" : "=&v"(d0) : "v"(o0), "s"(sb) : "memory");
}
__device__ __forceinline__ void reload2(v4u& d0, v4u& d1, unsigned o0, unsigned o1, const char* sb) {
  asm volatile("global_load_dwordx4 %0, %2, %4 sc1\n\tglobal_load_dwordx4 %1, %3, %4 sc1\n\ts_waitcnt vmcnt(0)"
               : "=&v"(d0), "=&v"(d1) : "v"(o0), "v"(o1), "s"(sb) : "memory");
}

__device__ __forceinline__ void reload12(v4u& d0, v4u& d1, unsigned o0, unsigned o1, unsigned two, const char* sb) {
  asm volatile("global_load_dwordx4 %0, %2, %5 sc1\n\ts_cmp_eq_u32 %4, 0\n\ts_cbranch_scc1 .Lr12_%=\n\t"
               "global_load_dwordx4 %1, %3, %5 sc1\n.Lr12_%=:\n\ts_waitcnt vmcnt(0)"
               : "=&v"(d0), "+v"(d1) : "v"(o0), "v"(o1), "s"(two), "s"(sb) : "memory", "scc");
}

#define STAMP(i) do { } while (0)

__global__ void __launch_bounds__(512, 2)
attn_fused_kernel(const char* __restrict__ x, const char* __restrict__ kc, const char* __restrict__ vc,
                  const char* __restrict__ wq, const char* __restrict__ wo, unsigned* __restrict__ out, char* __restrict__ ws) {
  extern __shared__ __attribute__((aligned(16))) unsigned char lds_raw[];
  LAS unsigned char* L = (LAS unsigned char*)lds_raw;
  const unsigned lbase = (unsigned)(size_t)L;
  STAMP(0);
  const int tid = threadIdx.x, lane = tid & 63, w = __builtin_amdgcn_readfirstlane(tid >> 6);
  const int g = lane >> 4, c = lane & 15;
  const int bid = blockIdx.x, kvh = bid & 7, s = (bid >> 3) & 3, b = bid >> 5;
#define ROT (8 * s + b)
  const int rot = ROT;
  const int rb = b >> 1, cb = (b & 1) * 4 + s;
  asm volatile("" :: "s"(x), "s"(kc), "s"(vc), "s"(wq), "s"(wo), "s"(out), "s"(ws));
  if (tid == 0) *(LAS unsigned*)(L + L_FLAG) = 0u;
  __builtin_amdgcn_s_setprio(3);

  const size_t headrow = (size_t)(b * HKV + kvh) * SK + (SK - WIN) + s * 512;
  const char* kwin = kc + headrow * 512;
  const char* vwin = vc + headrow * 512;
  {
    const unsigned r8 = lane >> 3, f = ((r8 >> 1) & 1) | ((w & 1) << 1);
    const unsigned vo = r8 * (NQ * 2) + (((lane & 7) ^ (f << 1)) << 4);
    const char* wqb = wq + (size_t)(8 * w) * (NQ * 2) + (kvh * 512 + cb * 64) * 2;
#pragma unroll
    for (int i = 0; i < 7; ++i) dma1w(wqb + (size_t)(64 * i) * (NQ * 2), lbase + L_WQ + (8 * i + w) * 1024, vo);
    const char* xb = x + (size_t)(16 * rb) * (DM * 2);
#pragma unroll
    for (int i = 0; i < 2; ++i) {
      const int m = 8 * i + w;
      if (m < 14) {
        const unsigned p = 64 * m + lane, row = (p * 1171u) >> 16, ch = p - 56 * row;
        dma1w(xb, lbase + L_X + m * 1024, row * (DM * 2) + (((ch & ~7u) | ((ch & 7u) ^ ((row >> 1) & 7u))) << 4));
      }
    }
  }
  unsigned ep = 0;
  if (w == 7) ld32_dev(ep, 0u, ws + W_EP + rb * 256);
  asm volatile("s_barrier" ::: "memory");
  unsigned ko[8];
  {
    const unsigned hi = lane >> 5, l31 = lane & 31;
    const unsigned laneK = (hi << 9) | ((l31 ^ hi) << 4);
#pragma unroll
    for (int q = 0; q < 8; ++q) ko[q] = (laneK ^ (unsigned)(q << 5)) + ((q >> 2) << 12);
  }
  auto tile_off = [&](int unit) -> size_t { return (size_t)((unit + rot) & 31) * 8192; };
  auto k_half = [&](int t, int h) {
    dma_half(kwin + tile_off(8 * t + w), lbase + L_RING + ((t & 1) * 8 + w) * 8192 + h * 4096, ko[4 * h], ko[4 * h + 1], ko[4 * h + 2], ko[4 * h + 3]);
  };
  k_half(0, 0);
  asm volatile("s_waitcnt vmcnt(4)" : "+v"(ep) :: "memory");
  if (w == 7 && lane == 0) *(LAS unsigned*)(L + L_FLAG + 4) = ep + 0x10000001u;
  STAMP(1);
  BARRIER();
  const unsigned T = __builtin_amdgcn_readfirstlane(*(LAS unsigned*)(L + L_FLAG + 4));
  char* qx = uni(ws + W_QX + (size_t)(kvh * 4 + rb) * (8 * 2048));
  {
    const int nt = w & 3, kh = w >> 2;
    const int q4 = (lane & 15) >> 2, p4 = lane & 3;
    const unsigned fa = (unsigned)(q4 >> 1) | ((unsigned)(g & 1) << 1);
    const unsigned ta = L_WQ + (224 * kh + 8 * g + q4) * 128 + (((2 * nt + (p4 >> 1)) ^ (fa << 1)) << 4) + (p4 & 1) * 8;
    f32x4 qa = {0.f, 0.f, 0.f, 0.f};
#pragma unroll
    for (int i = 0; i < 7; ++i) {
      const s16x4 a0 = __builtin_amdgcn_ds_read_tr16_b64_v4i16((LAS s16x4*)(L + ta + i * 4096));
      const s16x4 a1 = __builtin_amdgcn_ds_read_tr16_b64_v4i16((LAS s16x4*)(L + ta + i * 4096 + 512));
      const unsigned ch = 4 * (7 * kh + i) + g;
      const v4u xb = *(LAS v4u*)(L + L_X + c * 896 + (((ch & ~7u) | ((ch & 7u) ^ ((c >> 1) & 7u))) << 4));
      const v2u x0 = __builtin_bit_cast(v2u, a0), x1 = __builtin_bit_cast(v2u, a1);
      qa = mfma16(v4u{x0.x, x0.y, x1.x, x1.y}, xb, qa);
    }
    if (kh == 1) *(LAS f32x4*)(L + L_QR + nt * 1024 + lane * 16) = qa;
    STAMP(17);
    BARRIER();
    if (kh == 0) {
      qa += *(LAS f32x4*)(L + L_QR + nt * 1024 + lane * 16);
      f32x4 qb;
#pragma unroll
      for (int i = 0; i < 4; ++i) qb[i] = __int_as_float(__builtin_amdgcn_mov_dpp(__float_as_int(qa[i]), 0xB1, 0xF, 0xF, true));
      const v4u u = bfp_pair(qa, qb, pair_tag(T, c >> 1));
      if ((c & 1) == 0) st128_dev(cb * 2048 + nt * 512 + (c >> 3) * 256 + g * 64 + ((c >> 1) & 3) * 16, u, qx);
    }
  }
  STAMP(18);
  k_half(0, 1);
  k_half(1, 0);
  if (w < 4) {
    v4u qu[2];
    const unsigned qgo = w * 2048 + (lane >> 4) * 512 + (b & 1) * 256 + (lane & 15) * 16;
    const PairTag tgq = pair_tag(T, lane & 3);
    STAMP(19);
    reload2(qu[0], qu[1], qgo, qgo + 8192, qx);
    STAMP(3);
    for (unsigned spin = 0; any_bad(pair_bad(qu[0], tgq) | pair_bad(qu[1], tgq)) && spin < SPIN_CAP; ++spin) reload2(qu[0], qu[1], qgo, qgo + 8192, qx);
    const int nt = lane >> 4, gq = (lane >> 2) & 3;
    const unsigned qfa = L_QF + (w * 2 + (nt >> 1)) * 1024 + (16 * (2 * (nt & 1) + (gq >> 1)) + 2 * (lane & 3)) * 16 + (gq & 1) * 8;
#pragma unroll
    for (int u = 0; u < 2; ++u) {
      const f32x4 r0 = bfp_dec(qu[u].x, qu[u].y), r1 = bfp_dec(qu[u].z, qu[u].w);
      *(LAS v2u*)(L + qfa + u * 128) = v2u{pack_bf16(r0[0], r0[1]), pack_bf16(r0[2], r0[3])};
      *(LAS v2u*)(L + qfa + u * 128 + 16) = v2u{pack_bf16(r1[0], r1[1]), pack_bf16(r1[2], r1[3])};
    }
    LGKM0();
    if (lane == 0) __hip_atomic_fetch_add((LAS unsigned*)(L + L_FLAG), 1u, __ATOMIC_RELAXED, __HIP_MEMORY_SCOPE_WORKGROUP);
  }
  STAMP(4);
  k_half(1, 1);
  v4u kreg[8];
  {
    const char* t2b = kwin + tile_off(16 + w);
#pragma unroll
    for (int q = 0; q < 8; ++q) ld128_nt(kreg[q], q * 1024 + lane * 16, t2b);
  }
  STAMP(2);
  for (unsigned spin = 0; *(volatile LAS unsigned*)(L + L_FLAG) < 4u && spin < (SPIN_CAP << 6); ++spin) __builtin_amdgcn_s_sleep(1);
  asm volatile("" ::: "memory");
  VMWAIT(16);
  v4u qf[8];
#pragma unroll
  for (int st = 0; st < 8; ++st) qf[st] = *(LAS v4u*)(L + L_QF + st * 1024 + lane * 16);

  f32x4 sc[4];
  auto qk_tile = [&](int slot) -> f32x4 {
    v4u kf[8];
#pragma unroll
    for (int st = 0; st < 8; ++st) kf[st] = *(LAS v4u*)(L + L_RING + slot * 8192 + c * 512 + (((4 * st + g) ^ c) << 4));
    f32x4 a = {0.f, 0.f, 0.f, 0.f};
#pragma unroll
    for (int st = 0; st < 8; ++st) a = mfma16(kf[st], qf[st], a);
    return a;
  };
  const unsigned vlane = (lane >> 3) * 512 + (((lane & 7) ^ (((lane >> 4) & 3) << 1)) << 4);
  auto v_piece = [&](int p, int q) {
    dma1(vwin + tile_off(4 * w + (q >> 1)) + (q & 1) * 4096 + ((s + 1 + p) & 3) * 128,
         lbase + L_RING + ((p & 1) * 8 + w) * 8192 + q * 1024, vlane);
  };
  const int wc = w < 7 ? w : 6;
  v2u wv[4][8];
  const char* wob = wo + (size_t)(2 * kvh * 256 + 64 * s) * (DM * 2) + wc * 128;
  const unsigned wvo = (unsigned)(8 * g) * (DM * 2) + c * 8;
  auto wo_load = [&](int ks, int i) { ld64(wv[ks][i], wvo + (unsigned)(256 * (ks >> 1) + 32 * (ks & 1) + i) * (DM * 2), wob); };

  auto wo_loads = [&](int n0, int n1) {
#pragma unroll
    for (int n = n0; n < n1; ++n) wo_load(n >> 3, n & 7);
  };
  auto v_half = [&](int p, int h) {
#pragma unroll
    for (int q = 4 * h; q < 4 * h + 4; ++q) v_piece(p, q);
  };
#define PRIO(n) __builtin_amdgcn_s_setprio(n)
  sc[0] = qk_tile(w);
  VMWAIT(8);
  sc[1] = qk_tile(8 + w);
  LGKM0();
  PRIO(2);
  wo_loads(0, 4); k_half(3, 0); wo_loads(4, 7);
  asm volatile("s_waitcnt vmcnt(11)"
               : "+v"(kreg[0]), "+v"(kreg[1]), "+v"(kreg[2]), "+v"(kreg[3]), "+v"(kreg[4]), "+v"(kreg[5]), "+v"(kreg[6]), "+v"(kreg[7]) :: "memory");
#pragma unroll
  for (int q = 0; q < 8; ++q) *(LAS v4u*)(L + L_RING + w * 8192 + ko[q] + (q & 3) * 1024) = kreg[q];
  sc[2] = qk_tile(w);
  LGKM0();
  PRIO(1);
  wo_loads(7, 10); k_half(3, 1); wo_loads(10, 13); v_half(0, 0);
  VMWAIT(7);
  sc[3] = qk_tile(8 + w);
  LGKM0();
  PRIO(0);
  wo_loads(13, 16); v_half(0, 1);
  STAMP(5);
  float mx = fmaxf(fmaxf(fmaxf(sc[0][0], sc[0][1]), fmaxf(sc[0][2], sc[0][3])), fmaxf(fmaxf(sc[1][0], sc[1][1]), fmaxf(sc[1][2], sc[1][3])));
  mx = fmaxf(mx, fmaxf(fmaxf(fmaxf(sc[2][0], sc[2][1]), fmaxf(sc[2][2], sc[2][3])), fmaxf(fmaxf(sc[3][0], sc[3][1]), fmaxf(sc[3][2], sc[3][3]))));
  mx = xg_max(mx);
  if (g == 0) *(LAS float*)(L + L_MX + (w * 16 + c) * 4) = mx;
  BARRIER();
  float mrow = *(LAS float*)(L + L_MX + c * 4);
#pragma unroll
  for (int ww = 1; ww < 8; ++ww) mrow = fmaxf(mrow, *(LAS float*)(L + L_MX + (ww * 16 + c) * 4));
  mrow *= C2;
  float lsum = 0.f;
#pragma unroll
  for (int t = 0; t < 4; ++t) {
    float p[4];
#pragma unroll
    for (int r = 0; r < 4; ++r) { p[r] = __builtin_amdgcn_exp2f(fmaf(sc[t][r], C2, -mrow)); lsum += p[r]; }
    *(LAS v2u*)(L + L_P + (4 * t + (w >> 1)) * 1024 + lane * 16 + (w & 1) * 8) = v2u{pack_bf16(p[0], p[1]), pack_bf16(p[2], p[3])};
  }
  lsum = xg_sum(lsum);
  if (g == 0) *(LAS float*)(L + L_LS + (w * 16 + c) * 4) = lsum;
  wo_loads(16, 20); v_half(1, 0);

  const int ct = w & 3, kh = w >> 2;
  unsigned tra;
  {
    const int q4 = (lane & 15) >> 2, p4 = lane & 3;
    tra = (4 * g + q4) * 128 + (((2 * ct + (p4 >> 1)) ^ (((2 * g + (q4 >> 1)) & 3) << 1)) << 4) + (p4 & 1) * 8 + kh * 32768;
  }
  auto pv_pass = [&](int p) -> f32x4 {
    f32x4 a = {0.f, 0.f, 0.f, 0.f};
#pragma unroll
    for (int i = 0; i < 8; ++i) {
      const v4u pf = *(LAS v4u*)(L + L_P + (8 * kh + i) * 1024 + lane * 16);
      const int ub = L_RING + (p & 1) * 65536 + i * 4096;
      const s16x4 a0 = __builtin_amdgcn_ds_read_tr16_b64_v4i16((LAS s16x4*)(L + ub + tra));
      const s16x4 a1 = __builtin_amdgcn_ds_read_tr16_b64_v4i16((LAS s16x4*)(L + ub + 2048 + tra));
      const v2u x0 = __builtin_bit_cast(v2u, a0), x1 = __builtin_bit_cast(v2u, a1);
      a = mfma16(v4u{x0.x, x0.y, x1.x, x1.y}, pf, a);
    }
    return a;
  };
  const int bh = b * HKV + kvh;
  char* stb = uni(ws + W_ST + (size_t)bh * 1024);
  char* h1 = uni(ws + W_H1 + (size_t)bh * (16 * 2048));
  const PairTag tg1 = pair_tag(T, lane);
  auto reduce_pass = [&](f32x4 a) -> v4u {
    *(LAS f32x4*)(L + L_HALF + (kh * 4 + ct) * 1024 + lane * 16) = a;
    BARRIER();
    v4u u = {0u, 0u, 0u, 0u};
    if (w < 2) {
      const LAS unsigned char* hp = L + L_HALF + 2 * w * 1024 + lane * 16;
      u = bfp_pair(*(const LAS f32x4*)hp + *(const LAS f32x4*)(hp + 4096), *(const LAS f32x4*)(hp + 1024) + *(const LAS f32x4*)(hp + 5120), tg1);
    }
    return u;
  };
  auto publish_pass = [&](int p, v4u u) {
    if (w < 2) st128_dev(((((s + 1 + p) & 3) * 4 + s) * 2 + w) * 1024 + lane * 16, u, h1);
  };
  STAMP(6);
  VMWAIT(8);
  BARRIER();
  STAMP(7);
  {
    const v4u a = reduce_pass(pv_pass(0));
    wo_loads(20, 23); v_half(1, 1);
    asm volatile("s_barrier" ::: "memory");
    wo_loads(23, 26); v_half(2, 0);
    publish_pass(0, a);
    if (w == 7) {
      float lt = *(LAS float*)(L + L_LS + c * 4);
#pragma unroll
      for (int ww = 1; ww < 8; ++ww) lt += *(LAS float*)(L + L_LS + (ww * 16 + c) * 4);
      if (g == 0) st128_dev((s * 16 + c) * 16, v4u{__float_as_uint(mrow), T, __float_as_uint(lt), T}, stb);
    }
    if (w < 2 || w == 7) VMWAIT(8); else VMWAIT(7);
  }
  BARRIER();
  v4u stu;
  {
    const v4u a = reduce_pass(pv_pass(1));
    wo_loads(26, 32); v_half(2, 1);
    asm volatile("s_barrier" ::: "memory");
    v_half(3, 0);
    publish_pass(1, a);
  }
  ld128_dev(stu, lane * 16, stb);
  if (w < 2) VMWAIT(6); else VMWAIT(5);
  asm volatile(""
               : "+v"(wv[0][0]), "+v"(wv[0][1]), "+v"(wv[0][2]), "+v"(wv[0][3]), "+v"(wv[0][4]), "+v"(wv[0][5]), "+v"(wv[0][6]), "+v"(wv[0][7]),
                 "+v"(wv[1][0]), "+v"(wv[1][1]), "+v"(wv[1][2]), "+v"(wv[1][3]), "+v"(wv[1][4]), "+v"(wv[1][5]), "+v"(wv[1][6]), "+v"(wv[1][7])
               :: "memory");
  asm volatile("" : "+v"(wv[2][0]), "+v"(wv[2][1]), "+v"(wv[2][2]), "+v"(wv[2][3]), "+v"(wv[2][4]), "+v"(wv[2][5]), "+v"(wv[2][6]), "+v"(wv[2][7]),
                    "+v"(wv[3][0]), "+v"(wv[3][1]), "+v"(wv[3][2]), "+v"(wv[3][3]), "+v"(wv[3][4]), "+v"(wv[3][5]), "+v"(wv[3][6]), "+v"(wv[3][7])
               :: "memory");
  BARRIER();
  publish_pass(2, reduce_pass(pv_pass(2)));
  v_half(3, 1);
  v4u hu[2];
  const int srcA = (s + (kh ? 3 : 1)) & 3, srcB = (s + (kh ? 3 : 2)) & 3;
  const unsigned hoA = ((s * 4 + srcA) * 2 + (ct >> 1)) * 1024 + lane * 16, hoB = ((s * 4 + srcB) * 2 + (ct >> 1)) * 1024 + lane * 16;
  v4u wf[4][4];
#pragma unroll
  for (int ks = 0; ks < 4; ++ks)
#pragma unroll
    for (int jj = 0; jj < 4; ++jj)
#pragma unroll
      for (int r = 0; r < 4; ++r) {
        const unsigned lo = wv[ks][2 * r][jj >> 1], hi = wv[ks][2 * r + 1][jj >> 1];
        wf[ks][jj][r] = (jj & 1) ? perm_hi(lo, hi) : perm_lo(lo, hi);
      }
  const int j32 = kvh * 4 + s;
  char* h2 = uni(ws + W_H2 + (size_t)b * (32 * 4 * 112 * 16));
  unsigned h2st = ((j32 * 4 + 2 * (g & 1) + (g >> 1)) * 112 + wc * 16 + c) * 16;
  const unsigned pos2 = 14 * (j32 & 7) + (c < 14 ? c : 13);
  unsigned h2ld = (((4 * w + g) * 4 + (j32 >> 3)) * 112 + pos2) * 16;
  PairTag tg2s = pair_tag(T, c), tg2l = pair_tag(T, pos2);
  unsigned obw = L_OB + c * 144 + (16 * ct + 4 * g) * 2, obr = L_OB + (c & 7) * 144 + g * 16, hlf = L_HALF + ct * 1024 + lane * 16;
  asm volatile("" : "+v"(wf[0][0]), "+v"(wf[0][1]), "+v"(wf[0][2]), "+v"(wf[0][3]), "+v"(wf[1][0]), "+v"(wf[1][1]), "+v"(wf[1][2]), "+v"(wf[1][3]),
                    "+v"(wf[2][0]), "+v"(wf[2][1]), "+v"(wf[2][2]), "+v"(wf[2][3]), "+v"(wf[3][0]), "+v"(wf[3][1]), "+v"(wf[3][2]), "+v"(wf[3][3]),
                    "+v"(h2st), "+v"(h2ld), "+v"(obw), "+v"(obr), "+v"(hlf), "+v"(tg2s.a), "+v"(tg2s.b), "+v"(tg2l.a), "+v"(tg2l.b));
  asm volatile("s_waitcnt vmcnt(4)" : "+v"(stu) :: "memory");
  for (unsigned spin = 0; any_bad(tags_bad(stu, T)) && spin < SPIN_CAP; ++spin) reload1(stu, lane * 16, stb);
  float fS, fA, fB;
  {
    const float ms = __uint_as_float(stu.x), ls = __uint_as_float(stu.z);
    const float M = xg_max(ms);
    const float e = __builtin_amdgcn_exp2f(ms - M);
    const float fw = e / xg_sum(e * ls);
    fS = __shfl(fw, 16 * s + c); fA = __shfl(fw, 16 * srcA + c); fB = kh ? 0.f : __shfl(fw, 16 * srcB + c);
  }
#define H1LATE 2
  asm volatile("s_waitcnt vmcnt(%0)" :: "n"(H1LATE) : "memory");
  const unsigned two = __builtin_amdgcn_readfirstlane(kh == 0);
  const PairTag tgB = two ? tg1 : PairTag{0u, 0u};
  hu[1] = v4u{0u, 0u, 0u, 0u};
  reload12(hu[0], hu[1], hoA, hoB, two, h1);
  STAMP(8);
  BARRIER();
  f32x4 own = pv_pass(3);
  STAMP(9);
  for (unsigned spin = 0; any_bad(pair_bad(hu[0], tg1) | pair_bad(hu[1], tgB)) && spin < SPIN_CAP; ++spin) reload12(hu[0], hu[1], hoA, hoB, two, h1);
  STAMP(11);
  {
    const unsigned odd = 0u - (unsigned)(ct & 1);
    own = own * fS + bfp_dec((hu[0].x & ~odd) | (hu[0].z & odd), (hu[0].y & ~odd) | (hu[0].w & odd)) * fA
                   + bfp_dec((hu[1].x & ~odd) | (hu[1].z & odd), (hu[1].y & ~odd) | (hu[1].w & odd)) * fB;
    if (kh == 1) *(LAS f32x4*)(L + hlf) = own;
    BARRIER();
    if (kh == 0) {
      own += *(LAS f32x4*)(L + hlf);
      *(LAS v2u*)(L + obw) = v2u{pack_bf16(own[0], own[1]), pack_bf16(own[2], own[3])};
    }
  }
  BARRIER();

  f32x4 pa[4];
#pragma unroll
  for (int jj = 0; jj < 4; ++jj) pa[jj] = f32x4{0.f, 0.f, 0.f, 0.f};
#pragma unroll
  for (int ks = 0; ks < 4; ++ks) {
    const v4u af = *(LAS v4u*)(L + obr + 8 * (ks >> 1) * 144 + 64 * (ks & 1));
#pragma unroll
    for (int jj = 0; jj < 4; ++jj) pa[jj] = mfma16(af, wf[ks][jj], pa[jj]);
  }
  if (w < 7) {
    const f32x4 v0 = (g >> 1) ? f32x4{pa[0][2], pa[1][2], pa[2][2], pa[3][2]} : f32x4{pa[0][0], pa[1][0], pa[2][0], pa[3][0]};
    const f32x4 v1 = (g >> 1) ? f32x4{pa[0][3], pa[1][3], pa[2][3], pa[3][3]} : f32x4{pa[0][1], pa[1][1], pa[2][1], pa[3][1]};
    st128_dev(h2st, bfp_pair(v0, v1, tg2s), h2);
  }
  STAMP(12);
  {
    v4u fu;
    ld128_dev(fu, h2ld, h2);
    asm volatile("s_waitcnt vmcnt(0)" : "+v"(fu) :: "memory");
    for (unsigned spin = 0; any_bad(pair_bad(fu, tg2l)) && spin < SPIN_CAP; ++spin) reload1(fu, h2ld, h2);
    STAMP(13);
    const f32x4 r0 = bfp_dec(fu.x, fu.y), r1 = bfp_dec(fu.z, fu.w);
    f32x4 t;
#pragma unroll
    for (int e = 0; e < 4; ++e) {
      const auto u = __builtin_amdgcn_permlane32_swap(__float_as_uint(r0[e]), __float_as_uint(r1[e]), false, false);
      const float h = __uint_as_float(u[0]) + __uint_as_float(u[1]);
      const auto v = __builtin_amdgcn_permlane16_swap(__float_as_uint(h), __float_as_uint(h), false, false);
      t[e] = __uint_as_float(v[0]) + __uint_as_float(v[1]);
    }
    *(LAS f32x4*)(L + L_RED + w * 1024 + lane * 16) = t;
  }
  BARRIER();
  if (w == 0) {
    f32x4 t = *(LAS f32x4*)(L + L_RED + lane * 16);
#pragma unroll
    for (int ww = 1; ww < 8; ++ww) t += *(LAS f32x4*)(L + L_RED + ww * 1024 + lane * 16);
    if ((lane & 31) < 14)
      *(v2u*)(out + (((b * SQ + 2 * (j32 >> 3) + (lane >> 5)) * DM + 56 * (j32 & 7) + 4 * (lane & 31)) >> 1)) =
          v2u{pack_bf16(t[0], t[1]), pack_bf16(t[2], t[3])};
  }
  if (w == 1 && (b & 1) == 0 && j32 == 0 && lane == 0) *(unsigned*)(ws + W_EP + rb * 256) = T;
}

}

extern "C" void kernel_launch(void* const* d_in, const int* in_sizes, int n_in, void* d_out, int out_size, void* d_ws,
                              size_t ws_size, hipStream_t stream) {
  (void)in_sizes; (void)n_in; (void)out_size;
  if (ws_size < W_END) return;
  (void)hipFuncSetAttribute((const void*)attn_fused_kernel, hipFuncAttributeMaxDynamicSharedMemorySize, LDS_BYTES);
  attn_fused_kernel<<<dim3(256), dim3(512), LDS_BYTES, stream>>>((const char*)d_in[0], (const char*)d_in[1], (const char*)d_in[2],
                                                                 (const char*)d_in[3], (const char*)d_in[4], (unsigned*)d_out, (char*)d_ws);
}
```

```cpp
#include <hip/hip_runtime.h>
#include <hip/hip_bf16.h>

namespace {

typedef unsigned v4u __attribute__((ext_vector_type(4)));
typedef unsigned v2u __attribute__((ext_vector_type(2)));
typedef float f32x4 __attribute__((ext_vector_type(4)));
typedef __bf16 bf16x8 __attribute__((ext_vector_type(8)));
typedef __bf16 bf16x2 __attribute__((ext_vector_type(2)));
typedef short s16x4 __attribute__((ext_vector_type(4)));
#define LAS __attribute__((address_space(3)))

constexpr int SQ = 8, DM = 448, HKV = 8, SK = 8192, WIN = 2048;
constexpr int NQ = 4096;
constexpr float C2 = 0.0625f * 1.4426950408889634f;
constexpr unsigned SPIN_CAP = 1u << 18;

constexpr int L_RING = 0;
constexpr int L_WQ = L_RING + 65536;
constexpr int L_P = 131072;
constexpr int L_X = L_P;
constexpr int L_QF = L_P + 16384;
constexpr int L_HALF = L_QF;
constexpr int L_MX = L_QF + 8192;
constexpr int L_LS = L_MX + 512;
constexpr int L_QR = L_LS + 512;
constexpr int L_FLAG = L_QR + 4096;
constexpr int L_OB = L_FLAG + 16;
constexpr int L_RED = L_P;
constexpr int LDS_BYTES = 163840;
static_assert(L_OB + 16 * 144 <= LDS_BYTES, "LDS map");

constexpr size_t W_EP = 0;
constexpr size_t W_QX = 4096;
constexpr size_t W_ST = W_QX + (size_t)8 * 4 * 8 * 2048;
constexpr size_t W_H1 = W_ST + (size_t)64 * 4 * 256;
constexpr size_t W_H2 = W_H1 + (size_t)64 * 16 * 2048;
constexpr size_t W_END = W_H2 + (size_t)8 * 32 * 4 * 112 * 16;

__device__ __forceinline__ f32x4 mfma16(v4u a, v4u b, f32x4 c) {
  return __builtin_amdgcn_mfma_f32_16x16x32_bf16(__builtin_bit_cast(bf16x8, a), __builtin_bit_cast(bf16x8, b), c, 0, 0, 0);
}
__device__ __forceinline__ unsigned pack_bf16(float lo, float hi) {
  bf16x2 r; r[0] = (__bf16)lo; r[1] = (__bf16)hi;
  return __builtin_bit_cast(unsigned, r);
}
__device__ __forceinline__ float xg_max(float x) {
  auto s = __builtin_amdgcn_permlane16_swap(__float_as_uint(x), __float_as_uint(x), false, false);
  x = fmaxf(__uint_as_float(s[0]), __uint_as_float(s[1]));
  auto t = __builtin_amdgcn_permlane32_swap(__float_as_uint(x), __float_as_uint(x), false, false);
  return fmaxf(__uint_as_float(t[0]), __uint_as_float(t[1]));
}
__device__ __forceinline__ float xg_sum(float x) {
  auto s = __builtin_amdgcn_permlane16_swap(__float_as_uint(x), __float_as_uint(x), false, false);
  x = __uint_as_float(s[0]) + __uint_as_float(s[1]);
  auto t = __builtin_amdgcn_permlane32_swap(__float_as_uint(x), __float_as_uint(x), false, false);
  return __uint_as_float(t[0]) + __uint_as_float(t[1]);
}
__device__ __forceinline__ unsigned perm_lo(unsigned a, unsigned b) { return __builtin_amdgcn_perm(b, a, 0x05040100u); }
__device__ __forceinline__ unsigned perm_hi(unsigned a, unsigned b) { return __builtin_amdgcn_perm(b, a, 0x07060302u); }

#define VMWAIT(N) asm volatile("s_waitcnt vmcnt(" #N ")" ::: "memory")
#define LGKM0() asm volatile("s_waitcnt lgkmcnt(0)" ::: "memory")
#define BARRIER() asm volatile("s_waitcnt lgkmcnt(0)\n\ts_barrier" ::: "memory")

__device__ __forceinline__ void dma_half(const char* gbase, unsigned lds_dst, unsigned v0, unsigned v1, unsigned v2, unsigned v3) {
  unsigned keep;
  asm volatile(
      "s_mov_b32 %[keep], m0\n\ts_mov_b32 m0, %[l0]\n\ts_nop 2\n\t"
      "global_load_lds_dwordx4 %[v0], %[sb] nt\n\t"
      "global_load_lds_dwordx4 %[v1], %[sb] offset:1024 nt\n\t"
      "global_load_lds_dwordx4 %[v2], %[sb] offset:2048 nt\n\t"
      "global_load_lds_dwordx4 %[v3], %[sb] offset:3072 nt\n\t"
      "s_mov_b32 m0, %[keep]"
      : [keep] "=&s"(keep)
      : [v0] "v"(v0), [v1] "v"(v1), [v2] "v"(v2), [v3] "v"(v3), [sb] "s"(gbase), [l0] "s"(lds_dst)
      : "memory");
}
__device__ __forceinline__ void dma1w(const char* sb, unsigned lds_dst, unsigned voff) {
  unsigned keep;
  asm volatile("s_mov_b32 %[keep], m0\n\ts_mov_b32 m0, %[l0]\n\ts_nop 2\n\tglobal_load_lds_dwordx4 %[v0], %[sb]\n\ts_mov_b32 m0, %[keep]"
               : [keep] "=&s"(keep) : [v0] "v"(voff), [sb] "s"(sb), [l0] "s"(lds_dst) : "memory");
}
__device__ __forceinline__ void dma1(const char* sb, unsigned lds_dst, unsigned voff) {
  unsigned keep;
  asm volatile("s_mov_b32 %[keep], m0\n\ts_mov_b32 m0, %[l0]\n\ts_nop 2\n\tglobal_load_lds_dwordx4 %[v0], %[sb] nt\n\ts_mov_b32 m0, %[keep]"
               : [keep] "=&s"(keep) : [v0] "v"(voff), [sb] "s"(sb), [l0] "s"(lds_dst) : "memory");
}
__device__ __forceinline__ void ld128_nt(v4u& d, unsigned voff, const char* sb) {
  asm volatile("global_load_dwordx4 %0, %1, %2 nt" : "=v"(d) : "v"(voff), "s"(sb) : "memory");
}
__device__ __forceinline__ void ld64(v2u& d, unsigned voff, const char* sb) {
  asm volatile("global_load_dwordx2 %0, %1, %2" : "=v"(d) : "v"(voff), "s"(sb) : "memory");
}
__device__ __forceinline__ void ld128_dev(v4u& d, unsigned voff, const char* sb) {
  asm volatile("global_load_dwordx4 %0, %1, %2 sc1" : "=v"(d) : "v"(voff), "s"(sb) : "memory");
}
__device__ __forceinline__ void ld32_dev(unsigned& d, unsigned voff, const char* sb) {
  asm volatile("global_load_dword %0, %1, %2 sc1" : "=v"(d) : "v"(voff), "s"(sb) : "memory");
}
__device__ __forceinline__ void st128_dev(unsigned voff, v4u v, char* sb) {
  asm volatile("global_store_dwordx4 %0, %1, %2 sc1\n\ts_nop 1" : : "v"(voff), "v"(v), "s"(sb) : "memory");
}
__device__ __forceinline__ unsigned tags_bad(v4u u, unsigned T) { return (u.y ^ T) | (u.w ^ T); }
struct PairTag { unsigned a, b; };
__device__ __forceinline__ PairTag pair_tag(unsigned T, unsigned pos) {
  const unsigned t = T >> (8u * (pos & 1u));
  return PairTag{t << 20, (t << 8) & 0xFFF00000u};
}
__device__ __forceinline__ v2u bfp_enc(f32x4 a, unsigned tag) {
  const unsigned m = max(max(max(__float_as_uint(a[0]) & 0x7FFFFFFFu, __float_as_uint(a[1]) & 0x7FFFFFFFu), __float_as_uint(a[2]) & 0x7FFFFFFFu),
                         __float_as_uint(a[3]) & 0x7FFFFFFFu);
  const unsigned e = max(m >> 23, 10u);
  const float sc = __uint_as_float((263u - e) << 23);
  unsigned q[4];
#pragma unroll
  for (int i = 0; i < 4; ++i) q[i] = __float_as_uint(__builtin_amdgcn_fmed3f(a[i] * sc, -1023.f, 1023.f) + 12582912.f);
  return v2u{(q[0] & 0x7FFu) | ((q[1] & 0x7FFu) << 11) | (q[2] << 22), ((q[2] >> 10) & 1u) | ((q[3] & 0x7FFu) << 1) | (e << 12) | tag};
}
__device__ __forceinline__ f32x4 bfp_dec(unsigned d0, unsigned d1) {
  const unsigned e = (d1 >> 12) & 0xFFu;
  const float sc = __uint_as_float(e == 255u ? 0x7F800000u : (e - 9u) << 23);
  const int q0 = __builtin_amdgcn_sbfe((int)d0, 0, 11), q1 = __builtin_amdgcn_sbfe((int)d0, 11, 11);
  const int q2 = __builtin_amdgcn_sbfe((int)__builtin_amdgcn_alignbit(d1, d0, 22), 0, 11), q3 = __builtin_amdgcn_sbfe((int)d1, 1, 11);
  return f32x4{(float)q0 * sc, (float)q1 * sc, (float)q2 * sc, (float)q3 * sc};
}
__device__ __forceinline__ v4u bfp_pair(f32x4 a, f32x4 b, PairTag t) {
  const v2u x = bfp_enc(a, t.a), y = bfp_enc(b, t.b);
  return v4u{x.x, x.y, y.x, y.y};
}
__device__ __forceinline__ unsigned pair_bad(v4u u, PairTag t) { return ((u.y ^ t.a) | (u.w ^ t.b)) & 0xFFF00000u; }
template <class P> __device__ __forceinline__ P* uni(P* p) {
  const unsigned long long a = (unsigned long long)p;
  const unsigned lo = __builtin_amdgcn_readfirstlane((unsigned)a), hi = __builtin_amdgcn_readfirstlane((unsigned)(a >> 32));
  return (P*)(((unsigned long long)hi << 32) | lo);
}
__device__ __forceinline__ bool any_bad(unsigned bad) { return __builtin_amdgcn_ballot_w64(bad != 0u) != 0ull; }
__device__ __forceinline__ void reload1(v4u& d0, unsigned o0, const char* sb) {
  asm volatile("global_load_dwordx4 %0, %1, %2 sc1\n\ts_waitcnt vmcnt(0)" : "=&v"(d0) : "v"(o0), "s"(sb) : "memory");
}
__device__ __forceinline__ void reload2(v4u& d0, v4u& d1, unsigned o0, unsigned o1, const char* sb) {
  asm volatile("global_load_dwordx4 %0, %2, %4 sc1\n\tglobal_load_dwordx4 %1, %3, %4 sc1\n\ts_waitcnt vmcnt(0)"
               : "=&v"(d0), "=&v"(d1) : "v"(o0), "v"(o1), "s"(sb) : "memory");
}

__device__ __forceinline__ void reload12(v4u& d0, v4u& d1, unsigned o0, unsigned o1, unsigned two, const char* sb) {
  asm volatile("global_load_dwordx4 %0, %2, %5 sc1\n\ts_cmp_eq_u32 %4, 0\n\ts_cbranch_scc1 .Lr12_%=\n\t"
               "global_load_dwordx4 %1, %3, %5 sc1\n.Lr12_%=:\n\ts_waitcnt vmcnt(0)"
               : "=&v"(d0), "+v"(d1) : "v"(o0), "v"(o1), "s"(two), "s"(sb) : "memory", "scc");
}

__global__ void __launch_bounds__(512, 2)
attn_fused_kernel(const char* __restrict__ x, const char* __restrict__ kc, const char* __restrict__ vc,
                  const char* __restrict__ wq, const char* __restrict__ wo, unsigned* __restrict__ out, char* __restrict__ ws) {
  extern __shared__ __attribute__((aligned(16))) unsigned char lds_raw[];
  LAS unsigned char* L = (LAS unsigned char*)lds_raw;
  const unsigned lbase = (unsigned)(size_t)L;
  const int tid = threadIdx.x, lane = tid & 63, w = __builtin_amdgcn_readfirstlane(tid >> 6);
  const int g = lane >> 4, c = lane & 15;
  const int bid = blockIdx.x, kvh = bid & 7, s = (bid >> 3) & 3, b = bid >> 5;
  const int rot = 8 * s + b;
  const int rb = b >> 1, cb = (b & 1) * 4 + s;
  asm volatile("" :: "s"(x), "s"(kc), "s"(vc), "s"(wq), "s"(wo), "s"(out), "s"(ws));
  if (tid == 0) *(LAS unsigned*)(L + L_FLAG) = 0u;

  const size_t headrow = (size_t)(b * HKV + kvh) * SK + (SK - WIN) + s * 512;
  const char* kwin = kc + headrow * 512;
  const char* vwin = vc + headrow * 512;
  {
    const unsigned r8 = lane >> 3, f = ((r8 >> 1) & 1) | ((w & 1) << 1);
    const unsigned vo = r8 * (NQ * 2) + (((lane & 7) ^ (f << 1)) << 4);
    const char* wqb = wq + (size_t)(8 * w) * (NQ * 2) + (kvh * 512 + cb * 64) * 2;
#pragma unroll
    for (int i = 0; i < 7; ++i) dma1w(wqb + (size_t)(64 * i) * (NQ * 2), lbase + L_WQ + (8 * i + w) * 1024, vo);
    const char* xb = x + (size_t)(16 * rb) * (DM * 2);
#pragma unroll
    for (int i = 0; i < 2; ++i) {
      const int m = 8 * i + w;
      if (m < 14) {
        const unsigned p = 64 * m + lane, row = (p * 1171u) >> 16, ch = p - 56 * row;
        dma1w(xb, lbase + L_X + m * 1024, row * (DM * 2) + (((ch & ~7u) | ((ch & 7u) ^ ((row >> 1) & 7u))) << 4));
      }
    }
  }
  unsigned ep = 0;
  if (w == 7) ld32_dev(ep, 0u, ws + W_EP + rb * 256);
  asm volatile("s_barrier" ::: "memory");
  unsigned ko[8];
  {
    const unsigned hi = lane >> 5, l31 = lane & 31;
    const unsigned laneK = (hi << 9) | ((l31 ^ hi) << 4);
#pragma unroll
    for (int q = 0; q < 8; ++q) ko[q] = (laneK ^ (unsigned)(q << 5)) + ((q >> 2) << 12);
  }
  auto tile_off = [&](int unit) -> size_t { return (size_t)((unit + rot) & 31) * 8192; };
  auto k_half = [&](int t, int h) {
    dma_half(kwin + tile_off(8 * t + w), lbase + L_RING + ((t & 1) * 8 + w) * 8192 + h * 4096, ko[4 * h], ko[4 * h + 1], ko[4 * h + 2], ko[4 * h + 3]);
  };
  k_half(0, 0);
  asm volatile("s_waitcnt vmcnt(4)" : "+v"(ep) :: "memory");
  if (w == 7 && lane == 0) *(LAS unsigned*)(L + L_FLAG + 4) = ep + 0x10000001u;
  BARRIER();
  const unsigned T = __builtin_amdgcn_readfirstlane(*(LAS unsigned*)(L + L_FLAG + 4));
  char* qx = uni(ws + W_QX + (size_t)(kvh * 4 + rb) * (8 * 2048));
  {
    const int nt = w & 3, kh = w >> 2;
    const int q4 = (lane & 15) >> 2, p4 = lane & 3;
    const unsigned fa = (unsigned)(q4 >> 1) | ((unsigned)(g & 1) << 1);
    const unsigned ta = L_WQ + (224 * kh + 8 * g + q4) * 128 + (((2 * nt + (p4 >> 1)) ^ (fa << 1)) << 4) + (p4 & 1) * 8;
    f32x4 qa = {0.f, 0.f, 0.f, 0.f};
#pragma unroll
    for (int i = 0; i < 7; ++i) {
      const s16x4 a0 = __builtin_amdgcn_ds_read_tr16_b64_v4i16((LAS s16x4*)(L + ta + i * 4096));
      const s16x4 a1 = __builtin_amdgcn_ds_read_tr16_b64_v4i16((LAS s16x4*)(L + ta + i * 4096 + 512));
      const unsigned ch = 4 * (7 * kh + i) + g;
      const v4u xb = *(LAS v4u*)(L + L_X + c * 896 + (((ch & ~7u) | ((ch & 7u) ^ ((c >> 1) & 7u))) << 4));
      const v2u x0 = __builtin_bit_cast(v2u, a0), x1 = __builtin_bit_cast(v2u, a1);
      qa = mfma16(v4u{x0.x, x0.y, x1.x, x1.y}, xb, qa);
    }
    if (kh == 1) *(LAS f32x4*)(L + L_QR + nt * 1024 + lane * 16) = qa;
    BARRIER();
    if (kh == 0) {
      qa += *(LAS f32x4*)(L + L_QR + nt * 1024 + lane * 16);
      f32x4 qb;
#pragma unroll
      for (int i = 0; i < 4; ++i) qb[i] = __int_as_float(__builtin_amdgcn_mov_dpp(__float_as_int(qa[i]), 0xB1, 0xF, 0xF, true));
      const v4u u = bfp_pair(qa, qb, pair_tag(T, c >> 1));
      if ((c & 1) == 0) st128_dev(cb * 2048 + nt * 512 + (c >> 3) * 256 + g * 64 + ((c >> 1) & 3) * 16, u, qx);
    }
  }
  v4u kreg[8];
  const char* t2b = kwin + tile_off(16 + w);
  k_half(0, 1);
  k_half(1, 0);
  k_half(1, 1);
  if (w < 4) {
    v4u qu[2];
    const unsigned qgo = w * 2048 + (lane >> 4) * 512 + (b & 1) * 256 + (lane & 15) * 16;
    const PairTag tgq = pair_tag(T, lane & 3);
    reload2(qu[0], qu[1], qgo, qgo + 8192, qx);
    for (unsigned spin = 0; any_bad(pair_bad(qu[0], tgq) | pair_bad(qu[1], tgq)) && spin < SPIN_CAP; ++spin) reload2(qu[0], qu[1], qgo, qgo + 8192, qx);
    const int nt = lane >> 4, gq = (lane >> 2) & 3;
    const unsigned qfa = L_QF + (w * 2 + (nt >> 1)) * 1024 + (16 * (2 * (nt & 1) + (gq >> 1)) + 2 * (lane & 3)) * 16 + (gq & 1) * 8;
#pragma unroll
    for (int u = 0; u < 2; ++u) {
      const f32x4 r0 = bfp_dec(qu[u].x, qu[u].y), r1 = bfp_dec(qu[u].z, qu[u].w);
      *(LAS v2u*)(L + qfa + u * 128) = v2u{pack_bf16(r0[0], r0[1]), pack_bf16(r0[2], r0[3])};
      *(LAS v2u*)(L + qfa + u * 128 + 16) = v2u{pack_bf16(r1[0], r1[1]), pack_bf16(r1[2], r1[3])};
    }
    LGKM0();
    if (lane == 0) __hip_atomic_fetch_add((LAS unsigned*)(L + L_FLAG), 1u, __ATOMIC_RELAXED, __HIP_MEMORY_SCOPE_WORKGROUP);
  }
#pragma unroll
  for (int q = 0; q < 8; ++q) ld128_nt(kreg[q], q * 1024 + lane * 16, t2b);
  for (unsigned spin = 0; *(volatile LAS unsigned*)(L + L_FLAG) < 4u && spin < (SPIN_CAP << 6); ++spin) __builtin_amdgcn_s_sleep(1);
  asm volatile("" ::: "memory");
  VMWAIT(16);
  v4u qf[8];
#pragma unroll
  for (int st = 0; st < 8; ++st) qf[st] = *(LAS v4u*)(L + L_QF + st * 1024 + lane * 16);

  f32x4 sc[4];
  auto qk_tile = [&](int slot) -> f32x4 {
    v4u kf[8];
#pragma unroll
    for (int st = 0; st < 8; ++st) kf[st] = *(LAS v4u*)(L + L_RING + slot * 8192 + c * 512 + (((4 * st + g) ^ c) << 4));
    f32x4 a = {0.f, 0.f, 0.f, 0.f};
#pragma unroll
    for (int st = 0; st < 8; ++st) a = mfma16(kf[st], qf[st], a);
    return a;
  };
  const unsigned vlane = (lane >> 3) * 512 + (((lane & 7) ^ (((lane >> 4) & 3) << 1)) << 4);
  auto v_piece = [&](int p, int q) {
    dma1(vwin + tile_off(4 * w + (q >> 1)) + (q & 1) * 4096 + ((s + 1 + p) & 3) * 128,
         lbase + L_RING + ((p & 1) * 8 + w) * 8192 + q * 1024, vlane);
  };
  const int wc = w < 7 ? w : 6;
  v2u wv[4][8];
  const char* wob = wo + (size_t)(2 * kvh * 256 + 64 * s) * (DM * 2) + wc * 128;
  const unsigned wvo = (unsigned)(8 * g) * (DM * 2) + c * 8;
  auto wo_load = [&](int ks, int i) { ld64(wv[ks][i], wvo + (unsigned)(256 * (ks >> 1) + 32 * (ks & 1) + i) * (DM * 2), wob); };

  auto wo_loads = [&](int n0, int n1) {
#pragma unroll
    for (int n = n0; n < n1; ++n) wo_load(n >> 3, n & 7);
  };
  auto v_half = [&](int p, int h) {
#pragma unroll
    for (int q = 4 * h; q < 4 * h + 4; ++q) v_piece(p, q);
  };
  sc[0] = qk_tile(w);
  VMWAIT(8);
  sc[1] = qk_tile(8 + w);
  LGKM0();
  wo_loads(0, 4); k_half(3, 0); wo_loads(4, 7);
  asm volatile("s_waitcnt vmcnt(11)"
               : "+v"(kreg[0]), "+v"(kreg[1]), "+v"(kreg[2]), "+v"(kreg[3]), "+v"(kreg[4]), "+v"(kreg[5]), "+v"(kreg[6]), "+v"(kreg[7]) :: "memory");
#pragma unroll
  for (int q = 0; q < 8; ++q) *(LAS v4u*)(L + L_RING + w * 8192 + ko[q] + (q & 3) * 1024) = kreg[q];
  sc[2] = qk_tile(w);
  LGKM0();
  wo_loads(7, 10); k_half(3, 1); wo_loads(10, 13); v_half(0, 0);
  VMWAIT(7);
  sc[3] = qk_tile(8 + w);
  LGKM0();
  wo_loads(13, 16); v_half(0, 1);
  float mx = fmaxf(fmaxf(fmaxf(sc[0][0], sc[0][1]), fmaxf(sc[0][2], sc[0][3])), fmaxf(fmaxf(sc[1][0], sc[1][1]), fmaxf(sc[1][2], sc[1][3])));
  mx = fmaxf(mx, fmaxf(fmaxf(fmaxf(sc[2][0], sc[2][1]), fmaxf(sc[2][2], sc[2][3])), fmaxf(fmaxf(sc[3][0], sc[3][1]), fmaxf(sc[3][2], sc[3][3]))));
  mx = xg_max(mx);
  if (g == 0) *(LAS float*)(L + L_MX + (w * 16 + c) * 4) = mx;
  BARRIER();
  float mrow = *(LAS float*)(L + L_MX + c * 4);
#pragma unroll
  for (int ww = 1; ww < 8; ++ww) mrow = fmaxf(mrow, *(LAS float*)(L + L_MX + (ww * 16 + c) * 4));
  mrow *= C2;
  float lsum = 0.f;
#pragma unroll
  for (int t = 0; t < 4; ++t) {
    float p[4];
#pragma unroll
    for (int r = 0; r < 4; ++r) { p[r] = __builtin_amdgcn_exp2f(fmaf(sc[t][r], C2, -mrow)); lsum += p[r]; }
    *(LAS v2u*)(L + L_P + (4 * t + (w >> 1)) * 1024 + lane * 16 + (w & 1) * 8) = v2u{pack_bf16(p[0], p[1]), pack_bf16(p[2], p[3])};
  }
  lsum = xg_sum(lsum);
  if (g == 0) *(LAS float*)(L + L_LS + (w * 16 + c) * 4) = lsum;
  wo_loads(16, 20); v_half(1, 0);

  const int ct = w & 3, kh = w >> 2;
  unsigned tra;
  {
    const int q4 = (lane & 15) >> 2, p4 = lane & 3;
    tra = (4 * g + q4) * 128 + (((2 * ct + (p4 >> 1)) ^ (((2 * g + (q4 >> 1)) & 3) << 1)) << 4) + (p4 & 1) * 8 + kh * 32768;
  }
  auto pv_pass = [&](int p) -> f32x4 {
    f32x4 a = {0.f, 0.f, 0.f, 0.f};
#pragma unroll
    for (int i = 0; i < 8; ++i) {
      const v4u pf = *(LAS v4u*)(L + L_P + (8 * kh + i) * 1024 + lane * 16);
      const int ub = L_RING + (p & 1) * 65536 + i * 4096;
      const s16x4 a0 = __builtin_amdgcn_ds_read_tr16_b64_v4i16((LAS s16x4*)(L + ub + tra));
      const s16x4 a1 = __builtin_amdgcn_ds_read_tr16_b64_v4i16((LAS s16x4*)(L + ub + 2048 + tra));
      const v2u x0 = __builtin_bit_cast(v2u, a0), x1 = __builtin_bit_cast(v2u, a1);
      a = mfma16(v4u{x0.x, x0.y, x1.x, x1.y}, pf, a);
    }
    return a;
  };
  const int bh = b * HKV + kvh;
  char* stb = uni(ws + W_ST + (size_t)bh * 1024);
  char* h1 = uni(ws + W_H1 + (size_t)bh * (16 * 2048));
  const PairTag tg1 = pair_tag(T, lane);
  auto reduce_pass = [&](f32x4 a) -> v4u {
    *(LAS f32x4*)(L + L_HALF + (kh * 4 + ct) * 1024 + lane * 16) = a;
    BARRIER();
    v4u u = {0u, 0u, 0u, 0u};
    if (w < 2) {
      const LAS unsigned char* hp = L + L_HALF + 2 * w * 1024 + lane * 16;
      u = bfp_pair(*(const LAS f32x4*)hp + *(const LAS f32x4*)(hp + 4096), *(const LAS f32x4*)(hp + 1024) + *(const LAS f32x4*)(hp + 5120), tg1);
    }
    return u;
  };
  auto publish_pass = [&](int p, v4u u) {
    if (w < 2) st128_dev(((((s + 1 + p) & 3) * 4 + s) * 2 + w) * 1024 + lane * 16, u, h1);
  };
  VMWAIT(8);
  BARRIER();
  {
    const v4u a = reduce_pass(pv_pass(0));
    wo_loads(20, 23); v_half(1, 1);
    asm volatile("s_barrier" ::: "memory");
    wo_loads(23, 26); v_half(2, 0);
    publish_pass(0, a);
    if (w == 7) {
      float lt = *(LAS float*)(L + L_LS + c * 4);
#pragma unroll
      for (int ww = 1; ww < 8; ++ww) lt += *(LAS float*)(L + L_LS + (ww * 16 + c) * 4);
      if (g == 0) st128_dev((s * 16 + c) * 16, v4u{__float_as_uint(mrow), T, __float_as_uint(lt), T}, stb);
    }
    if (w < 2 || w == 7) VMWAIT(8); else VMWAIT(7);
  }
  BARRIER();
  v4u stu;
  {
    const v4u a = reduce_pass(pv_pass(1));
    wo_loads(26, 32); v_half(2, 1);
    asm volatile("s_barrier" ::: "memory");
    v_half(3, 0);
    publish_pass(1, a);
  }
  ld128_dev(stu, lane * 16, stb);
  if (w < 2) VMWAIT(6); else VMWAIT(5);
  asm volatile(""
               : "+v"(wv[0][0]), "+v"(wv[0][1]), "+v"(wv[0][2]), "+v"(wv[0][3]), "+v"(wv[0][4]), "+v"(wv[0][5]), "+v"(wv[0][6]), "+v"(wv[0][7]),
                 "+v"(wv[1][0]), "+v"(wv[1][1]), "+v"(wv[1][2]), "+v"(wv[1][3]), "+v"(wv[1][4]), "+v"(wv[1][5]), "+v"(wv[1][6]), "+v"(wv[1][7])
               :: "memory");
  asm volatile("" : "+v"(wv[2][0]), "+v"(wv[2][1]), "+v"(wv[2][2]), "+v"(wv[2][3]), "+v"(wv[2][4]), "+v"(wv[2][5]), "+v"(wv[2][6]), "+v"(wv[2][7]),
                    "+v"(wv[3][0]), "+v"(wv[3][1]), "+v"(wv[3][2]), "+v"(wv[3][3]), "+v"(wv[3][4]), "+v"(wv[3][5]), "+v"(wv[3][6]), "+v"(wv[3][7])
               :: "memory");
  BARRIER();
  publish_pass(2, reduce_pass(pv_pass(2)));
  v_half(3, 1);
  v4u hu[2];
  const int srcA = (s + (kh ? 3 : 1)) & 3, srcB = (s + (kh ? 3 : 2)) & 3;
  const unsigned hoA = ((s * 4 + srcA) * 2 + (ct >> 1)) * 1024 + lane * 16, hoB = ((s * 4 + srcB) * 2 + (ct >> 1)) * 1024 + lane * 16;
  v4u wf[4][4];
#pragma unroll
  for (int ks = 0; ks < 4; ++ks)
#pragma unroll
    for (int jj = 0; jj < 4; ++jj)
#pragma unroll
      for (int r = 0; r < 4; ++r) {
        const unsigned lo = wv[ks][2 * r][jj >> 1], hi = wv[ks][2 * r + 1][jj >> 1];
        wf[ks][jj][r] = (jj & 1) ? perm_hi(lo, hi) : perm_lo(lo, hi);
      }
  const int j32 = kvh * 4 + s;
  char* h2 = uni(ws + W_H2 + (size_t)b * (32 * 4 * 112 * 16));
  unsigned h2st = ((j32 * 4 + 2 * (g & 1) + (g >> 1)) * 112 + wc * 16 + c) * 16;
  const unsigned pos2 = 14 * (j32 & 7) + (c < 14 ? c : 13);
  unsigned h2ld = (((4 * w + g) * 4 + (j32 >> 3)) * 112 + pos2) * 16;
  PairTag tg2s = pair_tag(T, c), tg2l = pair_tag(T, pos2);
  unsigned obw = L_OB + c * 144 + (16 * ct + 4 * g) * 2, obr = L_OB + (c & 7) * 144 + g * 16, hlf = L_HALF + ct * 1024 + lane * 16;
  asm volatile("" : "+v"(wf[0][0]), "+v"(wf[0][1]), "+v"(wf[0][2]), "+v"(wf[0][3]), "+v"(wf[1][0]), "+v"(wf[1][1]), "+v"(wf[1][2]), "+v"(wf[1][3]),
                    "+v"(wf[2][0]), "+v"(wf[2][1]), "+v"(wf[2][2]), "+v"(wf[2][3]), "+v"(wf[3][0]), "+v"(wf[3][1]), "+v"(wf[3][2]), "+v"(wf[3][3]),
                    "+v"(h2st), "+v"(h2ld), "+v"(obw), "+v"(obr), "+v"(hlf), "+v"(tg2s.a), "+v"(tg2s.b), "+v"(tg2l.a), "+v"(tg2l.b));
  asm volatile("s_waitcnt vmcnt(4)" : "+v"(stu) :: "memory");
  for (unsigned spin = 0; any_bad(tags_bad(stu, T)) && spin < SPIN_CAP; ++spin) reload1(stu, lane * 16, stb);
  float fS, fA, fB;
  {
    const float ms = __uint_as_float(stu.x), ls = __uint_as_float(stu.z);
    const float M = xg_max(ms);
    const float e = __builtin_amdgcn_exp2f(ms - M);
    const float fw = e / xg_sum(e * ls);
    fS = __shfl(fw, 16 * s + c); fA = __shfl(fw, 16 * srcA + c); fB = kh ? 0.f : __shfl(fw, 16 * srcB + c);
  }
  VMWAIT(2);
  const unsigned two = __builtin_amdgcn_readfirstlane(kh == 0);
  const PairTag tgB = two ? tg1 : PairTag{0u, 0u};
  hu[1] = v4u{0u, 0u, 0u, 0u};
  reload12(hu[0], hu[1], hoA, hoB, two, h1);
  BARRIER();
  f32x4 own = pv_pass(3);
  for (unsigned spin = 0; any_bad(pair_bad(hu[0], tg1) | pair_bad(hu[1], tgB)) && spin < SPIN_CAP; ++spin) reload12(hu[0], hu[1], hoA, hoB, two, h1);
  {
    const unsigned odd = 0u - (unsigned)(ct & 1);
    own = own * fS + bfp_dec((hu[0].x & ~odd) | (hu[0].z & odd), (hu[0].y & ~odd) | (hu[0].w & odd)) * fA
                   + bfp_dec((hu[1].x & ~odd) | (hu[1].z & odd), (hu[1].y & ~odd) | (hu[1].w & odd)) * fB;
    if (kh == 1) *(LAS f32x4*)(L + hlf) = own;
    BARRIER();
    if (kh == 0) {
      own += *(LAS f32x4*)(L + hlf);
      *(LAS v2u*)(L + obw) = v2u{pack_bf16(own[0], own[1]), pack_bf16(own[2], own[3])};
    }
  }
  BARRIER();

  f32x4 pa[4];
#pragma unroll
  for (int jj = 0; jj < 4; ++jj) pa[jj] = f32x4{0.f, 0.f, 0.f, 0.f};
#pragma unroll
  for (int ks = 0; ks < 4; ++ks) {
    const v4u af = *(LAS v4u*)(L + obr + 8 * (ks >> 1) * 144 + 64 * (ks & 1));
#pragma unroll
    for (int jj = 0; jj < 4; ++jj) pa[jj] = mfma16(af, wf[ks][jj], pa[jj]);
  }
  if (w < 7) {
    const f32x4 v0 = (g >> 1) ? f32x4{pa[0][2], pa[1][2], pa[2][2], pa[3][2]} : f32x4{pa[0][0], pa[1][0], pa[2][0], pa[3][0]};
    const f32x4 v1 = (g >> 1) ? f32x4{pa[0][3], pa[1][3], pa[2][3], pa[3][3]} : f32x4{pa[0][1], pa[1][1], pa[2][1], pa[3][1]};
    st128_dev(h2st, bfp_pair(v0, v1, tg2s), h2);
  }
  {
    v4u fu;
    ld128_dev(fu, h2ld, h2);
    asm volatile("s_waitcnt vmcnt(0)" : "+v"(fu) :: "memory");
    for (unsigned spin = 0; any_bad(pair_bad(fu, tg2l)) && spin < SPIN_CAP; ++spin) reload1(fu, h2ld, h2);
    const f32x4 r0 = bfp_dec(fu.x, fu.y), r1 = bfp_dec(fu.z, fu.w);
    f32x4 t;
#pragma unroll
    for (int e = 0; e < 4; ++e) {
      const auto u = __builtin_amdgcn_permlane32_swap(__float_as_uint(r0[e]), __float_as_uint(r1[e]), false, false);
      const float h = __uint_as_float(u[0]) + __uint_as_float(u[1]);
      const auto v = __builtin_amdgcn_permlane16_swap(__float_as_uint(h), __float_as_uint(h), false, false);
      t[e] = __uint_as_float(v[0]) + __uint_as_float(v[1]);
    }
    *(LAS f32x4*)(L + L_RED + w * 1024 + lane * 16) = t;
  }
  BARRIER();
  if (w == 0) {
    f32x4 t = *(LAS f32x4*)(L + L_RED + lane * 16);
#pragma unroll
    for (int ww = 1; ww < 8; ++ww) t += *(LAS f32x4*)(L + L_RED + ww * 1024 + lane * 16);
    if ((lane & 31) < 14)
      *(v2u*)(out + (((b * SQ + 2 * (j32 >> 3) + (lane >> 5)) * DM + 56 * (j32 & 7) + 4 * (lane & 31)) >> 1)) =
          v2u{pack_bf16(t[0], t[1]), pack_bf16(t[2], t[3])};
  }
  if (w == 1 && (b & 1) == 0 && j32 == 0 && lane == 0)
    __hip_atomic_store((unsigned*)(ws + W_EP + rb * 256), T, __ATOMIC_RELAXED, __HIP_MEMORY_SCOPE_AGENT);
}

}

extern "C" void kernel_launch(void* const* d_in, const int* in_sizes, int n_in, void* d_out, int out_size, void* d_ws,
                              size_t ws_size, hipStream_t stream) {
  (void)in_sizes; (void)n_in; (void)out_size;
  if (ws_size < W_END) return;
  (void)hipFuncSetAttribute((const void*)attn_fused_kernel, hipFuncAttributeMaxDynamicSharedMemorySize, LDS_BYTES);
  attn_fused_kernel<<<dim3(256), dim3(512), LDS_BYTES, stream>>>((const char*)d_in[0], (const char*)d_in[1], (const char*)d_in[2],
                                                                 (const char*)d_in[3], (const char*)d_in[4], (unsigned*)d_out, (char*)d_ws);
}
```

```cpp
#define PROBE 14
#define CODEPF 8
#include <hip/hip_runtime.h>
#include <hip/hip_bf16.h>

namespace {

typedef unsigned v4u __attribute__((ext_vector_type(4)));
typedef unsigned v2u __attribute__((ext_vector_type(2)));
typedef float f32x4 __attribute__((ext_vector_type(4)));
typedef float f32x2 __attribute__((ext_vector_type(2)));
typedef __bf16 bf16x8 __attribute__((ext_vector_type(8)));
typedef __bf16 bf16x2 __attribute__((ext_vector_type(2)));
typedef short s16x4 __attribute__((ext_vector_type(4)));
#define LAS __attribute__((address_space(3)))

constexpr int NB = 8, SQ = 8, DM = 448, HKV = 8, SK = 8192, WIN = 2048;
constexpr int NQ = 4096;
constexpr float C2 = 0.0625f * 1.4426950408889634f;
constexpr unsigned SPIN_CAP = 1u << 18;

constexpr int L_RING = 0;
constexpr int L_WQ = L_RING + 65536;
constexpr int L_P = 131072;
constexpr int L_X = L_P;
constexpr int L_QF = L_P + 16384;
constexpr int L_HALF = L_QF;
constexpr int L_MX = L_QF + 8192;
constexpr int L_LS = L_MX + 512;
constexpr int L_QR = L_LS + 512;
constexpr int L_FLAG = L_QR + 4096;
constexpr int L_OB = L_FLAG + 16;
constexpr int L_RED = L_P;
constexpr int LDS_BYTES = 163840;
static_assert(L_OB + 16 * 144 <= LDS_BYTES, "LDS map");

constexpr size_t W_EP = 0;
constexpr size_t W_QX = 4096;
constexpr size_t W_ST = W_QX + (size_t)8 * 4 * 8 * 2048;
constexpr size_t W_H1 = W_ST + (size_t)64 * 4 * 256;
constexpr size_t W_H2 = W_H1 + (size_t)64 * 16 * 2048;
constexpr size_t W_END = W_H2 + (size_t)8 * 32 * 4 * 112 * 16;

__device__ __forceinline__ f32x4 mfma16(v4u a, v4u b, f32x4 c) {
  return __builtin_amdgcn_mfma_f32_16x16x32_bf16(__builtin_bit_cast(bf16x8, a), __builtin_bit_cast(bf16x8, b), c, 0, 0, 0);
}
__device__ __forceinline__ unsigned pack_bf16(float lo, float hi) {
  bf16x2 r; r[0] = (__bf16)lo; r[1] = (__bf16)hi;
  return __builtin_bit_cast(unsigned, r);
}
__device__ __forceinline__ float xg_max(float x) {
  auto s = __builtin_amdgcn_permlane16_swap(__float_as_uint(x), __float_as_uint(x), false, false);
  x = fmaxf(__uint_as_float(s[0]), __uint_as_float(s[1]));
  auto t = __builtin_amdgcn_permlane32_swap(__float_as_uint(x), __float_as_uint(x), false, false);
  return fmaxf(__uint_as_float(t[0]), __uint_as_float(t[1]));
}
__device__ __forceinline__ float xg_sum(float x) {
  auto s = __builtin_amdgcn_permlane16_swap(__float_as_uint(x), __float_as_uint(x), false, false);
  x = __uint_as_float(s[0]) + __uint_as_float(s[1]);
  auto t = __builtin_amdgcn_permlane32_swap(__float_as_uint(x), __float_as_uint(x), false, false);
  return __uint_as_float(t[0]) + __uint_as_float(t[1]);
}
__device__ __forceinline__ unsigned perm_lo(unsigned a, unsigned b) { return __builtin_amdgcn_perm(b, a, 0x05040100u); }
__device__ __forceinline__ unsigned perm_hi(unsigned a, unsigned b) { return __builtin_amdgcn_perm(b, a, 0x07060302u); }

#define KPOL " nt"
#define VPOL " nt"
#define WPOL ""
#define VMWAIT(N) asm volatile("s_waitcnt vmcnt(" #N ")" ::: "memory")
#define LGKM0() asm volatile("s_waitcnt lgkmcnt(0)" ::: "memory")
#define BARRIER() asm volatile("s_waitcnt lgkmcnt(0)\n\ts_barrier" ::: "memory")

__device__ __forceinline__ void dma_half(const char* gbase, unsigned lds_dst, unsigned v0, unsigned v1, unsigned v2, unsigned v3) {
  unsigned keep;
  asm volatile(
      "s_mov_b32 %[keep], m0\n\ts_mov_b32 m0, %[l0]\n\ts_nop 2\n\t"
      "global_load_lds_dwordx4 %[v0], %[sb]" KPOL "\n\t"
      "global_load_lds_dwordx4 %[v1], %[sb] offset:1024" KPOL "\n\t"
      "global_load_lds_dwordx4 %[v2], %[sb] offset:2048" KPOL "\n\t"
      "global_load_lds_dwordx4 %[v3], %[sb] offset:3072" KPOL "\n\t"
      "s_mov_b32 m0, %[keep]"
      : [keep] "=&s"(keep)
      : [v0] "v"(v0), [v1] "v"(v1), [v2] "v"(v2), [v3] "v"(v3), [sb] "s"(gbase), [l0] "s"(lds_dst)
      : "memory");
}
__device__ __forceinline__ void dma1w(const char* sb, unsigned lds_dst, unsigned voff) {
  unsigned keep;
  asm volatile("s_mov_b32 %[keep], m0\n\ts_mov_b32 m0, %[l0]\n\ts_nop 2\n\tglobal_load_lds_dwordx4 %[v0], %[sb]" WPOL "\n\ts_mov_b32 m0, %[keep]"
               : [keep] "=&s"(keep) : [v0] "v"(voff), [sb] "s"(sb), [l0] "s"(lds_dst) : "memory");
}
__device__ __forceinline__ void dma1(const char* sb, unsigned lds_dst, unsigned voff) {
  unsigned keep;
  asm volatile("s_mov_b32 %[keep], m0\n\ts_mov_b32 m0, %[l0]\n\ts_nop 2\n\tglobal_load_lds_dwordx4 %[v0], %[sb]" VPOL "\n\ts_mov_b32 m0, %[keep]"
               : [keep] "=&s"(keep) : [v0] "v"(voff), [sb] "s"(sb), [l0] "s"(lds_dst) : "memory");
}
__device__ __forceinline__ void ld128_nt(v4u& d, unsigned voff, const char* sb) {
  asm volatile("global_load_dwordx4 %0, %1, %2" KPOL : "=v"(d) : "v"(voff), "s"(sb) : "memory");
}
__device__ __forceinline__ void ld64(v2u& d, unsigned voff, const char* sb) {
  asm volatile("global_load_dwordx2 %0, %1, %2" WPOL : "=v"(d) : "v"(voff), "s"(sb) : "memory");
}
__device__ __forceinline__ void ld128_dev(v4u& d, unsigned voff, const char* sb) {
  asm volatile("global_load_dwordx4 %0, %1, %2 sc1" : "=v"(d) : "v"(voff), "s"(sb) : "memory");
}
__device__ __forceinline__ void ld32_dev(unsigned& d, unsigned voff, const char* sb) {
  asm volatile("global_load_dword %0, %1, %2 sc1" : "=v"(d) : "v"(voff), "s"(sb) : "memory");
}
__device__ __forceinline__ void st128_dev(unsigned voff, v4u v, char* sb) {
  asm volatile("global_store_dwordx4 %0, %1, %2 sc1\n\ts_nop 1" : : "v"(voff), "v"(v), "s"(sb) : "memory");
}
__device__ __forceinline__ unsigned tags_bad(v4u u, unsigned T) { return (u.y ^ T) | (u.w ^ T); }
struct PairTag { unsigned a, b; };
__device__ __forceinline__ PairTag pair_tag(unsigned T, unsigned pos) {
  const unsigned t = T >> (8u * (pos & 1u));
  return PairTag{t << 20, (t << 8) & 0xFFF00000u};
}
__device__ __forceinline__ v2u bfp_enc(f32x4 a, unsigned tag) {
  const unsigned m = max(max(max(__float_as_uint(a[0]) & 0x7FFFFFFFu, __float_as_uint(a[1]) & 0x7FFFFFFFu), __float_as_uint(a[2]) & 0x7FFFFFFFu),
                         __float_as_uint(a[3]) & 0x7FFFFFFFu);
  const unsigned e = max(m >> 23, 10u);
  const float sc = __uint_as_float((263u - e) << 23);
  unsigned q[4];
#pragma unroll
  for (int i = 0; i < 4; ++i) q[i] = __float_as_uint(__builtin_amdgcn_fmed3f(a[i] * sc, -1023.f, 1023.f) + 12582912.f);
  return v2u{(q[0] & 0x7FFu) | ((q[1] & 0x7FFu) << 11) | (q[2] << 22), ((q[2] >> 10) & 1u) | ((q[3] & 0x7FFu) << 1) | (e << 12) | tag};
}
__device__ __forceinline__ f32x4 bfp_dec(unsigned d0, unsigned d1) {
  const unsigned e = (d1 >> 12) & 0xFFu;
  const float sc = __uint_as_float(e == 255u ? 0x7F800000u : (e - 9u) << 23);
  const int q0 = __builtin_amdgcn_sbfe((int)d0, 0, 11), q1 = __builtin_amdgcn_sbfe((int)d0, 11, 11);
  const int q2 = __builtin_amdgcn_sbfe((int)__builtin_amdgcn_alignbit(d1, d0, 22), 0, 11), q3 = __builtin_amdgcn_sbfe((int)d1, 1, 11);
  return f32x4{(float)q0 * sc, (float)q1 * sc, (float)q2 * sc, (float)q3 * sc};
}
__device__ __forceinline__ v4u bfp_pair(f32x4 a, f32x4 b, PairTag t) {
  const v2u x = bfp_enc(a, t.a), y = bfp_enc(b, t.b);
  return v4u{x.x, x.y, y.x, y.y};
}
__device__ __forceinline__ unsigned pair_bad(v4u u, PairTag t) { return ((u.y ^ t.a) | (u.w ^ t.b)) & 0xFFF00000u; }
template <class P> __device__ __forceinline__ P* uni(P* p) {
  const unsigned long long a = (unsigned long long)p;
  const unsigned lo = __builtin_amdgcn_readfirstlane((unsigned)a), hi = __builtin_amdgcn_readfirstlane((unsigned)(a >> 32));
  return (P*)(((unsigned long long)hi << 32) | lo);
}
__device__ __forceinline__ bool any_bad(unsigned bad) { return __builtin_amdgcn_ballot_w64(bad != 0u) != 0ull; }
__device__ __forceinline__ void reload1(v4u& d0, unsigned o0, const char* sb) {
  asm volatile("global_load_dwordx4 %0, %1, %2 sc1\n\ts_waitcnt vmcnt(0)" : "=&v"(d0) : "v"(o0), "s"(sb) : "memory");
}
__device__ __forceinline__ void reload2(v4u& d0, v4u& d1, unsigned o0, unsigned o1, const char* sb) {
  asm volatile("global_load_dwordx4 %0, %2, %4 sc1\n\tglobal_load_dwordx4 %1, %3, %4 sc1\n\ts_waitcnt vmcnt(0)"
               : "=&v"(d0), "=&v"(d1) : "v"(o0), "v"(o1), "s"(sb) : "memory");
}

__device__ __forceinline__ void reload12(v4u& d0, v4u& d1, unsigned o0, unsigned o1, unsigned two, const char* sb) {
  asm volatile("global_load_dwordx4 %0, %2, %5 sc1\n\ts_cmp_eq_u32 %4, 0\n\ts_cbranch_scc1 .Lr12_%=\n\t"
               "global_load_dwordx4 %1, %3, %5 sc1\n.Lr12_%=:\n\ts_waitcnt vmcnt(0)"
               : "=&v"(d0), "+v"(d1) : "v"(o0), "v"(o1), "s"(two), "s"(sb) : "memory", "scc");
}

#define STAMP(i) do { if ((i) == PROBE) { unsigned long long t_; asm volatile("s_memrealtime %0\n\ts_waitcnt lgkmcnt(0)" : "=s"(t_) :: "memory"); tprobe = t_; } } while (0)

__global__ void __launch_bounds__(512, 2)
attn_fused_kernel(const char* __restrict__ x, const char* __restrict__ kc, const char* __restrict__ vc,
                  const char* __restrict__ wq, const char* __restrict__ wo, unsigned* __restrict__ out, char* __restrict__ ws) {
  extern __shared__ __attribute__((aligned(16))) unsigned char lds_raw[];
  LAS unsigned char* L = (LAS unsigned char*)lds_raw;
  const unsigned lbase = (unsigned)(size_t)L;
  const unsigned long long tbeg = __builtin_amdgcn_s_memrealtime();
  unsigned long long tprobe = tbeg;
  STAMP(0);
  const int tid = threadIdx.x, lane = tid & 63, w = __builtin_amdgcn_readfirstlane(tid >> 6);
  const int g = lane >> 4, c = lane & 15;
  const int bid = blockIdx.x, kvh = bid & 7, s = (bid >> 3) & 3, b = bid >> 5;
#define ROT (8 * s + b)
  const int rot = ROT;
  const int rb = b >> 1, cb = (b & 1) * 4 + s;
  asm volatile("" :: "s"(x), "s"(kc), "s"(vc), "s"(wq), "s"(wo), "s"(out), "s"(ws));
  if (tid == 0) *(LAS unsigned*)(L + L_FLAG) = 0u;
  __builtin_amdgcn_s_setprio(3);

  const size_t headrow = (size_t)(b * HKV + kvh) * SK + (SK - WIN) + s * 512;
  const char* kwin = kc + headrow * 512;
  const char* vwin = vc + headrow * 512;
  {
    const unsigned r8 = lane >> 3, f = ((r8 >> 1) & 1) | ((w & 1) << 1);
    const unsigned vo = r8 * (NQ * 2) + (((lane & 7) ^ (f << 1)) << 4);
    const char* wqb = wq + (size_t)(8 * w) * (NQ * 2) + (kvh * 512 + cb * 64) * 2;
#pragma unroll
    for (int i = 0; i < 7; ++i) dma1w(wqb + (size_t)(64 * i) * (NQ * 2), lbase + L_WQ + (8 * i + w) * 1024, vo);
    const char* xb = x + (size_t)(16 * rb) * (DM * 2);
#pragma unroll
    for (int i = 0; i < 2; ++i) {
      const int m = 8 * i + w;
      if (m < 14) {
        const unsigned p = 64 * m + lane, row = (p * 1171u) >> 16, ch = p - 56 * row;
        dma1w(xb, lbase + L_X + m * 1024, row * (DM * 2) + (((ch & ~7u) | ((ch & 7u) ^ ((row >> 1) & 7u))) << 4));
      }
    }
  }
  unsigned ep = 0;
  if (w == 7) ld32_dev(ep, 0u, ws + W_EP + rb * 256);
  unsigned pf0 = 0, pf1 = 0;
  if (w == 6 && bid < CODEPF) {
    unsigned long long pc;
    asm volatile("s_getpc_b64 %0" : "=s"(pc));
    asm volatile("global_load_dword %0, %2, %4\n\tglobal_load_dword %1, %3, %4" : "=&v"(pf0), "=&v"(pf1) : "v"(lane * 128u), "v"(lane * 128u + 5632u), "s"(pc) : "memory");
  }
  asm volatile("s_barrier" ::: "memory");
  unsigned ko[8];
  {
    const unsigned hi = lane >> 5, l31 = lane & 31;
    const unsigned laneK = (hi << 9) | ((l31 ^ hi) << 4);
#pragma unroll
    for (int q = 0; q < 8; ++q) ko[q] = (laneK ^ (unsigned)(q << 5)) + ((q >> 2) << 12);
  }
  auto tile_off = [&](int unit) -> size_t { return (size_t)((unit + rot) & 31) * 8192; };
  auto k_half = [&](int t, int h) {
    dma_half(kwin + tile_off(8 * t + w), lbase + L_RING + ((t & 1) * 8 + w) * 8192 + h * 4096, ko[4 * h], ko[4 * h + 1], ko[4 * h + 2], ko[4 * h + 3]);
  };
  k_half(0, 0);
  asm volatile("s_waitcnt vmcnt(4)" : "+v"(ep), "+v"(pf0), "+v"(pf1) :: "memory");
  if (w == 7 && lane == 0) *(LAS unsigned*)(L + L_FLAG + 4) = ep + 0x10000001u;
  STAMP(1);
  BARRIER();
  const unsigned T = __builtin_amdgcn_readfirstlane(*(LAS unsigned*)(L + L_FLAG + 4));
  char* qx = uni(ws + W_QX + (size_t)(kvh * 4 + rb) * (8 * 2048));
  {
    const int nt = w & 3, kh = w >> 2;
    const int q4 = (lane & 15) >> 2, p4 = lane & 3;
    const unsigned fa = (unsigned)(q4 >> 1) | ((unsigned)(g & 1) << 1);
    const unsigned ta = L_WQ + (224 * kh + 8 * g + q4) * 128 + (((2 * nt + (p4 >> 1)) ^ (fa << 1)) << 4) + (p4 & 1) * 8;
    f32x4 qa = {0.f, 0.f, 0.f, 0.f};
#pragma unroll
    for (int i = 0; i < 7; ++i) {
      const s16x4 a0 = __builtin_amdgcn_ds_read_tr16_b64_v4i16((LAS s16x4*)(L + ta + i * 4096));
      const s16x4 a1 = __builtin_amdgcn_ds_read_tr16_b64_v4i16((LAS s16x4*)(L + ta + i * 4096 + 512));
      const unsigned ch = 4 * (7 * kh + i) + g;
      const v4u xb = *(LAS v4u*)(L + L_X + c * 896 + (((ch & ~7u) | ((ch & 7u) ^ ((c >> 1) & 7u))) << 4));
      const v2u x0 = __builtin_bit_cast(v2u, a0), x1 = __builtin_bit_cast(v2u, a1);
      qa = mfma16(v4u{x0.x, x0.y, x1.x, x1.y}, xb, qa);
    }
    if (kh == 1) *(LAS f32x4*)(L + L_QR + nt * 1024 + lane * 16) = qa;
    STAMP(17);
    BARRIER();
    if (kh == 0) {
      qa += *(LAS f32x4*)(L + L_QR + nt * 1024 + lane * 16);
      f32x4 qb;
#pragma unroll
      for (int i = 0; i < 4; ++i) qb[i] = __int_as_float(__builtin_amdgcn_mov_dpp(__float_as_int(qa[i]), 0xB1, 0xF, 0xF, true));
      const v4u u = bfp_pair(qa, qb, pair_tag(T, c >> 1));
      if ((c & 1) == 0) st128_dev(cb * 2048 + nt * 512 + (c >> 3) * 256 + g * 64 + ((c >> 1) & 3) * 16, u, qx);
    }
  }
  STAMP(18);
#define QSHIM 1
  v4u kreg[8];
  const char* t2b = kwin + tile_off(16 + w);
  k_half(0, 1);
  k_half(1, 0);
  k_half(1, 1);
#pragma unroll
  for (int q = 0; q < QSHIM - 1; ++q) ld128_nt(kreg[q], q * 1024 + lane * 16, t2b);
  if (w < 4) {
    v4u qu[2];
    const unsigned qgo = w * 2048 + (lane >> 4) * 512 + (b & 1) * 256 + (lane & 15) * 16;
    const PairTag tgq = pair_tag(T, lane & 3);
    STAMP(19);
    reload2(qu[0], qu[1], qgo, qgo + 8192, qx);
    STAMP(3);
    for (unsigned spin = 0; any_bad(pair_bad(qu[0], tgq) | pair_bad(qu[1], tgq)) && spin < SPIN_CAP; ++spin) reload2(qu[0], qu[1], qgo, qgo + 8192, qx);
    const int nt = lane >> 4, gq = (lane >> 2) & 3;
    const unsigned qfa = L_QF + (w * 2 + (nt >> 1)) * 1024 + (16 * (2 * (nt & 1) + (gq >> 1)) + 2 * (lane & 3)) * 16 + (gq & 1) * 8;
#pragma unroll
    for (int u = 0; u < 2; ++u) {
      const f32x4 r0 = bfp_dec(qu[u].x, qu[u].y), r1 = bfp_dec(qu[u].z, qu[u].w);
      *(LAS v2u*)(L + qfa + u * 128) = v2u{pack_bf16(r0[0], r0[1]), pack_bf16(r0[2], r0[3])};
      *(LAS v2u*)(L + qfa + u * 128 + 16) = v2u{pack_bf16(r1[0], r1[1]), pack_bf16(r1[2], r1[3])};
    }
    LGKM0();
    if (lane == 0) __hip_atomic_fetch_add((LAS unsigned*)(L + L_FLAG), 1u, __ATOMIC_RELAXED, __HIP_MEMORY_SCOPE_WORKGROUP);
  }
  STAMP(4);
#pragma unroll
  for (int q = QSHIM - 1; q < 8; ++q) ld128_nt(kreg[q], q * 1024 + lane * 16, t2b);
  STAMP(2);
  for (unsigned spin = 0; *(volatile LAS unsigned*)(L + L_FLAG) < 4u && spin < (SPIN_CAP << 6); ++spin) __builtin_amdgcn_s_sleep(1);
  asm volatile("" ::: "memory");
  VMWAIT(16);
  v4u qf[8];
#pragma unroll
  for (int st = 0; st < 8; ++st) qf[st] = *(LAS v4u*)(L + L_QF + st * 1024 + lane * 16);

  f32x4 sc[4];
  auto qk_tile = [&](int slot) -> f32x4 {
    v4u kf[8];
#pragma unroll
    for (int st = 0; st < 8; ++st) kf[st] = *(LAS v4u*)(L + L_RING + slot * 8192 + c * 512 + (((4 * st + g) ^ c) << 4));
    f32x4 a = {0.f, 0.f, 0.f, 0.f};
#pragma unroll
    for (int st = 0; st < 8; ++st) a = mfma16(kf[st], qf[st], a);
    return a;
  };
  const unsigned vlane = (lane >> 3) * 512 + (((lane & 7) ^ (((lane >> 4) & 3) << 1)) << 4);
  auto v_piece = [&](int p, int q) {
    dma1(vwin + tile_off(4 * w + (q >> 1)) + (q & 1) * 4096 + ((s + 1 + p) & 3) * 128,
         lbase + L_RING + ((p & 1) * 8 + w) * 8192 + q * 1024, vlane);
  };
  const int wc = w < 7 ? w : 6;
  v2u wv[4][8];
  const char* wob = wo + (size_t)(2 * kvh * 256 + 64 * s) * (DM * 2) + wc * 128;
  const unsigned wvo = (unsigned)(8 * g) * (DM * 2) + c * 8;
  auto wo_load = [&](int ks, int i) { ld64(wv[ks][i], wvo + (unsigned)(256 * (ks >> 1) + 32 * (ks & 1) + i) * (DM * 2), wob); };

  auto wo_loads = [&](int n0, int n1) {
#pragma unroll
    for (int n = n0; n < n1; ++n) wo_load(n >> 3, n & 7);
  };
  auto v_half = [&](int p, int h) {
#pragma unroll
    for (int q = 4 * h; q < 4 * h + 4; ++q) v_piece(p, q);
  };
#define PRIOMODE 0
#define PRIO(n) __builtin_amdgcn_s_setprio(n)
  sc[0] = qk_tile(w);
  VMWAIT(8);
  sc[1] = qk_tile(8 + w);
  LGKM0();
  PRIO(2);
  wo_loads(0, 4); k_half(3, 0); wo_loads(4, 7);
  asm volatile("s_waitcnt vmcnt(11)"
               : "+v"(kreg[0]), "+v"(kreg[1]), "+v"(kreg[2]), "+v"(kreg[3]), "+v"(kreg[4]), "+v"(kreg[5]), "+v"(kreg[6]), "+v"(kreg[7]) :: "memory");
#pragma unroll
  for (int q = 0; q < 8; ++q) *(LAS v4u*)(L + L_RING + w * 8192 + ko[q] + (q & 3) * 1024) = kreg[q];
  sc[2] = qk_tile(w);
  LGKM0();
  PRIO(1);
  wo_loads(7, 10); k_half(3, 1); wo_loads(10, 13); v_half(0, 0);
  VMWAIT(7);
  sc[3] = qk_tile(8 + w);
  LGKM0();
  PRIO(0);
  wo_loads(13, 16); v_half(0, 1);
  STAMP(5);
  float mx = fmaxf(fmaxf(fmaxf(sc[0][0], sc[0][1]), fmaxf(sc[0][2], sc[0][3])), fmaxf(fmaxf(sc[1][0], sc[1][1]), fmaxf(sc[1][2], sc[1][3])));
  mx = fmaxf(mx, fmaxf(fmaxf(fmaxf(sc[2][0], sc[2][1]), fmaxf(sc[2][2], sc[2][3])), fmaxf(fmaxf(sc[3][0], sc[3][1]), fmaxf(sc[3][2], sc[3][3]))));
  mx = xg_max(mx);
  if (g == 0) *(LAS float*)(L + L_MX + (w * 16 + c) * 4) = mx;
  BARRIER();
  float mrow = *(LAS float*)(L + L_MX + c * 4);
#pragma unroll
  for (int ww = 1; ww < 8; ++ww) mrow = fmaxf(mrow, *(LAS float*)(L + L_MX + (ww * 16 + c) * 4));
  mrow *= C2;
  float lsum = 0.f;
#pragma unroll
  for (int t = 0; t < 4; ++t) {
    float p[4];
#pragma unroll
    for (int r = 0; r < 4; ++r) { p[r] = __builtin_amdgcn_exp2f(fmaf(sc[t][r], C2, -mrow)); lsum += p[r]; }
    *(LAS v2u*)(L + L_P + (4 * t + (w >> 1)) * 1024 + lane * 16 + (w & 1) * 8) = v2u{pack_bf16(p[0], p[1]), pack_bf16(p[2], p[3])};
  }
  lsum = xg_sum(lsum);
  if (g == 0) *(LAS float*)(L + L_LS + (w * 16 + c) * 4) = lsum;
  wo_loads(16, 20); v_half(1, 0);

  const int ct = w & 3, kh = w >> 2;
  unsigned tra;
  {
    const int q4 = (lane & 15) >> 2, p4 = lane & 3;
    tra = (4 * g + q4) * 128 + (((2 * ct + (p4 >> 1)) ^ (((2 * g + (q4 >> 1)) & 3) << 1)) << 4) + (p4 & 1) * 8 + kh * 32768;
  }
  auto pv_pass = [&](int p) -> f32x4 {
    f32x4 a = {0.f, 0.f, 0.f, 0.f};
#pragma unroll
    for (int i = 0; i < 8; ++i) {
      const v4u pf = *(LAS v4u*)(L + L_P + (8 * kh + i) * 1024 + lane * 16);
      const int ub = L_RING + (p & 1) * 65536 + i * 4096;
      const s16x4 a0 = __builtin_amdgcn_ds_read_tr16_b64_v4i16((LAS s16x4*)(L + ub + tra));
      const s16x4 a1 = __builtin_amdgcn_ds_read_tr16_b64_v4i16((LAS s16x4*)(L + ub + 2048 + tra));
      const v2u x0 = __builtin_bit_cast(v2u, a0), x1 = __builtin_bit_cast(v2u, a1);
      a = mfma16(v4u{x0.x, x0.y, x1.x, x1.y}, pf, a);
    }
    return a;
  };
  const int bh = b * HKV + kvh;
  char* stb = uni(ws + W_ST + (size_t)bh * 1024);
  char* h1 = uni(ws + W_H1 + (size_t)bh * (16 * 2048));
  const PairTag tg1 = pair_tag(T, lane);
  auto reduce_pass = [&](f32x4 a) -> v4u {
    *(LAS f32x4*)(L + L_HALF + (kh * 4 + ct) * 1024 + lane * 16) = a;
    BARRIER();
    v4u u = {0u, 0u, 0u, 0u};
    if (w < 2) {
      const LAS unsigned char* hp = L + L_HALF + 2 * w * 1024 + lane * 16;
      u = bfp_pair(*(const LAS f32x4*)hp + *(const LAS f32x4*)(hp + 4096), *(const LAS f32x4*)(hp + 1024) + *(const LAS f32x4*)(hp + 5120), tg1);
    }
    return u;
  };
  auto publish_pass = [&](int p, v4u u) {
    if (w < 2) st128_dev(((((s + 1 + p) & 3) * 4 + s) * 2 + w) * 1024 + lane * 16, u, h1);
  };
  STAMP(6);
  VMWAIT(8);
  BARRIER();
  STAMP(7);
  {
    const v4u a = reduce_pass(pv_pass(0));
    wo_loads(20, 23); v_half(1, 1);
    asm volatile("s_barrier" ::: "memory");
    wo_loads(23, 26); v_half(2, 0);
    publish_pass(0, a);
    if (w == 7) {
      float lt = *(LAS float*)(L + L_LS + c * 4);
#pragma unroll
      for (int ww = 1; ww < 8; ++ww) lt += *(LAS float*)(L + L_LS + (ww * 16 + c) * 4);
      if (g == 0) st128_dev((s * 16 + c) * 16, v4u{__float_as_uint(mrow), T, __float_as_uint(lt), T}, stb);
    }
    if (w < 2 || w == 7) VMWAIT(8); else VMWAIT(7);
  }
  BARRIER();
  v4u stu;
  {
    const v4u a = reduce_pass(pv_pass(1));
    wo_loads(26, 32); v_half(2, 1);
    asm volatile("s_barrier" ::: "memory");
    v_half(3, 0);
    publish_pass(1, a);
  }
  ld128_dev(stu, lane * 16, stb);
  if (w < 2) VMWAIT(6); else VMWAIT(5);
  asm volatile(""
               : "+v"(wv[0][0]), "+v"(wv[0][1]), "+v"(wv[0][2]), "+v"(wv[0][3]), "+v"(wv[0][4]), "+v"(wv[0][5]), "+v"(wv[0][6]), "+v"(wv[0][7]),
                 "+v"(wv[1][0]), "+v"(wv[1][1]), "+v"(wv[1][2]), "+v"(wv[1][3]), "+v"(wv[1][4]), "+v"(wv[1][5]), "+v"(wv[1][6]), "+v"(wv[1][7])
               :: "memory");
  asm volatile("" : "+v"(wv[2][0]), "+v"(wv[2][1]), "+v"(wv[2][2]), "+v"(wv[2][3]), "+v"(wv[2][4]), "+v"(wv[2][5]), "+v"(wv[2][6]), "+v"(wv[2][7]),
                    "+v"(wv[3][0]), "+v"(wv[3][1]), "+v"(wv[3][2]), "+v"(wv[3][3]), "+v"(wv[3][4]), "+v"(wv[3][5]), "+v"(wv[3][6]), "+v"(wv[3][7])
               :: "memory");
  BARRIER();
  publish_pass(2, reduce_pass(pv_pass(2)));
  v_half(3, 1);
  v4u hu[2];
  const int srcA = (s + (kh ? 3 : 1)) & 3, srcB = (s + (kh ? 3 : 2)) & 3;
  const unsigned hoA = ((s * 4 + srcA) * 2 + (ct >> 1)) * 1024 + lane * 16, hoB = ((s * 4 + srcB) * 2 + (ct >> 1)) * 1024 + lane * 16;
  v4u wf[4][4];
#pragma unroll
  for (int ks = 0; ks < 4; ++ks)
#pragma unroll
    for (int jj = 0; jj < 4; ++jj)
#pragma unroll
      for (int r = 0; r < 4; ++r) {
        const unsigned lo = wv[ks][2 * r][jj >> 1], hi = wv[ks][2 * r + 1][jj >> 1];
        wf[ks][jj][r] = (jj & 1) ? perm_hi(lo, hi) : perm_lo(lo, hi);
      }
  const int j32 = kvh * 4 + s;
  char* h2 = uni(ws + W_H2 + (size_t)b * (32 * 4 * 112 * 16));
  unsigned h2st = ((j32 * 4 + 2 * (g & 1) + (g >> 1)) * 112 + wc * 16 + c) * 16;
  const unsigned pos2 = 14 * (j32 & 7) + (c < 14 ? c : 13);
  unsigned h2ld = (((4 * w + g) * 4 + (j32 >> 3)) * 112 + pos2) * 16;
  PairTag tg2s = pair_tag(T, c), tg2l = pair_tag(T, pos2);
  unsigned obw = L_OB + c * 144 + (16 * ct + 4 * g) * 2, obr = L_OB + (c & 7) * 144 + g * 16, hlf = L_HALF + ct * 1024 + lane * 16;
  asm volatile("" : "+v"(wf[0][0]), "+v"(wf[0][1]), "+v"(wf[0][2]), "+v"(wf[0][3]), "+v"(wf[1][0]), "+v"(wf[1][1]), "+v"(wf[1][2]), "+v"(wf[1][3]),
                    "+v"(wf[2][0]), "+v"(wf[2][1]), "+v"(wf[2][2]), "+v"(wf[2][3]), "+v"(wf[3][0]), "+v"(wf[3][1]), "+v"(wf[3][2]), "+v"(wf[3][3]),
                    "+v"(h2st), "+v"(h2ld), "+v"(obw), "+v"(obr), "+v"(hlf), "+v"(tg2s.a), "+v"(tg2s.b), "+v"(tg2l.a), "+v"(tg2l.b));
  asm volatile("s_waitcnt vmcnt(4)" : "+v"(stu) :: "memory");
  for (unsigned spin = 0; any_bad(tags_bad(stu, T)) && spin < SPIN_CAP; ++spin) reload1(stu, lane * 16, stb);
  float fS, fA, fB;
  {
    const float ms = __uint_as_float(stu.x), ls = __uint_as_float(stu.z);
    const float M = xg_max(ms);
    const float e = __builtin_amdgcn_exp2f(ms - M);
    const float fw = e / xg_sum(e * ls);
    fS = __shfl(fw, 16 * s + c); fA = __shfl(fw, 16 * srcA + c); fB = kh ? 0.f : __shfl(fw, 16 * srcB + c);
  }
#define H1LATE 2
  asm volatile("s_waitcnt vmcnt(%0)" :: "n"(H1LATE) : "memory");
  const unsigned two = __builtin_amdgcn_readfirstlane(kh == 0);
  const PairTag tgB = two ? tg1 : PairTag{0u, 0u};
  hu[1] = v4u{0u, 0u, 0u, 0u};
  reload12(hu[0], hu[1], hoA, hoB, two, h1);
  STAMP(8);
  BARRIER();
  f32x4 own = pv_pass(3);
  STAMP(9);
  for (unsigned spin = 0; any_bad(pair_bad(hu[0], tg1) | pair_bad(hu[1], tgB)) && spin < SPIN_CAP; ++spin) reload12(hu[0], hu[1], hoA, hoB, two, h1);
  STAMP(11);
  {
    const unsigned odd = 0u - (unsigned)(ct & 1);
    own = own * fS + bfp_dec((hu[0].x & ~odd) | (hu[0].z & odd), (hu[0].y & ~odd) | (hu[0].w & odd)) * fA
                   + bfp_dec((hu[1].x & ~odd) | (hu[1].z & odd), (hu[1].y & ~odd) | (hu[1].w & odd)) * fB;
    if (kh == 1) *(LAS f32x4*)(L + hlf) = own;
    BARRIER();
    if (kh == 0) {
      own += *(LAS f32x4*)(L + hlf);
      *(LAS v2u*)(L + obw) = v2u{pack_bf16(own[0], own[1]), pack_bf16(own[2], own[3])};
    }
  }
  BARRIER();

  f32x4 pa[4];
#pragma unroll
  for (int jj = 0; jj < 4; ++jj) pa[jj] = f32x4{0.f, 0.f, 0.f, 0.f};
#pragma unroll
  for (int ks = 0; ks < 4; ++ks) {
    const v4u af = *(LAS v4u*)(L + obr + 8 * (ks >> 1) * 144 + 64 * (ks & 1));
#pragma unroll
    for (int jj = 0; jj < 4; ++jj) pa[jj] = mfma16(af, wf[ks][jj], pa[jj]);
  }
  if (w < 7) {
    const f32x4 v0 = (g >> 1) ? f32x4{pa[0][2], pa[1][2], pa[2][2], pa[3][2]} : f32x4{pa[0][0], pa[1][0], pa[2][0], pa[3][0]};
    const f32x4 v1 = (g >> 1) ? f32x4{pa[0][3], pa[1][3], pa[2][3], pa[3][3]} : f32x4{pa[0][1], pa[1][1], pa[2][1], pa[3][1]};
    st128_dev(h2st, bfp_pair(v0, v1, tg2s), h2);
  }
  STAMP(12);
  {
    v4u fu;
    ld128_dev(fu, h2ld, h2);
    asm volatile("s_waitcnt vmcnt(0)" : "+v"(fu) :: "memory");
    for (unsigned spin = 0; any_bad(pair_bad(fu, tg2l)) && spin < SPIN_CAP; ++spin) reload1(fu, h2ld, h2);
    STAMP(13);
    const f32x4 r0 = bfp_dec(fu.x, fu.y), r1 = bfp_dec(fu.z, fu.w);
    f32x4 t;
#pragma unroll
    for (int e = 0; e < 4; ++e) {
      const auto u = __builtin_amdgcn_permlane32_swap(__float_as_uint(r0[e]), __float_as_uint(r1[e]), false, false);
      const float h = __uint_as_float(u[0]) + __uint_as_float(u[1]);
      const auto v = __builtin_amdgcn_permlane16_swap(__float_as_uint(h), __float_as_uint(h), false, false);
      t[e] = __uint_as_float(v[0]) + __uint_as_float(v[1]);
    }
    *(LAS f32x4*)(L + L_RED + w * 1024 + lane * 16) = t;
  }
  BARRIER();
  if (w == 0) {
    f32x4 t = *(LAS f32x4*)(L + L_RED + lane * 16);
#pragma unroll
    for (int ww = 1; ww < 8; ++ww) t += *(LAS f32x4*)(L + L_RED + ww * 1024 + lane * 16);
    if ((lane & 31) < 14)
      *(v2u*)(out + (((b * SQ + 2 * (j32 >> 3) + (lane >> 5)) * DM + 56 * (j32 & 7) + 4 * (lane & 31)) >> 1)) =
          v2u{pack_bf16(t[0], t[1]), pack_bf16(t[2], t[3])};
  }
  if (w == 1 && (b & 1) == 0 && j32 == 0 && lane == 0)
    __hip_atomic_store((unsigned*)(ws + W_EP + rb * 256), T, __ATOMIC_RELAXED, __HIP_MEMORY_SCOPE_AGENT);
  asm volatile("s_waitcnt vmcnt(0)" ::: "memory");
  STAMP(14);
  {
    const unsigned long long until = tbeg + 6000ull + 4ull * (tprobe - tbeg);
    while (__builtin_amdgcn_s_memrealtime() < until) __builtin_amdgcn_s_sleep(4);
  }
}

}

extern "C" void kernel_launch(void* const* d_in, const int* in_sizes, int n_in, void* d_out, int out_size, void* d_ws,
                              size_t ws_size, hipStream_t stream) {
  (void)in_sizes; (void)n_in; (void)out_size;
  if (ws_size < W_END) return;
  (void)hipFuncSetAttribute((const void*)attn_fused_kernel, hipFuncAttributeMaxDynamicSharedMemorySize, LDS_BYTES);
  attn_fused_kernel<<<dim3(256), dim3(512), LDS_BYTES, stream>>>((const char*)d_in[0], (const char*)d_in[1], (const char*)d_in[2],
                                                                 (const char*)d_in[3], (const char*)d_in[4], (unsigned*)d_out, (char*)d_ws);
}
```

```cpp
#define CODEPF 8
#define PTOUCH 5
#define PROBE 14
#include <hip/hip_runtime.h>
#include <hip/hip_bf16.h>

namespace {

typedef unsigned v4u __attribute__((ext_vector_type(4)));
typedef unsigned v2u __attribute__((ext_vector_type(2)));
typedef float f32x4 __attribute__((ext_vector_type(4)));
typedef float f32x2 __attribute__((ext_vector_type(2)));
typedef __bf16 bf16x8 __attribute__((ext_vector_type(8)));
typedef __bf16 bf16x2 __attribute__((ext_vector_type(2)));
typedef short s16x4 __attribute__((ext_vector_type(4)));
#define LAS __attribute__((address_space(3)))

constexpr int NB = 8, SQ = 8, DM = 448, HKV = 8, SK = 8192, WIN = 2048;
constexpr int NQ = 4096;
constexpr float C2 = 0.0625f * 1.4426950408889634f;
constexpr unsigned SPIN_CAP = 1u << 18;

constexpr int L_RING = 0;
constexpr int L_WQ = L_RING + 65536;
constexpr int L_P = 131072;
constexpr int L_X = L_P;
constexpr int L_QF = L_P + 16384;
constexpr int L_HALF = L_QF;
constexpr int L_MX = L_QF + 8192;
constexpr int L_LS = L_MX + 512;
constexpr int L_QR = L_LS + 512;
constexpr int L_FLAG = L_QR + 4096;
constexpr int L_OB = L_FLAG + 16;
constexpr int L_RED = L_P;
constexpr int LDS_BYTES = 163840;
static_assert(L_OB + 16 * 144 <= LDS_BYTES, "LDS map");

constexpr size_t W_EP = 0;
constexpr size_t W_QX = 4096;
constexpr size_t W_ST = W_QX + (size_t)8 * 4 * 8 * 2048;
constexpr size_t W_H1 = W_ST + (size_t)64 * 4 * 256;
constexpr size_t W_H2 = W_H1 + (size_t)64 * 16 * 2048;
constexpr size_t W_END = W_H2 + (size_t)8 * 32 * 4 * 112 * 16;

__device__ __forceinline__ f32x4 mfma16(v4u a, v4u b, f32x4 c) {
  return __builtin_amdgcn_mfma_f32_16x16x32_bf16(__builtin_bit_cast(bf16x8, a), __builtin_bit_cast(bf16x8, b), c, 0, 0, 0);
}
__device__ __forceinline__ unsigned pack_bf16(float lo, float hi) {
  bf16x2 r; r[0] = (__bf16)lo; r[1] = (__bf16)hi;
  return __builtin_bit_cast(unsigned, r);
}
__device__ __forceinline__ float xg_max(float x) {
  auto s = __builtin_amdgcn_permlane16_swap(__float_as_uint(x), __float_as_uint(x), false, false);
  x = fmaxf(__uint_as_float(s[0]), __uint_as_float(s[1]));
  auto t = __builtin_amdgcn_permlane32_swap(__float_as_uint(x), __float_as_uint(x), false, false);
  return fmaxf(__uint_as_float(t[0]), __uint_as_float(t[1]));
}
__device__ __forceinline__ float xg_sum(float x) {
  auto s = __builtin_amdgcn_permlane16_swap(__float_as_uint(x), __float_as_uint(x), false, false);
  x = __uint_as_float(s[0]) + __uint_as_float(s[1]);
  auto t = __builtin_amdgcn_permlane32_swap(__float_as_uint(x), __float_as_uint(x), false, false);
  return __uint_as_float(t[0]) + __uint_as_float(t[1]);
}
__device__ __forceinline__ unsigned perm_lo(unsigned a, unsigned b) { return __builtin_amdgcn_perm(b, a, 0x05040100u); }
__device__ __forceinline__ unsigned perm_hi(unsigned a, unsigned b) { return __builtin_amdgcn_perm(b, a, 0x07060302u); }

#define KPOL " nt"
#define VPOL " nt"
#define WPOL ""
#define VMWAIT(N) asm volatile("s_waitcnt vmcnt(" #N ")" ::: "memory")
#define LGKM0() asm volatile("s_waitcnt lgkmcnt(0)" ::: "memory")
#define BARRIER() asm volatile("s_waitcnt lgkmcnt(0)\n\ts_barrier" ::: "memory")

__device__ __forceinline__ void dma_half(const char* gbase, unsigned lds_dst, unsigned v0, unsigned v1, unsigned v2, unsigned v3) {
  unsigned keep;
  asm volatile(
      "s_mov_b32 %[keep], m0\n\ts_mov_b32 m0, %[l0]\n\ts_nop 2\n\t"
      "global_load_lds_dwordx4 %[v0], %[sb]" KPOL "\n\t"
      "global_load_lds_dwordx4 %[v1], %[sb] offset:1024" KPOL "\n\t"
      "global_load_lds_dwordx4 %[v2], %[sb] offset:2048" KPOL "\n\t"
      "global_load_lds_dwordx4 %[v3], %[sb] offset:3072" KPOL "\n\t"
      "s_mov_b32 m0, %[keep]"
      : [keep] "=&s"(keep)
      : [v0] "v"(v0), [v1] "v"(v1), [v2] "v"(v2), [v3] "v"(v3), [sb] "s"(gbase), [l0] "s"(lds_dst)
      : "memory");
}
__device__ __forceinline__ void dma1w(const char* sb, unsigned lds_dst, unsigned voff) {
  unsigned keep;
  asm volatile("s_mov_b32 %[keep], m0\n\ts_mov_b32 m0, %[l0]\n\ts_nop 2\n\tglobal_load_lds_dwordx4 %[v0], %[sb]" WPOL "\n\ts_mov_b32 m0, %[keep]"
               : [keep] "=&s"(keep) : [v0] "v"(voff), [sb] "s"(sb), [l0] "s"(lds_dst) : "memory");
}
__device__ __forceinline__ void dma1(const char* sb, unsigned lds_dst, unsigned voff) {
  unsigned keep;
  asm volatile("s_mov_b32 %[keep], m0\n\ts_mov_b32 m0, %[l0]\n\ts_nop 2\n\tglobal_load_lds_dwordx4 %[v0], %[sb]" VPOL "\n\ts_mov_b32 m0, %[keep]"
               : [keep] "=&s"(keep) : [v0] "v"(voff), [sb] "s"(sb), [l0] "s"(lds_dst) : "memory");
}
__device__ __forceinline__ void ld128_nt(v4u& d, unsigned voff, const char* sb) {
  asm volatile("global_load_dwordx4 %0, %1, %2" KPOL : "=v"(d) : "v"(voff), "s"(sb) : "memory");
}
__device__ __forceinline__ void ld64(v2u& d, unsigned voff, const char* sb) {
  asm volatile("global_load_dwordx2 %0, %1, %2" WPOL : "=v"(d) : "v"(voff), "s"(sb) : "memory");
}
__device__ __forceinline__ void ld128_dev(v4u& d, unsigned voff, const char* sb) {
  asm volatile("global_load_dwordx4 %0, %1, %2 sc1" : "=v"(d) : "v"(voff), "s"(sb) : "memory");
}
__device__ __forceinline__ void ld32_dev(unsigned& d, unsigned voff, const char* sb) {
  asm volatile("global_load_dword %0, %1, %2 sc1" : "=v"(d) : "v"(voff), "s"(sb) : "memory");
}
__device__ __forceinline__ void st128_dev(unsigned voff, v4u v, char* sb) {
  asm volatile("global_store_dwordx4 %0, %1, %2 sc1\n\ts_nop 1" : : "v"(voff), "v"(v), "s"(sb) : "memory");
}
__device__ __forceinline__ unsigned tags_bad(v4u u, unsigned T) { return (u.y ^ T) | (u.w ^ T); }
struct PairTag { unsigned a, b; };
__device__ __forceinline__ PairTag pair_tag(unsigned T, unsigned pos) {
  const unsigned t = T >> (8u * (pos & 1u));
  return PairTag{t << 20, (t << 8) & 0xFFF00000u};
}
__device__ __forceinline__ v2u bfp_enc(f32x4 a, unsigned tag) {
  const unsigned m = max(max(max(__float_as_uint(a[0]) & 0x7FFFFFFFu, __float_as_uint(a[1]) & 0x7FFFFFFFu), __float_as_uint(a[2]) & 0x7FFFFFFFu),
                         __float_as_uint(a[3]) & 0x7FFFFFFFu);
  const unsigned e = max(m >> 23, 10u);
  const float sc = __uint_as_float((263u - e) << 23);
  unsigned q[4];
#pragma unroll
  for (int i = 0; i < 4; ++i) q[i] = __float_as_uint(__builtin_amdgcn_fmed3f(a[i] * sc, -1023.f, 1023.f) + 12582912.f);
  return v2u{(q[0] & 0x7FFu) | ((q[1] & 0x7FFu) << 11) | (q[2] << 22), ((q[2] >> 10) & 1u) | ((q[3] & 0x7FFu) << 1) | (e << 12) | tag};
}
__device__ __forceinline__ f32x4 bfp_dec(unsigned d0, unsigned d1) {
  const unsigned e = (d1 >> 12) & 0xFFu;
  const float sc = __uint_as_float(e == 255u ? 0x7F800000u : (e - 9u) << 23);
  const int q0 = __builtin_amdgcn_sbfe((int)d0, 0, 11), q1 = __builtin_amdgcn_sbfe((int)d0, 11, 11);
  const int q2 = __builtin_amdgcn_sbfe((int)__builtin_amdgcn_alignbit(d1, d0, 22), 0, 11), q3 = __builtin_amdgcn_sbfe((int)d1, 1, 11);
  return f32x4{(float)q0 * sc, (float)q1 * sc, (float)q2 * sc, (float)q3 * sc};
}
__device__ __forceinline__ v4u bfp_pair(f32x4 a, f32x4 b, PairTag t) {
  const v2u x = bfp_enc(a, t.a), y = bfp_enc(b, t.b);
  return v4u{x.x, x.y, y.x, y.y};
}
__device__ __forceinline__ unsigned pair_bad(v4u u, PairTag t) { return ((u.y ^ t.a) | (u.w ^ t.b)) & 0xFFF00000u; }
template <class P> __device__ __forceinline__ P* uni(P* p) {
  const unsigned long long a = (unsigned long long)p;
  const unsigned lo = __builtin_amdgcn_readfirstlane((unsigned)a), hi = __builtin_amdgcn_readfirstlane((unsigned)(a >> 32));
  return (P*)(((unsigned long long)hi << 32) | lo);
}
__device__ __forceinline__ bool any_bad(unsigned bad) { return __builtin_amdgcn_ballot_w64(bad != 0u) != 0ull; }
__device__ __forceinline__ void reload1(v4u& d0, unsigned o0, const char* sb) {
  asm volatile("global_load_dwordx4 %0, %1, %2 sc1\n\ts_waitcnt vmcnt(0)" : "=&v"(d0) : "v"(o0), "s"(sb) : "memory");
}
__device__ __forceinline__ void reload2(v4u& d0, v4u& d1, unsigned o0, unsigned o1, const char* sb) {
  asm volatile("global_load_dwordx4 %0, %2, %4 sc1\n\tglobal_load_dwordx4 %1, %3, %4 sc1\n\ts_waitcnt vmcnt(0)"
               : "=&v"(d0), "=&v"(d1) : "v"(o0), "v"(o1), "s"(sb) : "memory");
}

__device__ __forceinline__ void reload12(v4u& d0, v4u& d1, unsigned o0, unsigned o1, unsigned two, const char* sb) {
  asm volatile("global_load_dwordx4 %0, %2, %5 sc1\n\ts_cmp_eq_u32 %4, 0\n\ts_cbranch_scc1 .Lr12_%=\n\t"
               "global_load_dwordx4 %1, %3, %5 sc1\n.Lr12_%=:\n\ts_waitcnt vmcnt(0)"
               : "=&v"(d0), "+v"(d1) : "v"(o0), "v"(o1), "s"(two), "s"(sb) : "memory", "scc");
}

#define STAMP(i) do { if ((i) == PROBE) { unsigned long long t_; asm volatile("s_memrealtime %0\n\ts_waitcnt lgkmcnt(0)" : "=s"(t_) :: "memory"); tprobe = t_; } } while (0)

__global__ void __launch_bounds__(512, 2)
attn_fused_kernel(const char* __restrict__ x, const char* __restrict__ kc, const char* __restrict__ vc,
                  const char* __restrict__ wq, const char* __restrict__ wo, unsigned* __restrict__ out, char* __restrict__ ws) {
  extern __shared__ __attribute__((aligned(16))) unsigned char lds_raw[];
  LAS unsigned char* L = (LAS unsigned char*)lds_raw;
  const unsigned lbase = (unsigned)(size_t)L;
  const unsigned long long tbeg = __builtin_amdgcn_s_memrealtime();
  unsigned long long tprobe = tbeg;
  STAMP(0);
  const int tid = threadIdx.x, lane = tid & 63, w = __builtin_amdgcn_readfirstlane(tid >> 6);
  const int g = lane >> 4, c = lane & 15;
  const int bid = blockIdx.x, kvh = bid & 7, s = (bid >> 3) & 3, b = bid >> 5;
#define ROT (8 * s + b)
  const int rot = ROT;
  const int rb = b >> 1, cb = (b & 1) * 4 + s;
  asm volatile("" :: "s"(x), "s"(kc), "s"(vc), "s"(wq), "s"(wo), "s"(out), "s"(ws));
  if (tid == 0) *(LAS unsigned*)(L + L_FLAG) = 0u;
  __builtin_amdgcn_s_setprio(3);

  const size_t headrow = (size_t)(b * HKV + kvh) * SK + (SK - WIN) + s * 512;
  const char* kwin = kc + headrow * 512;
  const char* vwin = vc + headrow * 512;
  unsigned ptouch = 0;
  {
    const char* tp = w == 7 ? kwin : w == 6 ? vwin : wo + (size_t)(2 * kvh * 256 + 64 * s) * (DM * 2);
    tp = uni(tp);
    if (w >= PTOUCH) asm volatile("global_load_dword %0, %1, %2 nt" : "+v"(ptouch) : "v"(0u), "s"(tp) : "memory");
  }
  {
    const unsigned r8 = lane >> 3, f = ((r8 >> 1) & 1) | ((w & 1) << 1);
    const unsigned vo = r8 * (NQ * 2) + (((lane & 7) ^ (f << 1)) << 4);
    const char* wqb = wq + (size_t)(8 * w) * (NQ * 2) + (kvh * 512 + cb * 64) * 2;
#pragma unroll
    for (int i = 0; i < 7; ++i) dma1w(wqb + (size_t)(64 * i) * (NQ * 2), lbase + L_WQ + (8 * i + w) * 1024, vo);
    const char* xb = x + (size_t)(16 * rb) * (DM * 2);
#pragma unroll
    for (int i = 0; i < 2; ++i) {
      const int m = 8 * i + w;
      if (m < 14) {
        const unsigned p = 64 * m + lane, row = (p * 1171u) >> 16, ch = p - 56 * row;
        dma1w(xb, lbase + L_X + m * 1024, row * (DM * 2) + (((ch & ~7u) | ((ch & 7u) ^ ((row >> 1) & 7u))) << 4));
      }
    }
  }
  unsigned ep = 0;
  if (w == 7) ld32_dev(ep, 0u, ws + W_EP + rb * 256);
  unsigned pf0 = 0, pf1 = 0;
  if (w == 6 && bid < CODEPF) {
    unsigned long long pc;
    asm volatile("s_getpc_b64 %0" : "=s"(pc));
    asm volatile("global_load_dword %0, %2, %4\n\tglobal_load_dword %1, %3, %4" : "=&v"(pf0), "=&v"(pf1) : "v"(lane * 128u), "v"(lane * 128u + 5632u), "s"(pc) : "memory");
  }
  asm volatile("s_barrier" ::: "memory");
  unsigned ko[8];
  {
    const unsigned hi = lane >> 5, l31 = lane & 31;
    const unsigned laneK = (hi << 9) | ((l31 ^ hi) << 4);
#pragma unroll
    for (int q = 0; q < 8; ++q) ko[q] = (laneK ^ (unsigned)(q << 5)) + ((q >> 2) << 12);
  }
  auto tile_off = [&](int unit) -> size_t { return (size_t)((unit + rot) & 31) * 8192; };
  auto k_half = [&](int t, int h) {
    dma_half(kwin + tile_off(8 * t + w), lbase + L_RING + ((t & 1) * 8 + w) * 8192 + h * 4096, ko[4 * h], ko[4 * h + 1], ko[4 * h + 2], ko[4 * h + 3]);
  };
  k_half(0, 0);
  asm volatile("s_waitcnt vmcnt(4)" : "+v"(ep), "+v"(pf0), "+v"(pf1) :: "memory");
  asm volatile("" : "+v"(ptouch));
  if (w == 7 && lane == 0) *(LAS unsigned*)(L + L_FLAG + 4) = ep + 0x10000001u;
  STAMP(1);
  BARRIER();
  const unsigned T = __builtin_amdgcn_readfirstlane(*(LAS unsigned*)(L + L_FLAG + 4));
  char* qx = uni(ws + W_QX + (size_t)(kvh * 4 + rb) * (8 * 2048));
  {
    const int nt = w & 3, kh = w >> 2;
    const int q4 = (lane & 15) >> 2, p4 = lane & 3;
    const unsigned fa = (unsigned)(q4 >> 1) | ((unsigned)(g & 1) << 1);
    const unsigned ta = L_WQ + (224 * kh + 8 * g + q4) * 128 + (((2 * nt + (p4 >> 1)) ^ (fa << 1)) << 4) + (p4 & 1) * 8;
    f32x4 qa = {0.f, 0.f, 0.f, 0.f};
#pragma unroll
    for (int i = 0; i < 7; ++i) {
      const s16x4 a0 = __builtin_amdgcn_ds_read_tr16_b64_v4i16((LAS s16x4*)(L + ta + i * 4096));
      const s16x4 a1 = __builtin_amdgcn_ds_read_tr16_b64_v4i16((LAS s16x4*)(L + ta + i * 4096 + 512));
      const unsigned ch = 4 * (7 * kh + i) + g;
      const v4u xb = *(LAS v4u*)(L + L_X + c * 896 + (((ch & ~7u) | ((ch & 7u) ^ ((c >> 1) & 7u))) << 4));
      const v2u x0 = __builtin_bit_cast(v2u, a0), x1 = __builtin_bit_cast(v2u, a1);
      qa = mfma16(v4u{x0.x, x0.y, x1.x, x1.y}, xb, qa);
    }
    if (kh == 1) *(LAS f32x4*)(L + L_QR + nt * 1024 + lane * 16) = qa;
    STAMP(17);
    BARRIER();
    if (kh == 0) {
      qa += *(LAS f32x4*)(L + L_QR + nt * 1024 + lane * 16);
      f32x4 qb;
#pragma unroll
      for (int i = 0; i < 4; ++i) qb[i] = __int_as_float(__builtin_amdgcn_mov_dpp(__float_as_int(qa[i]), 0xB1, 0xF, 0xF, true));
      const v4u u = bfp_pair(qa, qb, pair_tag(T, c >> 1));
      if ((c & 1) == 0) st128_dev(cb * 2048 + nt * 512 + (c >> 3) * 256 + g * 64 + ((c >> 1) & 3) * 16, u, qx);
    }
  }
  STAMP(18);
#define QSHIM 1
  v4u kreg[8];
  const char* t2b = kwin + tile_off(16 + w);
  k_half(0, 1);
  k_half(1, 0);
  k_half(1, 1);
#pragma unroll
  for (int q = 0; q < QSHIM - 1; ++q) ld128_nt(kreg[q], q * 1024 + lane * 16, t2b);
  if (w < 4) {
    v4u qu[2];
    const unsigned qgo = w * 2048 + (lane >> 4) * 512 + (b & 1) * 256 + (lane & 15) * 16;
    const PairTag tgq = pair_tag(T, lane & 3);
    STAMP(19);
    reload2(qu[0], qu[1], qgo, qgo + 8192, qx);
    STAMP(3);
    for (unsigned spin = 0; any_bad(pair_bad(qu[0], tgq) | pair_bad(qu[1], tgq)) && spin < SPIN_CAP; ++spin) reload2(qu[0], qu[1], qgo, qgo + 8192, qx);
    const int nt = lane >> 4, gq = (lane >> 2) & 3;
    const unsigned qfa = L_QF + (w * 2 + (nt >> 1)) * 1024 + (16 * (2 * (nt & 1) + (gq >> 1)) + 2 * (lane & 3)) * 16 + (gq & 1) * 8;
#pragma unroll
    for (int u = 0; u < 2; ++u) {
      const f32x4 r0 = bfp_dec(qu[u].x, qu[u].y), r1 = bfp_dec(qu[u].z, qu[u].w);
      *(LAS v2u*)(L + qfa + u * 128) = v2u{pack_bf16(r0[0], r0[1]), pack_bf16(r0[2], r0[3])};
      *(LAS v2u*)(L + qfa + u * 128 + 16) = v2u{pack_bf16(r1[0], r1[1]), pack_bf16(r1[2], r1[3])};
    }
    LGKM0();
    if (lane == 0) __hip_atomic_fetch_add((LAS unsigned*)(L + L_FLAG), 1u, __ATOMIC_RELAXED, __HIP_MEMORY_SCOPE_WORKGROUP);
  }
  STAMP(4);
#pragma unroll
  for (int q = QSHIM - 1; q < 8; ++q) ld128_nt(kreg[q], q * 1024 + lane * 16, t2b);
  STAMP(2);
  for (unsigned spin = 0; *(volatile LAS unsigned*)(L + L_FLAG) < 4u && spin < (SPIN_CAP << 6); ++spin) __builtin_amdgcn_s_sleep(1);
  asm volatile("" ::: "memory");
  VMWAIT(16);
  v4u qf[8];
#pragma unroll
  for (int st = 0; st < 8; ++st) qf[st] = *(LAS v4u*)(L + L_QF + st * 1024 + lane * 16);

  f32x4 sc[4];
  auto qk_tile = [&](int slot) -> f32x4 {
    v4u kf[8];
#pragma unroll
    for (int st = 0; st < 8; ++st) kf[st] = *(LAS v4u*)(L + L_RING + slot * 8192 + c * 512 + (((4 * st + g) ^ c) << 4));
    f32x4 a = {0.f, 0.f, 0.f, 0.f};
#pragma unroll
    for (int st = 0; st < 8; ++st) a = mfma16(kf[st], qf[st], a);
    return a;
  };
  const unsigned vlane = (lane >> 3) * 512 + (((lane & 7) ^ (((lane >> 4) & 3) << 1)) << 4);
  auto v_piece = [&](int p, int q) {
    dma1(vwin + tile_off(4 * w + (q >> 1)) + (q & 1) * 4096 + ((s + 1 + p) & 3) * 128,
         lbase + L_RING + ((p & 1) * 8 + w) * 8192 + q * 1024, vlane);
  };
  const int wc = w < 7 ? w : 6;
  v2u wv[4][8];
  const char* wob = wo + (size_t)(2 * kvh * 256 + 64 * s) * (DM * 2) + wc * 128;
  const unsigned wvo = (unsigned)(8 * g) * (DM * 2) + c * 8;
  auto wo_load = [&](int ks, int i) { ld64(wv[ks][i], wvo + (unsigned)(256 * (ks >> 1) + 32 * (ks & 1) + i) * (DM * 2), wob); };

  auto wo_loads = [&](int n0, int n1) {
#pragma unroll
    for (int n = n0; n < n1; ++n) wo_load(n >> 3, n & 7);
  };
  auto v_half = [&](int p, int h) {
#pragma unroll
    for (int q = 4 * h; q < 4 * h + 4; ++q) v_piece(p, q);
  };
#define PRIOMODE 0
#define PRIO(n) __builtin_amdgcn_s_setprio(n)
  sc[0] = qk_tile(w);
  VMWAIT(8);
  sc[1] = qk_tile(8 + w);
  LGKM0();
  PRIO(2);
  wo_loads(0, 4); k_half(3, 0); wo_loads(4, 7);
  asm volatile("s_waitcnt vmcnt(11)"
               : "+v"(kreg[0]), "+v"(kreg[1]), "+v"(kreg[2]), "+v"(kreg[3]), "+v"(kreg[4]), "+v"(kreg[5]), "+v"(kreg[6]), "+v"(kreg[7]) :: "memory");
#pragma unroll
  for (int q = 0; q < 8; ++q) *(LAS v4u*)(L + L_RING + w * 8192 + ko[q] + (q & 3) * 1024) = kreg[q];
  sc[2] = qk_tile(w);
  LGKM0();
  PRIO(1);
  wo_loads(7, 10); k_half(3, 1); wo_loads(10, 13); v_half(0, 0);
  VMWAIT(7);
  sc[3] = qk_tile(8 + w);
  LGKM0();
  PRIO(0);
  wo_loads(13, 16); v_half(0, 1);
  STAMP(5);
  float mx = fmaxf(fmaxf(fmaxf(sc[0][0], sc[0][1]), fmaxf(sc[0][2], sc[0][3])), fmaxf(fmaxf(sc[1][0], sc[1][1]), fmaxf(sc[1][2], sc[1][3])));
  mx = fmaxf(mx, fmaxf(fmaxf(fmaxf(sc[2][0], sc[2][1]), fmaxf(sc[2][2], sc[2][3])), fmaxf(fmaxf(sc[3][0], sc[3][1]), fmaxf(sc[3][2], sc[3][3]))));
  mx = xg_max(mx);
  if (g == 0) *(LAS float*)(L + L_MX + (w * 16 + c) * 4) = mx;
  BARRIER();
  float mrow = *(LAS float*)(L + L_MX + c * 4);
#pragma unroll
  for (int ww = 1; ww < 8; ++ww) mrow = fmaxf(mrow, *(LAS float*)(L + L_MX + (ww * 16 + c) * 4));
  mrow *= C2;
  float lsum = 0.f;
#pragma unroll
  for (int t = 0; t < 4; ++t) {
    float p[4];
#pragma unroll
    for (int r = 0; r < 4; ++r) { p[r] = __builtin_amdgcn_exp2f(fmaf(sc[t][r], C2, -mrow)); lsum += p[r]; }
    *(LAS v2u*)(L + L_P + (4 * t + (w >> 1)) * 1024 + lane * 16 + (w & 1) * 8) = v2u{pack_bf16(p[0], p[1]), pack_bf16(p[2], p[3])};
  }
  lsum = xg_sum(lsum);
  if (g == 0) *(LAS float*)(L + L_LS + (w * 16 + c) * 4) = lsum;
  wo_loads(16, 20); v_half(1, 0);

  const int ct = w & 3, kh = w >> 2;
  unsigned tra;
  {
    const int q4 = (lane & 15) >> 2, p4 = lane & 3;
    tra = (4 * g + q4) * 128 + (((2 * ct + (p4 >> 1)) ^ (((2 * g + (q4 >> 1)) & 3) << 1)) << 4) + (p4 & 1) * 8 + kh * 32768;
  }
  auto pv_pass = [&](int p) -> f32x4 {
    f32x4 a = {0.f, 0.f, 0.f, 0.f};
#pragma unroll
    for (int i = 0; i < 8; ++i) {
      const v4u pf = *(LAS v4u*)(L + L_P + (8 * kh + i) * 1024 + lane * 16);
      const int ub = L_RING + (p & 1) * 65536 + i * 4096;
      const s16x4 a0 = __builtin_amdgcn_ds_read_tr16_b64_v4i16((LAS s16x4*)(L + ub + tra));
      const s16x4 a1 = __builtin_amdgcn_ds_read_tr16_b64_v4i16((LAS s16x4*)(L + ub + 2048 + tra));
      const v2u x0 = __builtin_bit_cast(v2u, a0), x1 = __builtin_bit_cast(v2u, a1);
      a = mfma16(v4u{x0.x, x0.y, x1.x, x1.y}, pf, a);
    }
    return a;
  };
  const int bh = b * HKV + kvh;
  char* stb = uni(ws + W_ST + (size_t)bh * 1024);
  char* h1 = uni(ws + W_H1 + (size_t)bh * (16 * 2048));
  const PairTag tg1 = pair_tag(T, lane);
  auto reduce_pass = [&](f32x4 a) -> v4u {
    *(LAS f32x4*)(L + L_HALF + (kh * 4 + ct) * 1024 + lane * 16) = a;
    BARRIER();
    v4u u = {0u, 0u, 0u, 0u};
    if (w < 2) {
      const LAS unsigned char* hp = L + L_HALF + 2 * w * 1024 + lane * 16;
      u = bfp_pair(*(const LAS f32x4*)hp + *(const LAS f32x4*)(hp + 4096), *(const LAS f32x4*)(hp + 1024) + *(const LAS f32x4*)(hp + 5120), tg1);
    }
    return u;
  };
  auto publish_pass = [&](int p, v4u u) {
    if (w < 2) st128_dev(((((s + 1 + p) & 3) * 4 + s) * 2 + w) * 1024 + lane * 16, u, h1);
  };
  STAMP(6);
  VMWAIT(8);
  BARRIER();
  STAMP(7);
  {
    const v4u a = reduce_pass(pv_pass(0));
    wo_loads(20, 23); v_half(1, 1);
    asm volatile("s_barrier" ::: "memory");
    wo_loads(23, 26); v_half(2, 0);
    publish_pass(0, a);
    if (w == 7) {
      float lt = *(LAS float*)(L + L_LS + c * 4);
#pragma unroll
      for (int ww = 1; ww < 8; ++ww) lt += *(LAS float*)(L + L_LS + (ww * 16 + c) * 4);
      if (g == 0) st128_dev((s * 16 + c) * 16, v4u{__float_as_uint(mrow), T, __float_as_uint(lt), T}, stb);
    }
    if (w < 2 || w == 7) VMWAIT(8); else VMWAIT(7);
  }
  BARRIER();
  v4u stu;
  {
    const v4u a = reduce_pass(pv_pass(1));
    wo_loads(26, 32); v_half(2, 1);
    asm volatile("s_barrier" ::: "memory");
    v_half(3, 0);
    publish_pass(1, a);
  }
  ld128_dev(stu, lane * 16, stb);
  if (w < 2) VMWAIT(6); else VMWAIT(5);
  asm volatile(""
               : "+v"(wv[0][0]), "+v"(wv[0][1]), "+v"(wv[0][2]), "+v"(wv[0][3]), "+v"(wv[0][4]), "+v"(wv[0][5]), "+v"(wv[0][6]), "+v"(wv[0][7]),
                 "+v"(wv[1][0]), "+v"(wv[1][1]), "+v"(wv[1][2]), "+v"(wv[1][3]), "+v"(wv[1][4]), "+v"(wv[1][5]), "+v"(wv[1][6]), "+v"(wv[1][7])
               :: "memory");
  asm volatile("" : "+v"(wv[2][0]), "+v"(wv[2][1]), "+v"(wv[2][2]), "+v"(wv[2][3]), "+v"(wv[2][4]), "+v"(wv[2][5]), "+v"(wv[2][6]), "+v"(wv[2][7]),
                    "+v"(wv[3][0]), "+v"(wv[3][1]), "+v"(wv[3][2]), "+v"(wv[3][3]), "+v"(wv[3][4]), "+v"(wv[3][5]), "+v"(wv[3][6]), "+v"(wv[3][7])
               :: "memory");
  BARRIER();
  publish_pass(2, reduce_pass(pv_pass(2)));
  v_half(3, 1);
  v4u hu[2];
  const int srcA = (s + (kh ? 3 : 1)) & 3, srcB = (s + (kh ? 3 : 2)) & 3;
  const unsigned hoA = ((s * 4 + srcA) * 2 + (ct >> 1)) * 1024 + lane * 16, hoB = ((s * 4 + srcB) * 2 + (ct >> 1)) * 1024 + lane * 16;
  v4u wf[4][4];
#pragma unroll
  for (int ks = 0; ks < 4; ++ks)
#pragma unroll
    for (int jj = 0; jj < 4; ++jj)
#pragma unroll
      for (int r = 0; r < 4; ++r) {
        const unsigned lo = wv[ks][2 * r][jj >> 1], hi = wv[ks][2 * r + 1][jj >> 1];
        wf[ks][jj][r] = (jj & 1) ? perm_hi(lo, hi) : perm_lo(lo, hi);
      }
  const int j32 = kvh * 4 + s;
  char* h2 = uni(ws + W_H2 + (size_t)b * (32 * 4 * 112 * 16));
  unsigned h2st = ((j32 * 4 + 2 * (g & 1) + (g >> 1)) * 112 + wc * 16 + c) * 16;
  const unsigned pos2 = 14 * (j32 & 7) + (c < 14 ? c : 13);
  unsigned h2ld = (((4 * w + g) * 4 + (j32 >> 3)) * 112 + pos2) * 16;
  PairTag tg2s = pair_tag(T, c), tg2l = pair_tag(T, pos2);
  unsigned obw = L_OB + c * 144 + (16 * ct + 4 * g) * 2, obr = L_OB + (c & 7) * 144 + g * 16, hlf = L_HALF + ct * 1024 + lane * 16;
  asm volatile("" : "+v"(wf[0][0]), "+v"(wf[0][1]), "+v"(wf[0][2]), "+v"(wf[0][3]), "+v"(wf[1][0]), "+v"(wf[1][1]), "+v"(wf[1][2]), "+v"(wf[1][3]),
                    "+v"(wf[2][0]), "+v"(wf[2][1]), "+v"(wf[2][2]), "+v"(wf[2][3]), "+v"(wf[3][0]), "+v"(wf[3][1]), "+v"(wf[3][2]), "+v"(wf[3][3]),
                    "+v"(h2st), "+v"(h2ld), "+v"(obw), "+v"(obr), "+v"(hlf), "+v"(tg2s.a), "+v"(tg2s.b), "+v"(tg2l.a), "+v"(tg2l.b));
  asm volatile("s_waitcnt vmcnt(4)" : "+v"(stu) :: "memory");
  for (unsigned spin = 0; any_bad(tags_bad(stu, T)) && spin < SPIN_CAP; ++spin) reload1(stu, lane * 16, stb);
  float fS, fA, fB;
  {
    const float ms = __uint_as_float(stu.x), ls = __uint_as_float(stu.z);
    const float M = xg_max(ms);
    const float e = __builtin_amdgcn_exp2f(ms - M);
    const float fw = e / xg_sum(e * ls);
    fS = __shfl(fw, 16 * s + c); fA = __shfl(fw, 16 * srcA + c); fB = kh ? 0.f : __shfl(fw, 16 * srcB + c);
  }
#define H1LATE 2
  asm volatile("s_waitcnt vmcnt(%0)" :: "n"(H1LATE) : "memory");
  const unsigned two = __builtin_amdgcn_readfirstlane(kh == 0);
  const PairTag tgB = two ? tg1 : PairTag{0u, 0u};
  hu[1] = v4u{0u, 0u, 0u, 0u};
  reload12(hu[0], hu[1], hoA, hoB, two, h1);
  STAMP(8);
  BARRIER();
  f32x4 own = pv_pass(3);
  STAMP(9);
  for (unsigned spin = 0; any_bad(pair_bad(hu[0], tg1) | pair_bad(hu[1], tgB)) && spin < SPIN_CAP; ++spin) reload12(hu[0], hu[1], hoA, hoB, two, h1);
  STAMP(11);
  {
    const unsigned odd = 0u - (unsigned)(ct & 1);
    own = own * fS + bfp_dec((hu[0].x & ~odd) | (hu[0].z & odd), (hu[0].y & ~odd) | (hu[0].w & odd)) * fA
                   + bfp_dec((hu[1].x & ~odd) | (hu[1].z & odd), (hu[1].y & ~odd) | (hu[1].w & odd)) * fB;
    if (kh == 1) *(LAS f32x4*)(L + hlf) = own;
    BARRIER();
    if (kh == 0) {
      own += *(LAS f32x4*)(L + hlf);
      *(LAS v2u*)(L + obw) = v2u{pack_bf16(own[0], own[1]), pack_bf16(own[2], own[3])};
    }
  }
  BARRIER();

  f32x4 pa[4];
#pragma unroll
  for (int jj = 0; jj < 4; ++jj) pa[jj] = f32x4{0.f, 0.f, 0.f, 0.f};
#pragma unroll
  for (int ks = 0; ks < 4; ++ks) {
    const v4u af = *(LAS v4u*)(L + obr + 8 * (ks >> 1) * 144 + 64 * (ks & 1));
#pragma unroll
    for (int jj = 0; jj < 4; ++jj) pa[jj] = mfma16(af, wf[ks][jj], pa[jj]);
  }
  if (w < 7) {
    const f32x4 v0 = (g >> 1) ? f32x4{pa[0][2], pa[1][2], pa[2][2], pa[3][2]} : f32x4{pa[0][0], pa[1][0], pa[2][0], pa[3][0]};
    const f32x4 v1 = (g >> 1) ? f32x4{pa[0][3], pa[1][3], pa[2][3], pa[3][3]} : f32x4{pa[0][1], pa[1][1], pa[2][1], pa[3][1]};
    st128_dev(h2st, bfp_pair(v0, v1, tg2s), h2);
  }
  STAMP(12);
  {
    v4u fu;
    ld128_dev(fu, h2ld, h2);
    asm volatile("s_waitcnt vmcnt(0)" : "+v"(fu) :: "memory");
    for (unsigned spin = 0; any_bad(pair_bad(fu, tg2l)) && spin < SPIN_CAP; ++spin) reload1(fu, h2ld, h2);
    STAMP(13);
    const f32x4 r0 = bfp_dec(fu.x, fu.y), r1 = bfp_dec(fu.z, fu.w);
    f32x4 t;
#pragma unroll
    for (int e = 0; e < 4; ++e) {
      const auto u = __builtin_amdgcn_permlane32_swap(__float_as_uint(r0[e]), __float_as_uint(r1[e]), false, false);
      const float h = __uint_as_float(u[0]) + __uint_as_float(u[1]);
      const auto v = __builtin_amdgcn_permlane16_swap(__float_as_uint(h), __float_as_uint(h), false, false);
      t[e] = __uint_as_float(v[0]) + __uint_as_float(v[1]);
    }
    *(LAS f32x4*)(L + L_RED + w * 1024 + lane * 16) = t;
  }
  BARRIER();
  if (w == 0) {
    f32x4 t = *(LAS f32x4*)(L + L_RED + lane * 16);
#pragma unroll
    for (int ww = 1; ww < 8; ++ww) t += *(LAS f32x4*)(L + L_RED + ww * 1024 + lane * 16);
    if ((lane & 31) < 14)
      *(v2u*)(out + (((b * SQ + 2 * (j32 >> 3) + (lane >> 5)) * DM + 56 * (j32 & 7) + 4 * (lane & 31)) >> 1)) =
          v2u{pack_bf16(t[0], t[1]), pack_bf16(t[2], t[3])};
  }
  if (w == 1 && (b & 1) == 0 && j32 == 0 && lane == 0)
    __hip_atomic_store((unsigned*)(ws + W_EP + rb * 256), T, __ATOMIC_RELAXED, __HIP_MEMORY_SCOPE_AGENT);
  asm volatile("s_waitcnt vmcnt(0)" ::: "memory");
  STAMP(14);
  {
    const unsigned long long until = tbeg + 6000ull + 4ull * (tprobe - tbeg);
    while (__builtin_amdgcn_s_memrealtime() < until) __builtin_amdgcn_s_sleep(4);
  }
}

}

extern "C" void kernel_launch(void* const* d_in, const int* in_sizes, int n_in, void* d_out, int out_size, void* d_ws,
                              size_t ws_size, hipStream_t stream) {
  (void)in_sizes; (void)n_in; (void)out_size;
  if (ws_size < W_END) return;
  (void)hipFuncSetAttribute((const void*)attn_fused_kernel, hipFuncAttributeMaxDynamicSharedMemorySize, LDS_BYTES);
  attn_fused_kernel<<<dim3(256), dim3(512), LDS_BYTES, stream>>>((const char*)d_in[0], (const char*)d_in[1], (const char*)d_in[2],
                                                                 (const char*)d_in[3], (const char*)d_in[4], (unsigned*)d_out, (char*)d_ws);
}
```

```cpp
#include <hip/hip_runtime.h>
#include <hip/hip_bf16.h>

namespace {

typedef unsigned v4u __attribute__((ext_vector_type(4)));
typedef unsigned v2u __attribute__((ext_vector_type(2)));
typedef float f32x4 __attribute__((ext_vector_type(4)));
typedef __bf16 bf16x8 __attribute__((ext_vector_type(8)));
typedef __bf16 bf16x2 __attribute__((ext_vector_type(2)));
typedef short s16x4 __attribute__((ext_vector_type(4)));
#define LAS __attribute__((address_space(3)))

constexpr int SQ = 8, DM = 448, HKV = 8, SK = 8192, WIN = 2048;
constexpr int NQ = 4096;
constexpr float C2 = 0.0625f * 1.4426950408889634f;
constexpr unsigned SPIN_CAP = 1u << 18;

constexpr int L_RING = 0;
constexpr int L_WQ = L_RING + 65536;
constexpr int L_P = 131072;
constexpr int L_X = L_P;
constexpr int L_QF = L_P + 16384;
constexpr int L_HALF = L_QF;
constexpr int L_MX = L_QF + 8192;
constexpr int L_LS = L_MX + 512;
constexpr int L_QR = L_LS + 512;
constexpr int L_FLAG = L_QR + 4096;
constexpr int L_OB = L_FLAG + 16;
constexpr int L_RED = L_P;
constexpr int LDS_BYTES = 163840;
static_assert(L_OB + 16 * 144 <= LDS_BYTES, "LDS map");

constexpr size_t W_EP = 0;
constexpr size_t W_QX = 4096;
constexpr size_t W_ST = W_QX + (size_t)8 * 4 * 8 * 2048;
constexpr size_t W_H1 = W_ST + (size_t)64 * 4 * 256;
constexpr size_t W_H2 = W_H1 + (size_t)64 * 16 * 2048;
constexpr size_t W_END = W_H2 + (size_t)8 * 32 * 4 * 112 * 16;

__device__ __forceinline__ f32x4 mfma16(v4u a, v4u b, f32x4 c) {
  return __builtin_amdgcn_mfma_f32_16x16x32_bf16(__builtin_bit_cast(bf16x8, a), __builtin_bit_cast(bf16x8, b), c, 0, 0, 0);
}
__device__ __forceinline__ unsigned pack_bf16(float lo, float hi) {
  bf16x2 r; r[0] = (__bf16)lo; r[1] = (__bf16)hi;
  return __builtin_bit_cast(unsigned, r);
}
__device__ __forceinline__ float xg_max(float x) {
  auto s = __builtin_amdgcn_permlane16_swap(__float_as_uint(x), __float_as_uint(x), false, false);
  x = fmaxf(__uint_as_float(s[0]), __uint_as_float(s[1]));
  auto t = __builtin_amdgcn_permlane32_swap(__float_as_uint(x), __float_as_uint(x), false, false);
  return fmaxf(__uint_as_float(t[0]), __uint_as_float(t[1]));
}
__device__ __forceinline__ float xg_sum(float x) {
  auto s = __builtin_amdgcn_permlane16_swap(__float_as_uint(x), __float_as_uint(x), false, false);
  x = __uint_as_float(s[0]) + __uint_as_float(s[1]);
  auto t = __builtin_amdgcn_permlane32_swap(__float_as_uint(x), __float_as_uint(x), false, false);
  return __uint_as_float(t[0]) + __uint_as_float(t[1]);
}
__device__ __forceinline__ unsigned perm_lo(unsigned a, unsigned b) { return __builtin_amdgcn_perm(b, a, 0x05040100u); }
__device__ __forceinline__ unsigned perm_hi(unsigned a, unsigned b) { return __builtin_amdgcn_perm(b, a, 0x07060302u); }

#define VMWAIT(N) asm volatile("s_waitcnt vmcnt(" #N ")" ::: "memory")
#define LGKM0() asm volatile("s_waitcnt lgkmcnt(0)" ::: "memory")
#define BARRIER() asm volatile("s_waitcnt lgkmcnt(0)\n\ts_barrier" ::: "memory")

__device__ __forceinline__ void dma_half(const char* gbase, unsigned lds_dst, unsigned v0, unsigned v1, unsigned v2, unsigned v3) {
  unsigned keep;
  asm volatile(
      "s_mov_b32 %[keep], m0\n\ts_mov_b32 m0, %[l0]\n\ts_nop 2\n\t"
      "global_load_lds_dwordx4 %[v0], %[sb] nt\n\t"
      "global_load_lds_dwordx4 %[v1], %[sb] offset:1024 nt\n\t"
      "global_load_lds_dwordx4 %[v2], %[sb] offset:2048 nt\n\t"
      "global_load_lds_dwordx4 %[v3], %[sb] offset:3072 nt\n\t"
      "s_mov_b32 m0, %[keep]"
      : [keep] "=&s"(keep)
      : [v0] "v"(v0), [v1] "v"(v1), [v2] "v"(v2), [v3] "v"(v3), [sb] "s"(gbase), [l0] "s"(lds_dst)
      : "memory");
}
__device__ __forceinline__ void dma1w(const char* sb, unsigned lds_dst, unsigned voff) {
  unsigned keep;
  asm volatile("s_mov_b32 %[keep], m0\n\ts_mov_b32 m0, %[l0]\n\ts_nop 2\n\tglobal_load_lds_dwordx4 %[v0], %[sb]\n\ts_mov_b32 m0, %[keep]"
               : [keep] "=&s"(keep) : [v0] "v"(voff), [sb] "s"(sb), [l0] "s"(lds_dst) : "memory");
}
__device__ __forceinline__ void dma1(const char* sb, unsigned lds_dst, unsigned voff) {
  unsigned keep;
  asm volatile("s_mov_b32 %[keep], m0\n\ts_mov_b32 m0, %[l0]\n\ts_nop 2\n\tglobal_load_lds_dwordx4 %[v0], %[sb] nt\n\ts_mov_b32 m0, %[keep]"
               : [keep] "=&s"(keep) : [v0] "v"(voff), [sb] "s"(sb), [l0] "s"(lds_dst) : "memory");
}
__device__ __forceinline__ void ld128_nt(v4u& d, unsigned voff, const char* sb) {
  asm volatile("global_load_dwordx4 %0, %1, %2 nt" : "=v"(d) : "v"(voff), "s"(sb) : "memory");
}
__device__ __forceinline__ void ld64(v2u& d, unsigned voff, const char* sb) {
  asm volatile("global_load_dwordx2 %0, %1, %2" : "=v"(d) : "v"(voff), "s"(sb) : "memory");
}
__device__ __forceinline__ void ld128_dev(v4u& d, unsigned voff, const char* sb) {
  asm volatile("global_load_dwordx4 %0, %1, %2 sc1" : "=v"(d) : "v"(voff), "s"(sb) : "memory");
}
__device__ __forceinline__ void ld32_dev(unsigned& d, unsigned voff, const char* sb) {
  asm volatile("global_load_dword %0, %1, %2 sc1" : "=v"(d) : "v"(voff), "s"(sb) : "memory");
}
__device__ __forceinline__ void st128_dev(unsigned voff, v4u v, char* sb) {
  asm volatile("global_store_dwordx4 %0, %1, %2 sc1\n\ts_nop 1" : : "v"(voff), "v"(v), "s"(sb) : "memory");
}
__device__ __forceinline__ unsigned tags_bad(v4u u, unsigned T) { return (u.y ^ T) | (u.w ^ T); }
struct PairTag { unsigned a, b; };
__device__ __forceinline__ PairTag pair_tag(unsigned T, unsigned pos) {
  const unsigned t = T >> (8u * (pos & 1u));
  return PairTag{t << 20, (t << 8) & 0xFFF00000u};
}
__device__ __forceinline__ v2u bfp_enc(f32x4 a, unsigned tag) {
  const unsigned m = max(max(max(__float_as_uint(a[0]) & 0x7FFFFFFFu, __float_as_uint(a[1]) & 0x7FFFFFFFu), __float_as_uint(a[2]) & 0x7FFFFFFFu),
                         __float_as_uint(a[3]) & 0x7FFFFFFFu);
  const unsigned e = max(m >> 23, 10u);
  const float sc = __uint_as_float((263u - e) << 23);
  unsigned q[4];
#pragma unroll
  for (int i = 0; i < 4; ++i) q[i] = __float_as_uint(__builtin_amdgcn_fmed3f(a[i] * sc, -1023.f, 1023.f) + 12582912.f);
  return v2u{(q[0] & 0x7FFu) | ((q[1] & 0x7FFu) << 11) | (q[2] << 22), ((q[2] >> 10) & 1u) | ((q[3] & 0x7FFu) << 1) | (e << 12) | tag};
}
__device__ __forceinline__ f32x4 bfp_dec(unsigned d0, unsigned d1) {
  const unsigned e = (d1 >> 12) & 0xFFu;
  const float sc = __uint_as_float(e == 255u ? 0x7F800000u : (e - 9u) << 23);
  const int q0 = __builtin_amdgcn_sbfe((int)d0, 0, 11), q1 = __builtin_amdgcn_sbfe((int)d0, 11, 11);
  const int q2 = __builtin_amdgcn_sbfe((int)__builtin_amdgcn_alignbit(d1, d0, 22), 0, 11), q3 = __builtin_amdgcn_sbfe((int)d1, 1, 11);
  return f32x4{(float)q0 * sc, (float)q1 * sc, (float)q2 * sc, (float)q3 * sc};
}
__device__ __forceinline__ v4u bfp_pair(f32x4 a, f32x4 b, PairTag t) {
  const v2u x = bfp_enc(a, t.a), y = bfp_enc(b, t.b);
  return v4u{x.x, x.y, y.x, y.y};
}
__device__ __forceinline__ unsigned pair_bad(v4u u, PairTag t) { return ((u.y ^ t.a) | (u.w ^ t.b)) & 0xFFF00000u; }
template <class P> __device__ __forceinline__ P* uni(P* p) {
  const unsigned long long a = (unsigned long long)p;
  const unsigned lo = __builtin_amdgcn_readfirstlane((unsigned)a), hi = __builtin_amdgcn_readfirstlane((unsigned)(a >> 32));
  return (P*)(((unsigned long long)hi << 32) | lo);
}
__device__ __forceinline__ bool any_bad(unsigned bad) { return __builtin_amdgcn_ballot_w64(bad != 0u) != 0ull; }
__device__ __forceinline__ void reload1(v4u& d0, unsigned o0, const char* sb) {
  asm volatile("global_load_dwordx4 %0, %1, %2 sc1\n\ts_waitcnt vmcnt(0)" : "=&v"(d0) : "v"(o0), "s"(sb) : "memory");
}
__device__ __forceinline__ void reload2(v4u& d0, v4u& d1, unsigned o0, unsigned o1, const char* sb) {
  asm volatile("global_load_dwordx4 %0, %2, %4 sc1\n\tglobal_load_dwordx4 %1, %3, %4 sc1\n\ts_waitcnt vmcnt(0)"
               : "=&v"(d0), "=&v"(d1) : "v"(o0), "v"(o1), "s"(sb) : "memory");
}

__device__ __forceinline__ void reload12(v4u& d0, v4u& d1, unsigned o0, unsigned o1, unsigned two, const char* sb) {
  asm volatile("global_load_dwordx4 %0, %2, %5 sc1\n\ts_cmp_eq_u32 %4, 0\n\ts_cbranch_scc1 .Lr12_%=\n\t"
               "global_load_dwordx4 %1, %3, %5 sc1\n.Lr12_%=:\n\ts_waitcnt vmcnt(0)"
               : "=&v"(d0), "+v"(d1) : "v"(o0), "v"(o1), "s"(two), "s"(sb) : "memory", "scc");
}

__global__ void __launch_bounds__(512, 2)
attn_fused_kernel(const char* __restrict__ x, const char* __restrict__ kc, const char* __restrict__ vc,
                  const char* __restrict__ wq, const char* __restrict__ wo, unsigned* __restrict__ out, char* __restrict__ ws) {
  extern __shared__ __attribute__((aligned(16))) unsigned char lds_raw[];
  LAS unsigned char* L = (LAS unsigned char*)lds_raw;
  const unsigned lbase = (unsigned)(size_t)L;
  const int tid = threadIdx.x, lane = tid & 63, w = __builtin_amdgcn_readfirstlane(tid >> 6);
  const int g = lane >> 4, c = lane & 15;
  const int bid = blockIdx.x, kvh = bid & 7, s = (bid >> 3) & 3, b = bid >> 5;
  const int rot = 8 * s + b;
  const int rb = b >> 1, cb = (b & 1) * 4 + s;
  asm volatile("" :: "s"(x), "s"(kc), "s"(vc), "s"(wq), "s"(wo), "s"(out), "s"(ws));
  if (tid == 0) *(LAS unsigned*)(L + L_FLAG) = 0u;

  const size_t headrow = (size_t)(b * HKV + kvh) * SK + (SK - WIN) + s * 512;
  const char* kwin = kc + headrow * 512;
  const char* vwin = vc + headrow * 512;
  {
    const unsigned r8 = lane >> 3, f = ((r8 >> 1) & 1) | ((w & 1) << 1);
    const unsigned vo = r8 * (NQ * 2) + (((lane & 7) ^ (f << 1)) << 4);
    const char* wqb = wq + (size_t)(8 * w) * (NQ * 2) + (kvh * 512 + cb * 64) * 2;
#pragma unroll
    for (int i = 0; i < 7; ++i) dma1w(wqb + (size_t)(64 * i) * (NQ * 2), lbase + L_WQ + (8 * i + w) * 1024, vo);
    const char* xb = x + (size_t)(16 * rb) * (DM * 2);
#pragma unroll
    for (int i = 0; i < 2; ++i) {
      const int m = 8 * i + w;
      if (m < 14) {
        const unsigned p = 64 * m + lane, row = (p * 1171u) >> 16, ch = p - 56 * row;
        dma1w(xb, lbase + L_X + m * 1024, row * (DM * 2) + (((ch & ~7u) | ((ch & 7u) ^ ((row >> 1) & 7u))) << 4));
      }
    }
  }
  unsigned ep = 0;
  if (w == 7) ld32_dev(ep, 0u, ws + W_EP + rb * 256);
  unsigned pf0 = 0, pf1 = 0;
  if (w == 6 && bid < 8) {
    unsigned long long pc;
    unsigned len;
    asm volatile("s_getpc_b64 %0\n.Lattn_pc:\n\ts_mov_b32 %1, .Lattn_end - .Lattn_pc - 4" : "=s"(pc), "=s"(len));
    asm volatile("s_nop 0\n\tglobal_load_dword %0, %2, %4\n\tglobal_load_dword %1, %3, %4"
                 : "=&v"(pf0), "=&v"(pf1) : "v"(min(lane * 128u, len)), "v"(min(lane * 128u + 8192u, len)), "s"(pc) : "memory");
  }
  asm volatile("s_barrier" ::: "memory");
  unsigned ko[8];
  {
    const unsigned hi = lane >> 5, l31 = lane & 31;
    const unsigned laneK = (hi << 9) | ((l31 ^ hi) << 4);
#pragma unroll
    for (int q = 0; q < 8; ++q) ko[q] = (laneK ^ (unsigned)(q << 5)) + ((q >> 2) << 12);
  }
  auto tile_off = [&](int unit) -> size_t { return (size_t)((unit + rot) & 31) * 8192; };
  auto k_half = [&](int t, int h) {
    dma_half(kwin + tile_off(8 * t + w), lbase + L_RING + ((t & 1) * 8 + w) * 8192 + h * 4096, ko[4 * h], ko[4 * h + 1], ko[4 * h + 2], ko[4 * h + 3]);
  };
  k_half(0, 0);
  asm volatile("s_waitcnt vmcnt(4)" : "+v"(ep), "+v"(pf0), "+v"(pf1) :: "memory");
  if (w == 7 && lane == 0) *(LAS unsigned*)(L + L_FLAG + 4) = ep + 0x10000001u;
  BARRIER();
  const unsigned T = __builtin_amdgcn_readfirstlane(*(LAS unsigned*)(L + L_FLAG + 4));
  char* qx = uni(ws + W_QX + (size_t)(kvh * 4 + rb) * (8 * 2048));
  {
    const int nt = w & 3, kh = w >> 2;
    const int q4 = (lane & 15) >> 2, p4 = lane & 3;
    const unsigned fa = (unsigned)(q4 >> 1) | ((unsigned)(g & 1) << 1);
    const unsigned ta = L_WQ + (224 * kh + 8 * g + q4) * 128 + (((2 * nt + (p4 >> 1)) ^ (fa << 1)) << 4) + (p4 & 1) * 8;
    f32x4 qa = {0.f, 0.f, 0.f, 0.f};
#pragma unroll
    for (int i = 0; i < 7; ++i) {
      const s16x4 a0 = __builtin_amdgcn_ds_read_tr16_b64_v4i16((LAS s16x4*)(L + ta + i * 4096));
      const s16x4 a1 = __builtin_amdgcn_ds_read_tr16_b64_v4i16((LAS s16x4*)(L + ta + i * 4096 + 512));
      const unsigned ch = 4 * (7 * kh + i) + g;
      const v4u xb = *(LAS v4u*)(L + L_X + c * 896 + (((ch & ~7u) | ((ch & 7u) ^ ((c >> 1) & 7u))) << 4));
      const v2u x0 = __builtin_bit_cast(v2u, a0), x1 = __builtin_bit_cast(v2u, a1);
      qa = mfma16(v4u{x0.x, x0.y, x1.x, x1.y}, xb, qa);
    }
    if (kh == 1) *(LAS f32x4*)(L + L_QR + nt * 1024 + lane * 16) = qa;
    BARRIER();
    if (kh == 0) {
      qa += *(LAS f32x4*)(L + L_QR + nt * 1024 + lane * 16);
      f32x4 qb;
#pragma unroll
      for (int i = 0; i < 4; ++i) qb[i] = __int_as_float(__builtin_amdgcn_mov_dpp(__float_as_int(qa[i]), 0xB1, 0xF, 0xF, true));
      const v4u u = bfp_pair(qa, qb, pair_tag(T, c >> 1));
      if ((c & 1) == 0) st128_dev(cb * 2048 + nt * 512 + (c >> 3) * 256 + g * 64 + ((c >> 1) & 3) * 16, u, qx);
    }
  }
  v4u kreg[8];
  const char* t2b = kwin + tile_off(16 + w);
  k_half(0, 1);
  k_half(1, 0);
  k_half(1, 1);
  if (w < 4) {
    v4u qu[2];
    const unsigned qgo = w * 2048 + (lane >> 4) * 512 + (b & 1) * 256 + (lane & 15) * 16;
    const PairTag tgq = pair_tag(T, lane & 3);
    reload2(qu[0], qu[1], qgo, qgo + 8192, qx);
    for (unsigned spin = 0; any_bad(pair_bad(qu[0], tgq) | pair_bad(qu[1], tgq)) && spin < SPIN_CAP; ++spin) reload2(qu[0], qu[1], qgo, qgo + 8192, qx);
    const int nt = lane >> 4, gq = (lane >> 2) & 3;
    const unsigned qfa = L_QF + (w * 2 + (nt >> 1)) * 1024 + (16 * (2 * (nt & 1) + (gq >> 1)) + 2 * (lane & 3)) * 16 + (gq & 1) * 8;
#pragma unroll
    for (int u = 0; u < 2; ++u) {
      const f32x4 r0 = bfp_dec(qu[u].x, qu[u].y), r1 = bfp_dec(qu[u].z, qu[u].w);
      *(LAS v2u*)(L + qfa + u * 128) = v2u{pack_bf16(r0[0], r0[1]), pack_bf16(r0[2], r0[3])};
      *(LAS v2u*)(L + qfa + u * 128 + 16) = v2u{pack_bf16(r1[0], r1[1]), pack_bf16(r1[2], r1[3])};
    }
    LGKM0();
    if (lane == 0) __hip_atomic_fetch_add((LAS unsigned*)(L + L_FLAG), 1u, __ATOMIC_RELAXED, __HIP_MEMORY_SCOPE_WORKGROUP);
  }
#pragma unroll
  for (int q = 0; q < 8; ++q) ld128_nt(kreg[q], q * 1024 + lane * 16, t2b);
  for (unsigned spin = 0; *(volatile LAS unsigned*)(L + L_FLAG) < 4u && spin < (SPIN_CAP << 6); ++spin) __builtin_amdgcn_s_sleep(1);
  asm volatile("" ::: "memory");
  VMWAIT(16);
  v4u qf[8];
#pragma unroll
  for (int st = 0; st < 8; ++st) qf[st] = *(LAS v4u*)(L + L_QF + st * 1024 + lane * 16);

  f32x4 sc[4];
  auto qk_tile = [&](int slot) -> f32x4 {
    v4u kf[8];
#pragma unroll
    for (int st = 0; st < 8; ++st) kf[st] = *(LAS v4u*)(L + L_RING + slot * 8192 + c * 512 + (((4 * st + g) ^ c) << 4));
    f32x4 a = {0.f, 0.f, 0.f, 0.f};
#pragma unroll
    for (int st = 0; st < 8; ++st) a = mfma16(kf[st], qf[st], a);
    return a;
  };
  const unsigned vlane = (lane >> 3) * 512 + (((lane & 7) ^ (((lane >> 4) & 3) << 1)) << 4);
  auto v_piece = [&](int p, int q) {
    dma1(vwin + tile_off(4 * w + (q >> 1)) + (q & 1) * 4096 + ((s + 1 + p) & 3) * 128,
         lbase + L_RING + ((p & 1) * 8 + w) * 8192 + q * 1024, vlane);
  };
  const int wc = w < 7 ? w : 6;
  v2u wv[4][8];
  const char* wob = wo + (size_t)(2 * kvh * 256 + 64 * s) * (DM * 2) + wc * 128;
  const unsigned wvo = (unsigned)(8 * g) * (DM * 2) + c * 8;
  auto wo_load = [&](int ks, int i) { ld64(wv[ks][i], wvo + (unsigned)(256 * (ks >> 1) + 32 * (ks & 1) + i) * (DM * 2), wob); };

  auto wo_loads = [&](int n0, int n1) {
#pragma unroll
    for (int n = n0; n < n1; ++n) wo_load(n >> 3, n & 7);
  };
  auto v_half = [&](int p, int h) {
#pragma unroll
    for (int q = 4 * h; q < 4 * h + 4; ++q) v_piece(p, q);
  };
  sc[0] = qk_tile(w);
  VMWAIT(8);
  sc[1] = qk_tile(8 + w);
  LGKM0();
  wo_loads(0, 4); k_half(3, 0); wo_loads(4, 7);
  asm volatile("s_waitcnt vmcnt(11)"
               : "+v"(kreg[0]), "+v"(kreg[1]), "+v"(kreg[2]), "+v"(kreg[3]), "+v"(kreg[4]), "+v"(kreg[5]), "+v"(kreg[6]), "+v"(kreg[7]) :: "memory");
#pragma unroll
  for (int q = 0; q < 8; ++q) *(LAS v4u*)(L + L_RING + w * 8192 + ko[q] + (q & 3) * 1024) = kreg[q];
  sc[2] = qk_tile(w);
  LGKM0();
  wo_loads(7, 10); k_half(3, 1); wo_loads(10, 13); v_half(0, 0);
  VMWAIT(7);
  sc[3] = qk_tile(8 + w);
  LGKM0();
  wo_loads(13, 16); v_half(0, 1);
  float mx = fmaxf(fmaxf(fmaxf(sc[0][0], sc[0][1]), fmaxf(sc[0][2], sc[0][3])), fmaxf(fmaxf(sc[1][0], sc[1][1]), fmaxf(sc[1][2], sc[1][3])));
  mx = fmaxf(mx, fmaxf(fmaxf(fmaxf(sc[2][0], sc[2][1]), fmaxf(sc[2][2], sc[2][3])), fmaxf(fmaxf(sc[3][0], sc[3][1]), fmaxf(sc[3][2], sc[3][3]))));
  mx = xg_max(mx);
  if (g == 0) *(LAS float*)(L + L_MX + (w * 16 + c) * 4) = mx;
  BARRIER();
  float mrow = *(LAS float*)(L + L_MX + c * 4);
#pragma unroll
  for (int ww = 1; ww < 8; ++ww) mrow = fmaxf(mrow, *(LAS float*)(L + L_MX + (ww * 16 + c) * 4));
  mrow *= C2;
  float lsum = 0.f;
#pragma unroll
  for (int t = 0; t < 4; ++t) {
    float p[4];
#pragma unroll
    for (int r = 0; r < 4; ++r) { p[r] = __builtin_amdgcn_exp2f(fmaf(sc[t][r], C2, -mrow)); lsum += p[r]; }
    *(LAS v2u*)(L + L_P + (4 * t + (w >> 1)) * 1024 + lane * 16 + (w & 1) * 8) = v2u{pack_bf16(p[0], p[1]), pack_bf16(p[2], p[3])};
  }
  lsum = xg_sum(lsum);
  if (g == 0) *(LAS float*)(L + L_LS + (w * 16 + c) * 4) = lsum;
  wo_loads(16, 20); v_half(1, 0);

  const int ct = w & 3, kh = w >> 2;
  unsigned tra;
  {
    const int q4 = (lane & 15) >> 2, p4 = lane & 3;
    tra = (4 * g + q4) * 128 + (((2 * ct + (p4 >> 1)) ^ (((2 * g + (q4 >> 1)) & 3) << 1)) << 4) + (p4 & 1) * 8 + kh * 32768;
  }
  auto pv_pass = [&](int p) -> f32x4 {
    f32x4 a = {0.f, 0.f, 0.f, 0.f};
#pragma unroll
    for (int i = 0; i < 8; ++i) {
      const v4u pf = *(LAS v4u*)(L + L_P + (8 * kh + i) * 1024 + lane * 16);
      const int ub = L_RING + (p & 1) * 65536 + i * 4096;
      const s16x4 a0 = __builtin_amdgcn_ds_read_tr16_b64_v4i16((LAS s16x4*)(L + ub + tra));
      const s16x4 a1 = __builtin_amdgcn_ds_read_tr16_b64_v4i16((LAS s16x4*)(L + ub + 2048 + tra));
      const v2u x0 = __builtin_bit_cast(v2u, a0), x1 = __builtin_bit_cast(v2u, a1);
      a = mfma16(v4u{x0.x, x0.y, x1.x, x1.y}, pf, a);
    }
    return a;
  };
  const int bh = b * HKV + kvh;
  char* stb = uni(ws + W_ST + (size_t)bh * 1024);
  char* h1 = uni(ws + W_H1 + (size_t)bh * (16 * 2048));
  const PairTag tg1 = pair_tag(T, lane);
  auto reduce_pass = [&](f32x4 a) -> v4u {
    *(LAS f32x4*)(L + L_HALF + (kh * 4 + ct) * 1024 + lane * 16) = a;
    BARRIER();
    v4u u = {0u, 0u, 0u, 0u};
    if (w < 2) {
      const LAS unsigned char* hp = L + L_HALF + 2 * w * 1024 + lane * 16;
      u = bfp_pair(*(const LAS f32x4*)hp + *(const LAS f32x4*)(hp + 4096), *(const LAS f32x4*)(hp + 1024) + *(const LAS f32x4*)(hp + 5120), tg1);
    }
    return u;
  };
  auto publish_pass = [&](int p, v4u u) {
    if (w < 2) st128_dev(((((s + 1 + p) & 3) * 4 + s) * 2 + w) * 1024 + lane * 16, u, h1);
  };
  VMWAIT(8);
  BARRIER();
  {
    const v4u a = reduce_pass(pv_pass(0));
    wo_loads(20, 23); v_half(1, 1);
    asm volatile("s_barrier" ::: "memory");
    wo_loads(23, 26); v_half(2, 0);
    publish_pass(0, a);
    if (w == 7) {
      float lt = *(LAS float*)(L + L_LS + c * 4);
#pragma unroll
      for (int ww = 1; ww < 8; ++ww) lt += *(LAS float*)(L + L_LS + (ww * 16 + c) * 4);
      if (g == 0) st128_dev((s * 16 + c) * 16, v4u{__float_as_uint(mrow), T, __float_as_uint(lt), T}, stb);
    }
    if (w < 2 || w == 7) VMWAIT(8); else VMWAIT(7);
  }
  BARRIER();
  v4u stu;
  {
    const v4u a = reduce_pass(pv_pass(1));
    wo_loads(26, 32); v_half(2, 1);
    asm volatile("s_barrier" ::: "memory");
    v_half(3, 0);
    publish_pass(1, a);
  }
  ld128_dev(stu, lane * 16, stb);
  if (w < 2) VMWAIT(6); else VMWAIT(5);
  asm volatile(""
               : "+v"(wv[0][0]), "+v"(wv[0][1]), "+v"(wv[0][2]), "+v"(wv[0][3]), "+v"(wv[0][4]), "+v"(wv[0][5]), "+v"(wv[0][6]), "+v"(wv[0][7]),
                 "+v"(wv[1][0]), "+v"(wv[1][1]), "+v"(wv[1][2]), "+v"(wv[1][3]), "+v"(wv[1][4]), "+v"(wv[1][5]), "+v"(wv[1][6]), "+v"(wv[1][7])
               :: "memory");
  asm volatile("" : "+v"(wv[2][0]), "+v"(wv[2][1]), "+v"(wv[2][2]), "+v"(wv[2][3]), "+v"(wv[2][4]), "+v"(wv[2][5]), "+v"(wv[2][6]), "+v"(wv[2][7]),
                    "+v"(wv[3][0]), "+v"(wv[3][1]), "+v"(wv[3][2]), "+v"(wv[3][3]), "+v"(wv[3][4]), "+v"(wv[3][5]), "+v"(wv[3][6]), "+v"(wv[3][7])
               :: "memory");
  BARRIER();
  publish_pass(2, reduce_pass(pv_pass(2)));
  v_half(3, 1);
  v4u hu[2];
  const int srcA = (s + (kh ? 3 : 1)) & 3, srcB = (s + (kh ? 3 : 2)) & 3;
  const unsigned hoA = ((s * 4 + srcA) * 2 + (ct >> 1)) * 1024 + lane * 16, hoB = ((s * 4 + srcB) * 2 + (ct >> 1)) * 1024 + lane * 16;
  v4u wf[4][4];
#pragma unroll
  for (int ks = 0; ks < 4; ++ks)
#pragma unroll
    for (int jj = 0; jj < 4; ++jj)
#pragma unroll
      for (int r = 0; r < 4; ++r) {
        const unsigned lo = wv[ks][2 * r][jj >> 1], hi = wv[ks][2 * r + 1][jj >> 1];
        wf[ks][jj][r] = (jj & 1) ? perm_hi(lo, hi) : perm_lo(lo, hi);
      }
  const int j32 = kvh * 4 + s;
  char* h2 = uni(ws + W_H2 + (size_t)b * (32 * 4 * 112 * 16));
  unsigned h2st = ((j32 * 4 + 2 * (g & 1) + (g >> 1)) * 112 + wc * 16 + c) * 16;
  const unsigned pos2 = 14 * (j32 & 7) + (c < 14 ? c : 13);
  unsigned h2ld = (((4 * w + g) * 4 + (j32 >> 3)) * 112 + pos2) * 16;
  PairTag tg2s = pair_tag(T, c), tg2l = pair_tag(T, pos2);
  unsigned obw = L_OB + c * 144 + (16 * ct + 4 * g) * 2, obr = L_OB + (c & 7) * 144 + g * 16, hlf = L_HALF + ct * 1024 + lane * 16;
  asm volatile("" : "+v"(wf[0][0]), "+v"(wf[0][1]), "+v"(wf[0][2]), "+v"(wf[0][3]), "+v"(wf[1][0]), "+v"(wf[1][1]), "+v"(wf[1][2]), "+v"(wf[1][3]),
                    "+v"(wf[2][0]), "+v"(wf[2][1]), "+v"(wf[2][2]), "+v"(wf[2][3]), "+v"(wf[3][0]), "+v"(wf[3][1]), "+v"(wf[3][2]), "+v"(wf[3][3]),
                    "+v"(h2st), "+v"(h2ld), "+v"(obw), "+v"(obr), "+v"(hlf), "+v"(tg2s.a), "+v"(tg2s.b), "+v"(tg2l.a), "+v"(tg2l.b));
  asm volatile("s_waitcnt vmcnt(4)" : "+v"(stu) :: "memory");
  for (unsigned spin = 0; any_bad(tags_bad(stu, T)) && spin < SPIN_CAP; ++spin) reload1(stu, lane * 16, stb);
  float fS, fA, fB;
  {
    const float ms = __uint_as_float(stu.x), ls = __uint_as_float(stu.z);
    const float M = xg_max(ms);
    const float e = __builtin_amdgcn_exp2f(ms - M);
    const float fw = e / xg_sum(e * ls);
    fS = __shfl(fw, 16 * s + c); fA = __shfl(fw, 16 * srcA + c); fB = kh ? 0.f : __shfl(fw, 16 * srcB + c);
  }
  VMWAIT(2);
  const unsigned two = __builtin_amdgcn_readfirstlane(kh == 0);
  const PairTag tgB = two ? tg1 : PairTag{0u, 0u};
  hu[1] = v4u{0u, 0u, 0u, 0u};
  reload12(hu[0], hu[1], hoA, hoB, two, h1);
  BARRIER();
  f32x4 own = pv_pass(3);
  for (unsigned spin = 0; any_bad(pair_bad(hu[0], tg1) | pair_bad(hu[1], tgB)) && spin < SPIN_CAP; ++spin) reload12(hu[0], hu[1], hoA, hoB, two, h1);
  {
    const unsigned odd = 0u - (unsigned)(ct & 1);
    own = own * fS + bfp_dec((hu[0].x & ~odd) | (hu[0].z & odd), (hu[0].y & ~odd) | (hu[0].w & odd)) * fA
                   + bfp_dec((hu[1].x & ~odd) | (hu[1].z & odd), (hu[1].y & ~odd) | (hu[1].w & odd)) * fB;
    if (kh == 1) *(LAS f32x4*)(L + hlf) = own;
    BARRIER();
    if (kh == 0) {
      own += *(LAS f32x4*)(L + hlf);
      *(LAS v2u*)(L + obw) = v2u{pack_bf16(own[0], own[1]), pack_bf16(own[2], own[3])};
    }
  }
  BARRIER();

  f32x4 pa[4];
#pragma unroll
  for (int jj = 0; jj < 4; ++jj) pa[jj] = f32x4{0.f, 0.f, 0.f, 0.f};
#pragma unroll
  for (int ks = 0; ks < 4; ++ks) {
    const v4u af = *(LAS v4u*)(L + obr + 8 * (ks >> 1) * 144 + 64 * (ks & 1));
#pragma unroll
    for (int jj = 0; jj < 4; ++jj) pa[jj] = mfma16(af, wf[ks][jj], pa[jj]);
  }
  if (w < 7) {
    const f32x4 v0 = (g >> 1) ? f32x4{pa[0][2], pa[1][2], pa[2][2], pa[3][2]} : f32x4{pa[0][0], pa[1][0], pa[2][0], pa[3][0]};
    const f32x4 v1 = (g >> 1) ? f32x4{pa[0][3], pa[1][3], pa[2][3], pa[3][3]} : f32x4{pa[0][1], pa[1][1], pa[2][1], pa[3][1]};
    st128_dev(h2st, bfp_pair(v0, v1, tg2s), h2);
  }
  {
    v4u fu;
    ld128_dev(fu, h2ld, h2);
    asm volatile("s_waitcnt vmcnt(0)" : "+v"(fu) :: "memory");
    for (unsigned spin = 0; any_bad(pair_bad(fu, tg2l)) && spin < SPIN_CAP; ++spin) reload1(fu, h2ld, h2);
    const f32x4 r0 = bfp_dec(fu.x, fu.y), r1 = bfp_dec(fu.z, fu.w);
    f32x4 t;
#pragma unroll
    for (int e = 0; e < 4; ++e) {
      const auto u = __builtin_amdgcn_permlane32_swap(__float_as_uint(r0[e]), __float_as_uint(r1[e]), false, false);
      const float h = __uint_as_float(u[0]) + __uint_as_float(u[1]);
      const auto v = __builtin_amdgcn_permlane16_swap(__float_as_uint(h), __float_as_uint(h), false, false);
      t[e] = __uint_as_float(v[0]) + __uint_as_float(v[1]);
    }
    *(LAS f32x4*)(L + L_RED + w * 1024 + lane * 16) = t;
  }
  BARRIER();
  if (w == 0) {
    f32x4 t = *(LAS f32x4*)(L + L_RED + lane * 16);
#pragma unroll
    for (int ww = 1; ww < 8; ++ww) t += *(LAS f32x4*)(L + L_RED + ww * 1024 + lane * 16);
    if ((lane & 31) < 14)
      *(v2u*)(out + (((b * SQ + 2 * (j32 >> 3) + (lane >> 5)) * DM + 56 * (j32 & 7) + 4 * (lane & 31)) >> 1)) =
          v2u{pack_bf16(t[0], t[1]), pack_bf16(t[2], t[3])};
  }
  if (w == 1 && (b & 1) == 0 && j32 == 0 && lane == 0)
    __hip_atomic_store((unsigned*)(ws + W_EP + rb * 256), T, __ATOMIC_RELAXED, __HIP_MEMORY_SCOPE_AGENT);
  asm volatile(".Lattn_end:" ::: "memory");
}

}

extern "C" void kernel_launch(void* const* d_in, const int* in_sizes, int n_in, void* d_out, int out_size, void* d_ws,
                              size_t ws_size, hipStream_t stream) {
  (void)in_sizes; (void)n_in; (void)out_size;
  if (ws_size < W_END) return;
  (void)hipFuncSetAttribute((const void*)attn_fused_kernel, hipFuncAttributeMaxDynamicSharedMemorySize, LDS_BYTES);
  attn_fused_kernel<<<dim3(256), dim3(512), LDS_BYTES, stream>>>((const char*)d_in[0], (const char*)d_in[1], (const char*)d_in[2],
                                                                 (const char*)d_in[3], (const char*)d_in[4], (unsigned*)d_out, (char*)d_ws);
}
```

```cpp
#include <hip/hip_runtime.h>
#include <hip/hip_bf16.h>

namespace {

typedef unsigned v4u __attribute__((ext_vector_type(4)));
typedef unsigned v2u __attribute__((ext_vector_type(2)));
typedef float f32x4 __attribute__((ext_vector_type(4)));
typedef __bf16 bf16x8 __attribute__((ext_vector_type(8)));
typedef __bf16 bf16x2 __attribute__((ext_vector_type(2)));
typedef short s16x4 __attribute__((ext_vector_type(4)));
#define LAS __attribute__((address_space(3)))

constexpr int SQ = 8, DM = 448, HKV = 8, SK = 8192, WIN = 2048;
constexpr int NQ = 4096;
constexpr float C2 = 0.0625f * 1.4426950408889634f;
constexpr unsigned SPIN_CAP = 1u << 18;

constexpr int L_RING = 0;
constexpr int L_WQ = L_RING + 65536;
constexpr int L_P = 131072;
constexpr int L_X = L_P;
constexpr int L_QF = L_P + 16384;
constexpr int L_HALF = L_QF;
constexpr int L_MX = L_QF + 8192;
constexpr int L_LS = L_MX + 512;
constexpr int L_QR = L_LS + 512;
constexpr int L_FLAG = L_QR + 4096;
constexpr int L_OB = L_FLAG + 16;
constexpr int L_RED = L_P;
constexpr int LDS_BYTES = 163840;
static_assert(L_OB + 16 * 144 <= LDS_BYTES, "LDS map");

constexpr size_t W_EP = 0;
constexpr size_t W_QX = 4096;
constexpr size_t W_ST = W_QX + (size_t)8 * 4 * 8 * 2048;
constexpr size_t W_H1 = W_ST + (size_t)64 * 4 * 256;
constexpr size_t W_H2 = W_H1 + (size_t)64 * 16 * 2048;
constexpr size_t W_END = W_H2 + (size_t)8 * 32 * 4 * 112 * 16;

__device__ __forceinline__ f32x4 mfma16(v4u a, v4u b, f32x4 c) {
  return __builtin_amdgcn_mfma_f32_16x16x32_bf16(__builtin_bit_cast(bf16x8, a), __builtin_bit_cast(bf16x8, b), c, 0, 0, 0);
}
__device__ __forceinline__ unsigned pack_bf16(float lo, float hi) {
  bf16x2 r; r[0] = (__bf16)lo; r[1] = (__bf16)hi;
  return __builtin_bit_cast(unsigned, r);
}
__device__ __forceinline__ float xg_max(float x) {
  auto s = __builtin_amdgcn_permlane16_swap(__float_as_uint(x), __float_as_uint(x), false, false);
  x = fmaxf(__uint_as_float(s[0]), __uint_as_float(s[1]));
  auto t = __builtin_amdgcn_permlane32_swap(__float_as_uint(x), __float_as_uint(x), false, false);
  return fmaxf(__uint_as_float(t[0]), __uint_as_float(t[1]));
}
__device__ __forceinline__ float xg_sum(float x) {
  auto s = __builtin_amdgcn_permlane16_swap(__float_as_uint(x), __float_as_uint(x), false, false);
  x = __uint_as_float(s[0]) + __uint_as_float(s[1]);
  auto t = __builtin_amdgcn_permlane32_swap(__float_as_uint(x), __float_as_uint(x), false, false);
  return __uint_as_float(t[0]) + __uint_as_float(t[1]);
}
__device__ __forceinline__ unsigned perm_lo(unsigned a, unsigned b) { return __builtin_amdgcn_perm(b, a, 0x05040100u); }
__device__ __forceinline__ unsigned perm_hi(unsigned a, unsigned b) { return __builtin_amdgcn_perm(b, a, 0x07060302u); }

#define VMWAIT(N) asm volatile("s_waitcnt vmcnt(" #N ")" ::: "memory")
#define LGKM0() asm volatile("s_waitcnt lgkmcnt(0)" ::: "memory")
#define BARRIER() asm volatile("s_waitcnt lgkmcnt(0)\n\ts_barrier" ::: "memory")

__device__ __forceinline__ void dma_half(const char* gbase, unsigned lds_dst, unsigned v0, unsigned v1, unsigned v2, unsigned v3) {
  unsigned keep;
  asm volatile(
      "s_mov_b32 %[keep], m0\n\ts_mov_b32 m0, %[l0]\n\ts_nop 2\n\t"
      "global_load_lds_dwordx4 %[v0], %[sb] nt\n\t"
      "global_load_lds_dwordx4 %[v1], %[sb] offset:1024 nt\n\t"
      "global_load_lds_dwordx4 %[v2], %[sb] offset:2048 nt\n\t"
      "global_load_lds_dwordx4 %[v3], %[sb] offset:3072 nt\n\t"
      "s_mov_b32 m0, %[keep]"
      : [keep] "=&s"(keep)
      : [v0] "v"(v0), [v1] "v"(v1), [v2] "v"(v2), [v3] "v"(v3), [sb] "s"(gbase), [l0] "s"(lds_dst)
      : "memory");
}
__device__ __forceinline__ void dma1w(const char* sb, unsigned lds_dst, unsigned voff) {
  unsigned keep;
  asm volatile("s_mov_b32 %[keep], m0\n\ts_mov_b32 m0, %[l0]\n\ts_nop 2\n\tglobal_load_lds_dwordx4 %[v0], %[sb]\n\ts_mov_b32 m0, %[keep]"
               : [keep] "=&s"(keep) : [v0] "v"(voff), [sb] "s"(sb), [l0] "s"(lds_dst) : "memory");
}
__device__ __forceinline__ void dma1(const char* sb, unsigned lds_dst, unsigned voff) {
  unsigned keep;
  asm volatile("s_mov_b32 %[keep], m0\n\ts_mov_b32 m0, %[l0]\n\ts_nop 2\n\tglobal_load_lds_dwordx4 %[v0], %[sb] nt\n\ts_mov_b32 m0, %[keep]"
               : [keep] "=&s"(keep) : [v0] "v"(voff), [sb] "s"(sb), [l0] "s"(lds_dst) : "memory");
}
__device__ __forceinline__ void ld128_nt(v4u& d, unsigned voff, const char* sb) {
  asm volatile("global_load_dwordx4 %0, %1, %2 nt" : "=v"(d) : "v"(voff), "s"(sb) : "memory");
}
__device__ __forceinline__ void ld64(v2u& d, unsigned voff, const char* sb) {
  asm volatile("global_load_dwordx2 %0, %1, %2" : "=v"(d) : "v"(voff), "s"(sb) : "memory");
}
__device__ __forceinline__ void ld128_dev(v4u& d, unsigned voff, const char* sb) {
  asm volatile("global_load_dwordx4 %0, %1, %2 sc1" : "=v"(d) : "v"(voff), "s"(sb) : "memory");
}
__device__ __forceinline__ void ld32_dev(unsigned& d, unsigned voff, const char* sb) {
  asm volatile("global_load_dword %0, %1, %2 sc1" : "=v"(d) : "v"(voff), "s"(sb) : "memory");
}
__device__ __forceinline__ void st128_dev(unsigned voff, v4u v, char* sb) {
  asm volatile("global_store_dwordx4 %0, %1, %2 sc1\n\ts_nop 1" : : "v"(voff), "v"(v), "s"(sb) : "memory");
}
__device__ __forceinline__ unsigned tags_bad(v4u u, unsigned T) { return (u.y ^ T) | (u.w ^ T); }
struct PairTag { unsigned a, b; };
__device__ __forceinline__ PairTag pair_tag(unsigned T, unsigned pos) {
  const unsigned t = T >> (8u * (pos & 1u));
  return PairTag{t << 20, (t << 8) & 0xFFF00000u};
}
__device__ __forceinline__ v2u bfp_enc(f32x4 a, unsigned tag) {
  const unsigned m = max(max(max(__float_as_uint(a[0]) & 0x7FFFFFFFu, __float_as_uint(a[1]) & 0x7FFFFFFFu), __float_as_uint(a[2]) & 0x7FFFFFFFu),
                         __float_as_uint(a[3]) & 0x7FFFFFFFu);
  const unsigned e = max(m >> 23, 10u);
  const float sc = __uint_as_float((263u - e) << 23);
  unsigned q[4];
#pragma unroll
  for (int i = 0; i < 4; ++i) q[i] = __float_as_uint(__builtin_amdgcn_fmed3f(a[i] * sc, -1023.f, 1023.f) + 12582912.f);
  return v2u{(q[0] & 0x7FFu) | ((q[1] & 0x7FFu) << 11) | (q[2] << 22), ((q[2] >> 10) & 1u) | ((q[3] & 0x7FFu) << 1) | (e << 12) | tag};
}
__device__ __forceinline__ f32x4 bfp_dec(unsigned d0, unsigned d1) {
  const unsigned e = (d1 >> 12) & 0xFFu;
  const float sc = __uint_as_float(e == 255u ? 0x7F800000u : (e - 9u) << 23);
  const int q0 = __builtin_amdgcn_sbfe((int)d0, 0, 11), q1 = __builtin_amdgcn_sbfe((int)d0, 11, 11);
  const int q2 = __builtin_amdgcn_sbfe((int)__builtin_amdgcn_alignbit(d1, d0, 22), 0, 11), q3 = __builtin_amdgcn_sbfe((int)d1, 1, 11);
  return f32x4{(float)q0 * sc, (float)q1 * sc, (float)q2 * sc, (float)q3 * sc};
}
__device__ __forceinline__ v4u bfp_pair(f32x4 a, f32x4 b, PairTag t) {
  const v2u x = bfp_enc(a, t.a), y = bfp_enc(b, t.b);
  return v4u{x.x, x.y, y.x, y.y};
}
__device__ __forceinline__ unsigned pair_bad(v4u u, PairTag t) { return ((u.y ^ t.a) | (u.w ^ t.b)) & 0xFFF00000u; }
template <class P> __device__ __forceinline__ P* uni(P* p) {
  const unsigned long long a = (unsigned long long)p;
  const unsigned lo = __builtin_amdgcn_readfirstlane((unsigned)a), hi = __builtin_amdgcn_readfirstlane((unsigned)(a >> 32));
  return (P*)(((unsigned long long)hi << 32) | lo);
}
__device__ __forceinline__ bool any_bad(unsigned bad) { return __builtin_amdgcn_ballot_w64(bad != 0u) != 0ull; }
__device__ __forceinline__ void reload1(v4u& d0, unsigned o0, const char* sb) {
  asm volatile("global_load_dwordx4 %0, %1, %2 sc1\n\ts_waitcnt vmcnt(0)" : "=&v"(d0) : "v"(o0), "s"(sb) : "memory");
}
__device__ __forceinline__ void reload2(v4u& d0, v4u& d1, unsigned o0, unsigned o1, const char* sb) {
  asm volatile("global_load_dwordx4 %0, %2, %4 sc1\n\tglobal_load_dwordx4 %1, %3, %4 sc1\n\ts_waitcnt vmcnt(0)"
               : "=&v"(d0), "=&v"(d1) : "v"(o0), "v"(o1), "s"(sb) : "memory");
}

__device__ __forceinline__ void reload12(v4u& d0, v4u& d1, unsigned o0, unsigned o1, unsigned two, const char* sb) {
  asm volatile("global_load_dwordx4 %0, %2, %5 sc1\n\ts_cmp_eq_u32 %4, 0\n\ts_cbranch_scc1 .Lr12_%=\n\t"
               "global_load_dwordx4 %1, %3, %5 sc1\n.Lr12_%=:\n\ts_waitcnt vmcnt(0)"
               : "=&v"(d0), "+v"(d1) : "v"(o0), "v"(o1), "s"(two), "s"(sb) : "memory", "scc");
}

__global__ void __launch_bounds__(512, 2)
attn_fused_kernel(const char* __restrict__ x, const char* __restrict__ kc, const char* __restrict__ vc,
                  const char* __restrict__ wq, const char* __restrict__ wo, unsigned* __restrict__ out, char* __restrict__ ws) {
  extern __shared__ __attribute__((aligned(16))) unsigned char lds_raw[];
  LAS unsigned char* L = (LAS unsigned char*)lds_raw;
  const unsigned lbase = (unsigned)(size_t)L;
  const int tid = threadIdx.x, lane = tid & 63, w = __builtin_amdgcn_readfirstlane(tid >> 6);
  const int g = lane >> 4, c = lane & 15;
  const int bid = blockIdx.x, kvh = bid & 7, s = (bid >> 3) & 3, b = bid >> 5;
  const int rot = 8 * s + b;
  const int rb = b >> 1, cb = (b & 1) * 4 + s;
  asm volatile("" :: "s"(x), "s"(kc), "s"(vc), "s"(wq), "s"(wo), "s"(out), "s"(ws));
  if (tid == 0) *(LAS unsigned*)(L + L_FLAG) = 0u;

  unsigned pf0 = 0;
  if (w == 6) {
    unsigned long long pc;
    unsigned len;
    asm volatile("s_getpc_b64 %0\n.Lattn_pc:\n\ts_mov_b32 %1, .Lattn_end - .Lattn_pc - 4" : "=s"(pc), "=s"(len));
    len = (int)len > 0 ? len : 0u;
    if (lane < 4) asm volatile("s_nop 0\n\tglobal_load_dword %0, %1, %2" : "+v"(pf0) : "v"(min(((bid >> 3) * 4u + lane) * 128u, len)), "s"(pc) : "memory");
  }
  const size_t headrow = (size_t)(b * HKV + kvh) * SK + (SK - WIN) + s * 512;
  const char* kwin = kc + headrow * 512;
  const char* vwin = vc + headrow * 512;
  {
    const unsigned r8 = lane >> 3, f = ((r8 >> 1) & 1) | ((w & 1) << 1);
    const unsigned vo = r8 * (NQ * 2) + (((lane & 7) ^ (f << 1)) << 4);
    const char* wqb = wq + (size_t)(8 * w) * (NQ * 2) + (kvh * 512 + cb * 64) * 2;
#pragma unroll
    for (int i = 0; i < 7; ++i) dma1w(wqb + (size_t)(64 * i) * (NQ * 2), lbase + L_WQ + (8 * i + w) * 1024, vo);
    const char* xb = x + (size_t)(16 * rb) * (DM * 2);
#pragma unroll
    for (int i = 0; i < 2; ++i) {
      const int m = 8 * i + w;
      if (m < 14) {
        const unsigned p = 64 * m + lane, row = (p * 1171u) >> 16, ch = p - 56 * row;
        dma1w(xb, lbase + L_X + m * 1024, row * (DM * 2) + (((ch & ~7u) | ((ch & 7u) ^ ((row >> 1) & 7u))) << 4));
      }
    }
  }
  unsigned ep = 0;
  if (w == 7) ld32_dev(ep, 0u, ws + W_EP + rb * 256);
  asm volatile("s_barrier" ::: "memory");
  unsigned ko[8];
  {
    const unsigned hi = lane >> 5, l31 = lane & 31;
    const unsigned laneK = (hi << 9) | ((l31 ^ hi) << 4);
#pragma unroll
    for (int q = 0; q < 8; ++q) ko[q] = (laneK ^ (unsigned)(q << 5)) + ((q >> 2) << 12);
  }
  auto tile_off = [&](int unit) -> size_t { return (size_t)((unit + rot) & 31) * 8192; };
  auto k_half = [&](int t, int h) {
    dma_half(kwin + tile_off(8 * t + w), lbase + L_RING + ((t & 1) * 8 + w) * 8192 + h * 4096, ko[4 * h], ko[4 * h + 1], ko[4 * h + 2], ko[4 * h + 3]);
  };
  k_half(0, 0);
  asm volatile("s_waitcnt vmcnt(4)" : "+v"(ep), "+v"(pf0) :: "memory");
  if (w == 7 && lane == 0) *(LAS unsigned*)(L + L_FLAG + 4) = ep + 0x10000001u;
  BARRIER();
  const unsigned T = __builtin_amdgcn_readfirstlane(*(LAS unsigned*)(L + L_FLAG + 4));
  char* qx = uni(ws + W_QX + (size_t)(kvh * 4 + rb) * (8 * 2048));
  {
    const int nt = w & 3, kh = w >> 2;
    const int q4 = (lane & 15) >> 2, p4 = lane & 3;
    const unsigned fa = (unsigned)(q4 >> 1) | ((unsigned)(g & 1) << 1);
    const unsigned ta = L_WQ + (224 * kh + 8 * g + q4) * 128 + (((2 * nt + (p4 >> 1)) ^ (fa << 1)) << 4) + (p4 & 1) * 8;
    f32x4 qa = {0.f, 0.f, 0.f, 0.f};
#pragma unroll
    for (int i = 0; i < 7; ++i) {
      const s16x4 a0 = __builtin_amdgcn_ds_read_tr16_b64_v4i16((LAS s16x4*)(L + ta + i * 4096));
      const s16x4 a1 = __builtin_amdgcn_ds_read_tr16_b64_v4i16((LAS s16x4*)(L + ta + i * 4096 + 512));
      const unsigned ch = 4 * (7 * kh + i) + g;
      const v4u xb = *(LAS v4u*)(L + L_X + c * 896 + (((ch & ~7u) | ((ch & 7u) ^ ((c >> 1) & 7u))) << 4));
      const v2u x0 = __builtin_bit_cast(v2u, a0), x1 = __builtin_bit_cast(v2u, a1);
      qa = mfma16(v4u{x0.x, x0.y, x1.x, x1.y}, xb, qa);
    }
    if (kh == 1) *(LAS f32x4*)(L + L_QR + nt * 1024 + lane * 16) = qa;
    BARRIER();
    if (kh == 0) {
      qa += *(LAS f32x4*)(L + L_QR + nt * 1024 + lane * 16);
      f32x4 qb;
#pragma unroll
      for (int i = 0; i < 4; ++i) qb[i] = __int_as_float(__builtin_amdgcn_mov_dpp(__float_as_int(qa[i]), 0xB1, 0xF, 0xF, true));
      const v4u u = bfp_pair(qa, qb, pair_tag(T, c >> 1));
      if ((c & 1) == 0) st128_dev(cb * 2048 + nt * 512 + (c >> 3) * 256 + g * 64 + ((c >> 1) & 3) * 16, u, qx);
    }
  }
  v4u kreg[8];
  const char* t2b = kwin + tile_off(16 + w);
  k_half(0, 1);
  k_half(1, 0);
  k_half(1, 1);
  if (w < 4) {
    v4u qu[2];
    const unsigned qgo = w * 2048 + (lane >> 4) * 512 + (b & 1) * 256 + (lane & 15) * 16;
    const PairTag tgq = pair_tag(T, lane & 3);
    reload2(qu[0], qu[1], qgo, qgo + 8192, qx);
    for (unsigned spin = 0; any_bad(pair_bad(qu[0], tgq) | pair_bad(qu[1], tgq)) && spin < SPIN_CAP; ++spin) reload2(qu[0], qu[1], qgo, qgo + 8192, qx);
    const int nt = lane >> 4, gq = (lane >> 2) & 3;
    const unsigned qfa = L_QF + (w * 2 + (nt >> 1)) * 1024 + (16 * (2 * (nt & 1) + (gq >> 1)) + 2 * (lane & 3)) * 16 + (gq & 1) * 8;
#pragma unroll
    for (int u = 0; u < 2; ++u) {
      const f32x4 r0 = bfp_dec(qu[u].x, qu[u].y), r1 = bfp_dec(qu[u].z, qu[u].w);
      *(LAS v2u*)(L + qfa + u * 128) = v2u{pack_bf16(r0[0], r0[1]), pack_bf16(r0[2], r0[3])};
      *(LAS v2u*)(L + qfa + u * 128 + 16) = v2u{pack_bf16(r1[0], r1[1]), pack_bf16(r1[2], r1[3])};
    }
    LGKM0();
    if (lane == 0) __hip_atomic_fetch_add((LAS unsigned*)(L + L_FLAG), 1u, __ATOMIC_RELAXED, __HIP_MEMORY_SCOPE_WORKGROUP);
  }
#pragma unroll
  for (int q = 0; q < 8; ++q) ld128_nt(kreg[q], q * 1024 + lane * 16, t2b);
  for (unsigned spin = 0; *(volatile LAS unsigned*)(L + L_FLAG) < 4u && spin < (SPIN_CAP << 6); ++spin) __builtin_amdgcn_s_sleep(1);
  asm volatile("" ::: "memory");
  VMWAIT(16);
  v4u qf[8];
#pragma unroll
  for (int st = 0; st < 8; ++st) qf[st] = *(LAS v4u*)(L + L_QF + st * 1024 + lane * 16);

  f32x4 sc[4];
  auto qk_tile = [&](int slot) -> f32x4 {
    v4u kf[8];
#pragma unroll
    for (int st = 0; st < 8; ++st) kf[st] = *(LAS v4u*)(L + L_RING + slot * 8192 + c * 512 + (((4 * st + g) ^ c) << 4));
    f32x4 a = {0.f, 0.f, 0.f, 0.f};
#pragma unroll
    for (int st = 0; st < 8; ++st) a = mfma16(kf[st], qf[st], a);
    return a;
  };
  const unsigned vlane = (lane >> 3) * 512 + (((lane & 7) ^ (((lane >> 4) & 3) << 1)) << 4);
  auto v_piece = [&](int p, int q) {
    dma1(vwin + tile_off(4 * w + (q >> 1)) + (q & 1) * 4096 + ((s + 1 + p) & 3) * 128,
         lbase + L_RING + ((p & 1) * 8 + w) * 8192 + q * 1024, vlane);
  };
  const int wc = w < 7 ? w : 6;
  v2u wv[4][8];
  const char* wob = wo + (size_t)(2 * kvh * 256 + 64 * s) * (DM * 2) + wc * 128;
  const unsigned wvo = (unsigned)(8 * g) * (DM * 2) + c * 8;
  auto wo_load = [&](int ks, int i) { ld64(wv[ks][i], wvo + (unsigned)(256 * (ks >> 1) + 32 * (ks & 1) + i) * (DM * 2), wob); };

  auto wo_loads = [&](int n0, int n1) {
#pragma unroll
    for (int n = n0; n < n1; ++n) wo_load(n >> 3, n & 7);
  };
  auto v_half = [&](int p, int h) {
#pragma unroll
    for (int q = 4 * h; q < 4 * h + 4; ++q) v_piece(p, q);
  };
  sc[0] = qk_tile(w);
  VMWAIT(8);
  sc[1] = qk_tile(8 + w);
  LGKM0();
  wo_loads(0, 4); k_half(3, 0); wo_loads(4, 7);
  asm volatile("s_waitcnt vmcnt(11)"
               : "+v"(kreg[0]), "+v"(kreg[1]), "+v"(kreg[2]), "+v"(kreg[3]), "+v"(kreg[4]), "+v"(kreg[5]), "+v"(kreg[6]), "+v"(kreg[7]) :: "memory");
#pragma unroll
  for (int q = 0; q < 8; ++q) *(LAS v4u*)(L + L_RING + w * 8192 + ko[q] + (q & 3) * 1024) = kreg[q];
  sc[2] = qk_tile(w);
  LGKM0();
  wo_loads(7, 10); k_half(3, 1); wo_loads(10, 13); v_half(0, 0);
  VMWAIT(7);
  sc[3] = qk_tile(8 + w);
  LGKM0();
  wo_loads(13, 16); v_half(0, 1);
  float mx = fmaxf(fmaxf(fmaxf(sc[0][0], sc[0][1]), fmaxf(sc[0][2], sc[0][3])), fmaxf(fmaxf(sc[1][0], sc[1][1]), fmaxf(sc[1][2], sc[1][3])));
  mx = fmaxf(mx, fmaxf(fmaxf(fmaxf(sc[2][0], sc[2][1]), fmaxf(sc[2][2], sc[2][3])), fmaxf(fmaxf(sc[3][0], sc[3][1]), fmaxf(sc[3][2], sc[3][3]))));
  mx = xg_max(mx);
  if (g == 0) *(LAS float*)(L + L_MX + (w * 16 + c) * 4) = mx;
  BARRIER();
  float mrow = *(LAS float*)(L + L_MX + c * 4);
#pragma unroll
  for (int ww = 1; ww < 8; ++ww) mrow = fmaxf(mrow, *(LAS float*)(L + L_MX + (ww * 16 + c) * 4));
  mrow *= C2;
  float lsum = 0.f;
#pragma unroll
  for (int t = 0; t < 4; ++t) {
    float p[4];
#pragma unroll
    for (int r = 0; r < 4; ++r) { p[r] = __builtin_amdgcn_exp2f(fmaf(sc[t][r], C2, -mrow)); lsum += p[r]; }
    *(LAS v2u*)(L + L_P + (4 * t + (w >> 1)) * 1024 + lane * 16 + (w & 1) * 8) = v2u{pack_bf16(p[0], p[1]), pack_bf16(p[2], p[3])};
  }
  lsum = xg_sum(lsum);
  if (g == 0) *(LAS float*)(L + L_LS + (w * 16 + c) * 4) = lsum;
  wo_loads(16, 20); v_half(1, 0);

  const int ct = w & 3, kh = w >> 2;
  unsigned tra;
  {
    const int q4 = (lane & 15) >> 2, p4 = lane & 3;
    tra = (4 * g + q4) * 128 + (((2 * ct + (p4 >> 1)) ^ (((2 * g + (q4 >> 1)) & 3) << 1)) << 4) + (p4 & 1) * 8 + kh * 32768;
  }
  auto pv_pass = [&](int p) -> f32x4 {
    f32x4 a = {0.f, 0.f, 0.f, 0.f};
#pragma unroll
    for (int i = 0; i < 8; ++i) {
      const v4u pf = *(LAS v4u*)(L + L_P + (8 * kh + i) * 1024 + lane * 16);
      const int ub = L_RING + (p & 1) * 65536 + i * 4096;
      const s16x4 a0 = __builtin_amdgcn_ds_read_tr16_b64_v4i16((LAS s16x4*)(L + ub + tra));
      const s16x4 a1 = __builtin_amdgcn_ds_read_tr16_b64_v4i16((LAS s16x4*)(L + ub + 2048 + tra));
      const v2u x0 = __builtin_bit_cast(v2u, a0), x1 = __builtin_bit_cast(v2u, a1);
      a = mfma16(v4u{x0.x, x0.y, x1.x, x1.y}, pf, a);
    }
    return a;
  };
  const int bh = b * HKV + kvh;
  char* stb = uni(ws + W_ST + (size_t)bh * 1024);
  char* h1 = uni(ws + W_H1 + (size_t)bh * (16 * 2048));
  const PairTag tg1 = pair_tag(T, lane);
  auto reduce_pass = [&](f32x4 a) -> v4u {
    *(LAS f32x4*)(L + L_HALF + (kh * 4 + ct) * 1024 + lane * 16) = a;
    BARRIER();
    v4u u = {0u, 0u, 0u, 0u};
    if (w < 2) {
      const LAS unsigned char* hp = L + L_HALF + 2 * w * 1024 + lane * 16;
      u = bfp_pair(*(const LAS f32x4*)hp + *(const LAS f32x4*)(hp + 4096), *(const LAS f32x4*)(hp + 1024) + *(const LAS f32x4*)(hp + 5120), tg1);
    }
    return u;
  };
  auto publish_pass = [&](int p, v4u u) {
    if (w < 2) st128_dev(((((s + 1 + p) & 3) * 4 + s) * 2 + w) * 1024 + lane * 16, u, h1);
  };
  VMWAIT(8);
  BARRIER();
  {
    const v4u a = reduce_pass(pv_pass(0));
    wo_loads(20, 23); v_half(1, 1);
    asm volatile("s_barrier" ::: "memory");
    wo_loads(23, 26); v_half(2, 0);
    publish_pass(0, a);
    if (w == 7) {
      float lt = *(LAS float*)(L + L_LS + c * 4);
#pragma unroll
      for (int ww = 1; ww < 8; ++ww) lt += *(LAS float*)(L + L_LS + (ww * 16 + c) * 4);
      if (g == 0) st128_dev((s * 16 + c) * 16, v4u{__float_as_uint(mrow), T, __float_as_uint(lt), T}, stb);
    }
    if (w < 2 || w == 7) VMWAIT(8); else VMWAIT(7);
  }
  BARRIER();
  v4u stu;
  {
    const v4u a = reduce_pass(pv_pass(1));
    wo_loads(26, 32); v_half(2, 1);
    asm volatile("s_barrier" ::: "memory");
    v_half(3, 0);
    publish_pass(1, a);
  }
  ld128_dev(stu, lane * 16, stb);
  if (w < 2) VMWAIT(6); else VMWAIT(5);
  asm volatile(""
               : "+v"(wv[0][0]), "+v"(wv[0][1]), "+v"(wv[0][2]), "+v"(wv[0][3]), "+v"(wv[0][4]), "+v"(wv[0][5]), "+v"(wv[0][6]), "+v"(wv[0][7]),
                 "+v"(wv[1][0]), "+v"(wv[1][1]), "+v"(wv[1][2]), "+v"(wv[1][3]), "+v"(wv[1][4]), "+v"(wv[1][5]), "+v"(wv[1][6]), "+v"(wv[1][7])
               :: "memory");
  asm volatile("" : "+v"(wv[2][0]), "+v"(wv[2][1]), "+v"(wv[2][2]), "+v"(wv[2][3]), "+v"(wv[2][4]), "+v"(wv[2][5]), "+v"(wv[2][6]), "+v"(wv[2][7]),
                    "+v"(wv[3][0]), "+v"(wv[3][1]), "+v"(wv[3][2]), "+v"(wv[3][3]), "+v"(wv[3][4]), "+v"(wv[3][5]), "+v"(wv[3][6]), "+v"(wv[3][7])
               :: "memory");
  BARRIER();
  publish_pass(2, reduce_pass(pv_pass(2)));
  v_half(3, 1);
  v4u hu[2];
  const int srcA = (s + (kh ? 3 : 1)) & 3, srcB = (s + (kh ? 3 : 2)) & 3;
  const unsigned hoA = ((s * 4 + srcA) * 2 + (ct >> 1)) * 1024 + lane * 16, hoB = ((s * 4 + srcB) * 2 + (ct >> 1)) * 1024 + lane * 16;
  v4u wf[4][4];
#pragma unroll
  for (int ks = 0; ks < 4; ++ks)
#pragma unroll
    for (int jj = 0; jj < 4; ++jj)
#pragma unroll
      for (int r = 0; r < 4; ++r) {
        const unsigned lo = wv[ks][2 * r][jj >> 1], hi = wv[ks][2 * r + 1][jj >> 1];
        wf[ks][jj][r] = (jj & 1) ? perm_hi(lo, hi) : perm_lo(lo, hi);
      }
  const int j32 = kvh * 4 + s;
  char* h2 = uni(ws + W_H2 + (size_t)b * (32 * 4 * 112 * 16));
  unsigned h2st = ((j32 * 4 + 2 * (g & 1) + (g >> 1)) * 112 + wc * 16 + c) * 16;
  const unsigned pos2 = 14 * (j32 & 7) + (c < 14 ? c : 13);
  unsigned h2ld = (((4 * w + g) * 4 + (j32 >> 3)) * 112 + pos2) * 16;
  PairTag tg2s = pair_tag(T, c), tg2l = pair_tag(T, pos2);
  unsigned obw = L_OB + c * 144 + (16 * ct + 4 * g) * 2, obr = L_OB + (c & 7) * 144 + g * 16, hlf = L_HALF + ct * 1024 + lane * 16;
  asm volatile("" : "+v"(wf[0][0]), "+v"(wf[0][1]), "+v"(wf[0][2]), "+v"(wf[0][3]), "+v"(wf[1][0]), "+v"(wf[1][1]), "+v"(wf[1][2]), "+v"(wf[1][3]),
                    "+v"(wf[2][0]), "+v"(wf[2][1]), "+v"(wf[2][2]), "+v"(wf[2][3]), "+v"(wf[3][0]), "+v"(wf[3][1]), "+v"(wf[3][2]), "+v"(wf[3][3]),
                    "+v"(h2st), "+v"(h2ld), "+v"(obw), "+v"(obr), "+v"(hlf), "+v"(tg2s.a), "+v"(tg2s.b), "+v"(tg2l.a), "+v"(tg2l.b));
  asm volatile("s_waitcnt vmcnt(4)" : "+v"(stu) :: "memory");
  for (unsigned spin = 0; any_bad(tags_bad(stu, T)) && spin < SPIN_CAP; ++spin) reload1(stu, lane * 16, stb);
  float fS, fA, fB;
  {
    const float ms = __uint_as_float(stu.x), ls = __uint_as_float(stu.z);
    const float M = xg_max(ms);
    const float e = __builtin_amdgcn_exp2f(ms - M);
    const float fw = e / xg_sum(e * ls);
    fS = __shfl(fw, 16 * s + c); fA = __shfl(fw, 16 * srcA + c); fB = kh ? 0.f : __shfl(fw, 16 * srcB + c);
  }
  VMWAIT(2);
  const unsigned two = __builtin_amdgcn_readfirstlane(kh == 0);
  const PairTag tgB = two ? tg1 : PairTag{0u, 0u};
  hu[1] = v4u{0u, 0u, 0u, 0u};
  reload12(hu[0], hu[1], hoA, hoB, two, h1);
  BARRIER();
  f32x4 own = pv_pass(3);
  for (unsigned spin = 0; any_bad(pair_bad(hu[0], tg1) | pair_bad(hu[1], tgB)) && spin < SPIN_CAP; ++spin) reload12(hu[0], hu[1], hoA, hoB, two, h1);
  {
    const unsigned odd = 0u - (unsigned)(ct & 1);
    own = own * fS + bfp_dec((hu[0].x & ~odd) | (hu[0].z & odd), (hu[0].y & ~odd) | (hu[0].w & odd)) * fA
                   + bfp_dec((hu[1].x & ~odd) | (hu[1].z & odd), (hu[1].y & ~odd) | (hu[1].w & odd)) * fB;
    if (kh == 1) *(LAS f32x4*)(L + hlf) = own;
    BARRIER();
    if (kh == 0) {
      own += *(LAS f32x4*)(L + hlf);
      *(LAS v2u*)(L + obw) = v2u{pack_bf16(own[0], own[1]), pack_bf16(own[2], own[3])};
    }
  }
  BARRIER();

  f32x4 pa[4];
#pragma unroll
  for (int jj = 0; jj < 4; ++jj) pa[jj] = f32x4{0.f, 0.f, 0.f, 0.f};
#pragma unroll
  for (int ks = 0; ks < 4; ++ks) {
    const v4u af = *(LAS v4u*)(L + obr + 8 * (ks >> 1) * 144 + 64 * (ks & 1));
#pragma unroll
    for (int jj = 0; jj < 4; ++jj) pa[jj] = mfma16(af, wf[ks][jj], pa[jj]);
  }
  if (w < 7) {
    const f32x4 v0 = (g >> 1) ? f32x4{pa[0][2], pa[1][2], pa[2][2], pa[3][2]} : f32x4{pa[0][0], pa[1][0], pa[2][0], pa[3][0]};
    const f32x4 v1 = (g >> 1) ? f32x4{pa[0][3], pa[1][3], pa[2][3], pa[3][3]} : f32x4{pa[0][1], pa[1][1], pa[2][1], pa[3][1]};
    st128_dev(h2st, bfp_pair(v0, v1, tg2s), h2);
  }
  {
    v4u fu;
    ld128_dev(fu, h2ld, h2);
    asm volatile("s_waitcnt vmcnt(0)" : "+v"(fu) :: "memory");
    for (unsigned spin = 0; any_bad(pair_bad(fu, tg2l)) && spin < SPIN_CAP; ++spin) reload1(fu, h2ld, h2);
    const f32x4 r0 = bfp_dec(fu.x, fu.y), r1 = bfp_dec(fu.z, fu.w);
    f32x4 t;
#pragma unroll
    for (int e = 0; e < 4; ++e) {
      const auto u = __builtin_amdgcn_permlane32_swap(__float_as_uint(r0[e]), __float_as_uint(r1[e]), false, false);
      const float h = __uint_as_float(u[0]) + __uint_as_float(u[1]);
      const auto v = __builtin_amdgcn_permlane16_swap(__float_as_uint(h), __float_as_uint(h), false, false);
      t[e] = __uint_as_float(v[0]) + __uint_as_float(v[1]);
    }
    *(LAS f32x4*)(L + L_RED + w * 1024 + lane * 16) = t;
  }
  BARRIER();
  if (w == 0) {
    f32x4 t = *(LAS f32x4*)(L + L_RED + lane * 16);
#pragma unroll
    for (int ww = 1; ww < 8; ++ww) t += *(LAS f32x4*)(L + L_RED + ww * 1024 + lane * 16);
    if ((lane & 31) < 14)
      *(v2u*)(out + (((b * SQ + 2 * (j32 >> 3) + (lane >> 5)) * DM + 56 * (j32 & 7) + 4 * (lane & 31)) >> 1)) =
          v2u{pack_bf16(t[0], t[1]), pack_bf16(t[2], t[3])};
  }
  if (w == 1 && (b & 1) == 0 && j32 == 0 && lane == 0)
    __hip_atomic_store((unsigned*)(ws + W_EP + rb * 256), T, __ATOMIC_RELAXED, __HIP_MEMORY_SCOPE_AGENT);
  asm volatile(".Lattn_end:" ::: "memory");
}

}

extern "C" void kernel_launch(void* const* d_in, const int* in_sizes, int n_in, void* d_out, int out_size, void* d_ws,
                              size_t ws_size, hipStream_t stream) {
  (void)in_sizes; (void)n_in; (void)out_size;
  if (ws_size < W_END) return;
  (void)hipFuncSetAttribute((const void*)attn_fused_kernel, hipFuncAttributeMaxDynamicSharedMemorySize, LDS_BYTES);
  attn_fused_kernel<<<dim3(256), dim3(512), LDS_BYTES, stream>>>((const char*)d_in[0], (const char*)d_in[1], (const char*)d_in[2],
                                                                 (const char*)d_in[3], (const char*)d_in[4], (unsigned*)d_out, (char*)d_ws);
}
```
